```python
import torch

def _softmax(x, axis=-1):
    return torch.softmax(x, dim=axis)
def _gelu(x):
    return torch.nn.functional.gelu(x, approximate="tanh")
def _silu(x):
    return torch.nn.functional.silu(x)
def _sigmoid(x):
    return torch.sigmoid(x)
def _layernorm(x, axis=-1, eps=1e-5):
    m = x.mean(dim=axis, keepdim=True)
    v = x.var(dim=axis, keepdim=True, unbiased=False)
    return (x - m) / torch.sqrt(v + eps)
def _scatter_add(tgt, idx, src, axis=0):
    return tgt.clone().index_add_(axis, idx, src)
def _segment_sum(src, seg, n):
    out = torch.zeros((n,) + src.shape[1:], dtype=src.dtype)
    return out.index_add_(0, seg, src)
def _pad(x, pw):
    flat = tuple(v for pair in reversed(pw) for v in pair)
    return torch.nn.functional.pad(x, flat)

kernel_name = 'interleave_layers__linear_attn_block__grouped_kv_mqa__prompt'

def setup_inputs():

    _w = (lambda a: (lambda t: t.to(torch.bfloat16) if t.is_floating_point() else t)(torch.as_tensor(a)))
    def _A_setup():
        B, S, D = 1, 2048, 512
        return {
            "x": _w((lambda *s: torch.randn(*s, dtype=torch.bfloat16) * 0.05)(B, S, D)),
            "wqkv": _w((lambda *s: torch.randn(*s, dtype=torch.bfloat16) * 0.05)(D, 3 * D)),
            "wo": _w((lambda *s: torch.randn(*s, dtype=torch.bfloat16) * 0.05)(D, D)),
        }
    _A_in = _A_setup()

    def _B_setup():
        B, S, D, H = 1, 2048, 512, 32
        Dh = D // H
        return {
            "x": _w((lambda *s: torch.randn(*s, dtype=torch.bfloat16) * 0.05)(B, S, D)),
            "wq": _w((lambda *s: torch.randn(*s, dtype=torch.bfloat16) * 0.1)(D, H * Dh)),
            "wk": _w((lambda *s: torch.randn(*s, dtype=torch.bfloat16) * 0.1)(D, Dh)),
            "wv": _w((lambda *s: torch.randn(*s, dtype=torch.bfloat16) * 0.05)(D, Dh)),
            "wo": _w((lambda *s: torch.randn(*s, dtype=torch.bfloat16) * 0.05)(H * Dh, D)),
        }
    _B_in = _B_setup()

    return {
        "A_x": _A_in["x"],
        "A_wqkv": _A_in["wqkv"],
        "A_wo": _A_in["wo"],
        "B_x": _B_in["x"],
        "B_wq": _B_in["wq"],
        "B_wk": _B_in["wk"],
        "B_wv": _B_in["wv"],
        "B_wo": _B_in["wo"],
    }

def reference(A_x, A_wqkv, A_wo, B_x, B_wq, B_wk, B_wv, B_wo):
    def _A_ref(x, wqkv, wo):
        B, S, D = x.shape
        H = 32; Dh = D // H
        qkv = (lambda x, *d: x.permute(*d))((x @ wqkv).reshape(B, S, 3, H, Dh), 2, 0, 3, 1, 4)
        q, k, v = qkv[0], qkv[1], qkv[2]
        phi = lambda t: torch.where(t > 0, t + 1, torch.exp(t)) + 1e-6
        pq, pk = phi(q), phi(k)
        kv = torch.einsum("bhsd,bhse->bhde", pk, v)
        z = torch.einsum("bhsd->bhd", pk)
        num = torch.einsum("bhqd,bhde->bhqe", pq, kv)
        den = torch.einsum("bhqd,bhd->bhq", pq, z)[..., None] + 1e-6
        o = num / den
        return (lambda x, *d: x.permute(*d))(o, 0, 2, 1, 3).reshape(B, S, D) @ wo

    def _B_ref(x, wq, wk, wv, wo):
        B, S, D = x.shape
        H = 32
        Dh = D // H
        q = (lambda x, *d: x.permute(*d))((x @ wq).reshape(B, S, H, Dh), 0, 2, 1, 3)
        k = x @ wk
        v = x @ wv
        scores = torch.einsum("bhqd,bkd->bhqk", q, k) * (Dh ** -0.5)
        p = _softmax(scores, axis=-1)
        o = torch.einsum("bhqk,bkd->bhqd", p, v)
        return (lambda x, *d: x.permute(*d))(o, 0, 2, 1, 3).reshape(B, S, H * Dh) @ wo

    x = A_x
    x = x + _A_ref(_layernorm(x, axis=-1), A_wqkv, A_wo)
    x = x + _B_ref(_layernorm(x, axis=-1), B_wq, B_wk, B_wv, B_wo)
    x = x + _A_ref(_layernorm(x, axis=-1), A_wqkv, A_wo)
    x = x + _B_ref(_layernorm(x, axis=-1), B_wq, B_wk, B_wv, B_wo)
    return x
```

```cpp
#include <hip/hip_runtime.h>
#include <hip/hip_bf16.h>
#include <stdint.h>

typedef __attribute__((ext_vector_type(8))) short bf16x8;
typedef __attribute__((ext_vector_type(16))) float f32x16;
typedef __attribute__((ext_vector_type(4))) float f32x4;
typedef __attribute__((ext_vector_type(2))) float f32x2;
typedef __attribute__((ext_vector_type(4))) unsigned u32x4;
typedef __attribute__((ext_vector_type(2))) unsigned u32x2;
typedef __attribute__((ext_vector_type(2))) __bf16 bf16x2_t;
#define LAS __attribute__((address_space(3)))
#define DI __device__ __forceinline__
#define MFMA(a, b, c) __builtin_amdgcn_mfma_f32_32x32x16_bf16(__builtin_bit_cast(bf16x8, (a)), __builtin_bit_cast(bf16x8, (b)), (c), 0, 0, 0)

constexpr int SEQ = 2048, DM = 512, NH = 32;
constexpr int NTT = SEQ / 32;
constexpr int NKS = DM / 16;
constexpr float LOG2E = 1.4426950408889634f;

constexpr size_t KB = 1024;
constexpr size_t OFF_WQKV = 0;
constexpr size_t OFF_WOA = OFF_WQKV + 48 * 32 * KB;
constexpr size_t OFF_WQ = OFF_WOA + 16 * 32 * KB;
constexpr size_t OFF_WKV = OFF_WQ + 16 * 32 * KB;
constexpr size_t OFF_WOB = OFF_WKV + 32 * KB;
constexpr size_t OFF_CS = OFF_WOB + 16 * 32 * KB;
constexpr size_t OFF_XF = OFF_CS + 16 * KB;
constexpr size_t OFF_ST = OFF_XF + 4 * 2048 * KB;
constexpr size_t OFF_PQ = OFF_ST + 4 * 256 * KB;
constexpr size_t OFF_QF = OFF_PQ + 2048 * KB;
constexpr size_t OFF_OF = OFF_QF + 2048 * KB;
constexpr size_t OFF_KV = OFF_OF + 2048 * KB;
constexpr size_t OFF_ACC = OFF_KV + 132 * KB;
constexpr int ACC_N = NH * 17 * 16;
constexpr int REC = 2080;

DI unsigned cvtpk(float lo, float hi) {
  f32x2 v = {lo, hi};
  return __builtin_bit_cast(unsigned, __builtin_convertvector(v, bf16x2_t));
}
DI float bflo(unsigned u) { return __uint_as_float(u << 16); }
DI float bfhi(unsigned u) { return __uint_as_float(u & 0xffff0000u); }
DI float bfround(float x) { return bflo(cvtpk(x, x)); }
DI u32x4 pack8(const f32x16& x, int s) {
  u32x4 p;
  p[0] = cvtpk(x[8 * s + 0], x[8 * s + 1]); p[1] = cvtpk(x[8 * s + 2], x[8 * s + 3]);
  p[2] = cvtpk(x[8 * s + 4], x[8 * s + 5]); p[3] = cvtpk(x[8 * s + 6], x[8 * s + 7]);
  return p;
}
DI void glds16(const void* g, unsigned lds_base) {
  unsigned sv;
  asm volatile("s_mov_b32 %0, m0\n\ts_mov_b32 m0, %2\n\ts_nop 0\n\tglobal_load_lds_dwordx4 %1, off\n\ts_mov_b32 m0, %0"
               : "=&s"(sv) : "v"(g), "s"(lds_base) : "memory");
}
#define WAIT_VM_BAR(N) asm volatile("s_waitcnt vmcnt(" #N ") lgkmcnt(0)\n\ts_barrier" ::: "memory")
DI float xhalf_sum(float v) {
  auto r = __builtin_amdgcn_permlane32_swap(__float_as_uint(v), __float_as_uint(v), false, false);
  return __uint_as_float(r[0]) + __uint_as_float(r[1]);
}
DI float xhalf_max(float v) {
  auto r = __builtin_amdgcn_permlane32_swap(__float_as_uint(v), __float_as_uint(v), false, false);
  return fmaxf(__uint_as_float(r[0]), __uint_as_float(r[1]));
}
DI f32x16 zero16() { f32x16 z; _Pragma("unroll") for (int i = 0; i < 16; ++i) z[i] = 0.f; return z; }

struct RowStat { float mu, rs; };
DI void load_stats(const f32x2* __restrict__ st, int tt, int r, int h, f32x2 (&p)[8]) {
#pragma unroll
  for (int i = 0; i < 8; ++i) p[i] = st[(tt * 16 + h * 8 + i) * 32 + r];
}
DI RowStat finish_stats(const f32x2 (&p)[8]) {
  float s = 0.f, q = 0.f;
#pragma unroll
  for (int i = 0; i < 8; ++i) { s += p[i][0]; q += p[i][1]; }
  s = xhalf_sum(s); q = xhalf_sum(q);
  const float mean = s * (1.f / DM);
  const float var = fmaxf(q * (1.f / DM) - mean * mean, 0.f);
  const float sd = bfround(__builtin_amdgcn_sqrtf(bfround(bfround(var) + 1e-5f)));
  RowStat o; o.mu = bfround(mean); o.rs = 1.f / sd; return o;
}
DI float phi(float t) { return fmaxf(t, 0.f) + __builtin_amdgcn_exp2f(fminf(t * LOG2E, 0.f)) + 1e-6f; }

__global__ __launch_bounds__(1024) void k_prep(const uint16_t* __restrict__ x0, const uint16_t* __restrict__ wqkv,
                                               const uint16_t* __restrict__ woa, const uint16_t* __restrict__ wq,
                                               const uint16_t* __restrict__ wk, const uint16_t* __restrict__ wv,
                                               const uint16_t* __restrict__ wob, char* __restrict__ ws) {
  __shared__ float red[16][32];
  const int b = blockIdx.x, tid = threadIdx.x, w = tid >> 6, lane = tid & 63, r = lane & 31, h = lane >> 5;
  if (b < 97) {
    const uint16_t* src; int ld, col;
    if (b < 48) { src = wqkv; ld = 1536; col = b * 32 + r; }
    else if (b < 64) { src = woa; ld = 512; col = (b - 48) * 32 + r; }
    else if (b < 80) { src = wq; ld = 512; col = (b - 64) * 32 + r; }
    else if (b == 80) { src = r < 16 ? wk : wv; ld = 16; col = r & 15; }
    else { src = wob; ld = 512; col = (b - 81) * 32 + r; }
    uint16_t e[2][8];
#pragma unroll
    for (int i = 0; i < 2; ++i)
#pragma unroll
      for (int j = 0; j < 8; ++j) e[i][j] = src[(size_t)((2 * w + i) * 16 + 8 * (j >> 2) + 4 * h + (j & 3)) * ld + col];
    float s = 0.f;
    u32x4* dst = (u32x4*)(ws + OFF_WQKV) + (size_t)b * 32 * 64;
#pragma unroll
    for (int i = 0; i < 2; ++i) {
      u32x4 v;
#pragma unroll
      for (int j = 0; j < 4; ++j) {
        v[j] = (unsigned)e[i][2 * j] | ((unsigned)e[i][2 * j + 1] << 16);
        s += bflo(v[j]) + bfhi(v[j]);
      }
      dst[(2 * w + i) * 64 + lane] = v;
    }
    s = xhalf_sum(s);
    if (h == 0) red[w][r] = s;
    __syncthreads();
    if (tid < 32) {
      float t = 0.f;
#pragma unroll
      for (int i = 0; i < 16; ++i) t += red[i][tid];
      ((float*)(ws + OFF_CS))[b * 32 + tid] = t;
    }
    if (b < 17) ((float*)(ws + OFF_ACC))[b * 1024 + tid] = 0.f;
  } else {
    const int tt = b - 97;
    const uint16_t* row = x0 + (size_t)(tt * 32 + r) * DM;
    u32x2 e[2][2];
#pragma unroll
    for (int i = 0; i < 2; ++i)
#pragma unroll
      for (int g = 0; g < 2; ++g) e[i][g] = *(const u32x2*)(row + (2 * w + i) * 16 + 8 * g + 4 * h);
    float s = 0.f, q = 0.f;
    u32x4* dst = (u32x4*)(ws + OFF_XF) + (size_t)tt * 32 * 64;
#pragma unroll
    for (int i = 0; i < 2; ++i) {
      u32x4 v = {e[i][0][0], e[i][0][1], e[i][1][0], e[i][1][1]};
#pragma unroll
      for (int j = 0; j < 4; ++j) {
        const float a = bflo(v[j]), c = bfhi(v[j]);
        s += a + c; q += a * a + c * c;
      }
      dst[(2 * w + i) * 64 + lane] = v;
    }
    s = xhalf_sum(s); q = xhalf_sum(q);
    if (h == 0) ((f32x2*)(ws + OFF_ST))[(tt * 16 + w) * 32 + r] = f32x2{s, q};
  }
}

constexpr int K1_LDS = 96 * 1024 + 4 * 544 * 4 + 4 * 256;
__global__ __launch_bounds__(256, 1) void k_qkv(const u32x4* __restrict__ XF, const f32x2* __restrict__ ST,
                                                const char* __restrict__ WF, const float* __restrict__ CS,
                                                u32x4* __restrict__ PQ, float* __restrict__ acc_out) {
  extern __shared__ __attribute__((aligned(16))) char smem[];
  const int tid = threadIdx.x, lane = tid & 63, r = lane & 31, h = lane >> 5;
  const int w = __builtin_amdgcn_readfirstlane(tid >> 6);
  const int xcd = blockIdx.x & 7, idx = blockIdx.x >> 3;
  const int panel = (xcd & 3) * 4 + (idx & 3), hp = (xcd >> 2) * 8 + (idx >> 2);
  const int tt = panel * 4 + w;
  const unsigned lds0 = (unsigned)(uintptr_t)smem;
#pragma unroll
  for (int i = 0; i < 24; ++i) {
    const int t = i >> 3, ks = (i & 7) * 4 + w;
    glds16(WF + ((size_t)(t * 16 + hp) * 32 + ks) * 1024 + lane * 16, lds0 + (t * 32 + ks) * 1024);
  }
  u32x4 xf[32];
#pragma unroll
  for (int ks = 0; ks < 32; ++ks) xf[ks] = XF[(tt * 32 + ks) * 64 + lane];
  f32x2 sp[8];
  load_stats(ST, tt, r, h, sp);
  f32x4 csq[4];
#pragma unroll
  for (int g = 0; g < 4; ++g) csq[g] = *(const f32x4*)(CS + hp * 32 + 8 * g + 4 * h);
  const float csk = CS[(16 + hp) * 32 + r], csv = CS[(32 + hp) * 32 + r];
  WAIT_VM_BAR(46);
  f32x16 aq = zero16(), ak = zero16(), av = zero16();
  const LAS u32x4* wl = (const LAS u32x4*)smem + lane;
#pragma unroll
  for (int ks = 0; ks < 32; ++ks) {
    const u32x4 wq = wl[ks * 64], wk = wl[(32 + ks) * 64], wv = wl[(64 + ks) * 64];
    aq = MFMA(wq, xf[ks], aq);
    ak = MFMA(xf[ks], wk, ak);
    av = MFMA(xf[ks], wv, av);
  }
  const RowStat rs = finish_stats(sp);
  float* sst = (float*)(smem + 96 * 1024 + 4 * 544 * 4) + w * 64;
  if (h == 0) { sst[r] = rs.mu; sst[32 + r] = rs.rs; }
#pragma unroll
  for (int i = 0; i < 16; ++i) aq[i] = phi(rs.rs * (aq[i] - rs.mu * csq[i >> 2][i & 3]));
  PQ[(tt * 32 + 2 * hp) * 64 + lane] = pack8(aq, 0);
  PQ[(tt * 32 + 2 * hp + 1) * 64 + lane] = pack8(aq, 1);
#pragma unroll
  for (int g = 0; g < 4; ++g) {
    const f32x4 mu = *(const f32x4*)(sst + 8 * g + 4 * h), rr = *(const f32x4*)(sst + 32 + 8 * g + 4 * h);
#pragma unroll
    for (int j = 0; j < 4; ++j) {
      ak[4 * g + j] = phi(rr[j] * (ak[4 * g + j] - mu[j] * csk));
      av[4 * g + j] = rr[j] * (av[4 * g + j] - mu[j] * csv);
    }
  }
  const u32x4 ones = {0x3f803f80u, 0x3f803f80u, 0x3f803f80u, 0x3f803f80u};
  f32x16 z = zero16(), zs = zero16();
#pragma unroll
  for (int s = 0; s < 2; ++s) {
    const u32x4 pk = pack8(ak, s), pv = pack8(av, s);
    z = MFMA(pk, pv, z);
    zs = MFMA(pk, ones, zs);
  }
  float* red = (float*)(smem + 96 * 1024) + w * 544;
  const int a = r >> 4, e = r & 15;
  {
    f32x4 lo, hi;
#pragma unroll
    for (int j = 0; j < 4; ++j) { lo[j] = a ? z[8 + j] : z[j]; hi[j] = a ? z[12 + j] : z[4 + j]; }
    *(f32x4*)(red + a * 272 + e * 16 + 4 * h) = lo;
    *(f32x4*)(red + a * 272 + e * 16 + 8 + 4 * h) = hi;
    if (e == 0) {
#pragma unroll
      for (int j = 0; j < 4; ++j) { lo[j] = a ? zs[8 + j] : zs[j]; hi[j] = a ? zs[12 + j] : zs[4 + j]; }
      *(f32x4*)(red + a * 272 + 256 + 4 * h) = lo;
      *(f32x4*)(red + a * 272 + 256 + 8 + 4 * h) = hi;
    }
  }
  __syncthreads();
  const float* redall = (const float*)(smem + 96 * 1024);
#pragma unroll
  for (int i = 0; i < 3; ++i) {
    const int j = i * 256 + tid;
    if (j < 544) atomicAdd(acc_out + hp * 544 + j, (redall[j] + redall[544 + j]) + (redall[2 * 544 + j] + redall[3 * 544 + j]));
  }
}

template <bool FINAL>
DI void resid_epilogue(const f32x16& acc, const u32x4 (&xr)[2], u32x4* __restrict__ XO, f32x2* __restrict__ STO,
                       uint16_t* __restrict__ out, int tt, int nt, int lane) {
  const int r = lane & 31, h = lane >> 5;
  float s = 0.f, q = 0.f;
#pragma unroll
  for (int g = 0; g < 2; ++g) {
    const u32x4 y = pack8(acc, g);
    u32x4 o;
#pragma unroll
    for (int j = 0; j < 4; ++j) {
      o[j] = cvtpk(bflo(xr[g][j]) + bflo(y[j]), bfhi(xr[g][j]) + bfhi(y[j]));
      if (!FINAL) { const float a = bflo(o[j]), c = bfhi(o[j]); s += a + c; q += a * a + c * c; }
    }
    if (FINAL) {
      uint16_t* p = out + (size_t)(tt * 32 + r) * DM + nt * 32 + 16 * g + 4 * h;
      *(u32x2*)p = u32x2{o[0], o[1]};
      *(u32x2*)(p + 8) = u32x2{o[2], o[3]};
    } else {
      XO[(tt * 32 + 2 * nt + g) * 64 + lane] = o;
    }
  }
  if (!FINAL) {
    s = xhalf_sum(s); q = xhalf_sum(q);
    if (h == 0) STO[(tt * 16 + nt) * 32 + r] = f32x2{s, q};
  }
}

__global__ __launch_bounds__(256, 1) void k_lin_out(const u32x4* __restrict__ PQ, const float* __restrict__ KVA,
                                                    const u32x4* __restrict__ WF, const u32x4* __restrict__ XI,
                                                    u32x4* __restrict__ XO, f32x2* __restrict__ STO) {
  __shared__ __attribute__((aligned(16))) u32x4 of[32 * 64];
  const int tid = threadIdx.x, lane = tid & 63, r = lane & 31, h = lane >> 5;
  const int w = __builtin_amdgcn_readfirstlane(tid >> 6);
  const int xcd = blockIdx.x & 7, idx = blockIdx.x >> 3;
  const int tt = xcd * 8 + (idx >> 2), nt = (idx & 3) * 4 + w;
  u32x4 pq[8]; f32x4 kv[8][2];
  const int row = r < 16 ? r : 16;
#pragma unroll
  for (int i = 0; i < 8; ++i) {
    const int hd = w * 8 + i;
    pq[i] = PQ[(tt * 32 + hd) * 64 + lane];
    kv[i][0] = *(const f32x4*)(KVA + hd * 272 + row * 16 + 4 * h);
    kv[i][1] = *(const f32x4*)(KVA + hd * 272 + row * 16 + 8 + 4 * h);
  }
  u32x4 wf[32];
#pragma unroll
  for (int ks = 0; ks < 32; ++ks) wf[ks] = WF[(nt * 32 + ks) * 64 + lane];
  u32x4 xr[2];
  xr[0] = XI[(tt * 32 + 2 * nt) * 64 + lane]; xr[1] = XI[(tt * 32 + 2 * nt + 1) * 64 + lane];
#pragma unroll
  for (int i = 0; i < 8; ++i) {
    const u32x4 a = {cvtpk(kv[i][0][0], kv[i][0][1]), cvtpk(kv[i][0][2], kv[i][0][3]), cvtpk(kv[i][1][0], kv[i][1][1]), cvtpk(kv[i][1][2], kv[i][1][3])};
    f32x16 d = MFMA(a, pq[i], zero16());
    const float inv = __builtin_amdgcn_rcpf(d[8] + 1e-6f);
#pragma unroll
    for (int j = 0; j < 8; ++j) d[j] *= inv;
    of[(w * 8 + i) * 64 + lane] = pack8(d, 0);
  }
  __syncthreads();
  f32x16 acc = zero16();
#pragma unroll
  for (int ks = 0; ks < 32; ++ks) acc = MFMA(wf[ks], of[ks * 64 + lane], acc);
  resid_epilogue<false>(acc, xr, XO, STO, nullptr, tt, nt, lane);
}

template <bool FINAL>
__global__ __launch_bounds__(256, 1) void k_attn_out(const char* __restrict__ OF, const u32x4* __restrict__ WF,
                                                     const u32x4* __restrict__ XI, u32x4* __restrict__ XO,
                                                     f32x2* __restrict__ STO, uint16_t* __restrict__ out) {
  __shared__ __attribute__((aligned(16))) u32x4 of[32 * 64];
  const int tid = threadIdx.x, lane = tid & 63;
  const int w = __builtin_amdgcn_readfirstlane(tid >> 6);
  const int xcd = blockIdx.x & 7, idx = blockIdx.x >> 3;
  const int tt = xcd * 8 + (idx >> 2), nt = (idx & 3) * 4 + w;
  const unsigned lds0 = (unsigned)(uintptr_t)of;
#pragma unroll
  for (int i = 0; i < 8; ++i) glds16(OF + ((size_t)tt * 32 + i * 4 + w) * 1024 + lane * 16, lds0 + (i * 4 + w) * 1024);
  u32x4 wf[32];
#pragma unroll
  for (int ks = 0; ks < 32; ++ks) wf[ks] = WF[(nt * 32 + ks) * 64 + lane];
  u32x4 xr[2];
  xr[0] = XI[(tt * 32 + 2 * nt) * 64 + lane]; xr[1] = XI[(tt * 32 + 2 * nt + 1) * 64 + lane];
  WAIT_VM_BAR(34);
  f32x16 acc = zero16();
  const LAS u32x4* ol = (const LAS u32x4*)of + lane;
#pragma unroll
  for (int ks = 0; ks < 32; ++ks) acc = MFMA(wf[ks], ol[ks * 64], acc);
  resid_epilogue<FINAL>(acc, xr, XO, STO, out, tt, nt, lane);
}

__global__ __launch_bounds__(256, 2) void k_qkv_b(const char* __restrict__ XF, const f32x2* __restrict__ ST,
                                                  const u32x4* __restrict__ WQ, const u32x4* __restrict__ WKV,
                                                  const float* __restrict__ CSQ, const float* __restrict__ CSKV,
                                                  u32x4* __restrict__ QF, char* __restrict__ KVI) {
  __shared__ __attribute__((aligned(16))) u32x4 xl[32 * 64];
  const int tid = threadIdx.x, lane = tid & 63, r = lane & 31, h = lane >> 5;
  const int w = __builtin_amdgcn_readfirstlane(tid >> 6);
  if (blockIdx.x < 64) {
    const int tt = (blockIdx.x & 7) * 8 + (blockIdx.x >> 3);
    u32x4 xf[8], wf[8];
#pragma unroll
    for (int i = 0; i < 8; ++i) {
      xf[i] = ((const u32x4*)XF)[(tt * 32 + w * 8 + i) * 64 + lane];
      wf[i] = WKV[(w * 8 + i) * 64 + lane];
    }
    f32x2 sp[8];
    load_stats(ST, tt, r, h, sp);
    f32x4 cs[4];
#pragma unroll
    for (int g = 0; g < 4; ++g) cs[g] = *(const f32x4*)(CSKV + 8 * g + 4 * h);
    f32x16 acc = zero16();
#pragma unroll
    for (int i = 0; i < 8; ++i) acc = MFMA(wf[i], xf[i], acc);
    f32x4* red = (f32x4*)xl;
    if (w > 0) {
#pragma unroll
      for (int g = 0; g < 4; ++g) red[((w - 1) * 4 + g) * 64 + lane] = f32x4{acc[4 * g], acc[4 * g + 1], acc[4 * g + 2], acc[4 * g + 3]};
    }
    __syncthreads();
    if (w == 0) {
      const RowStat rs = finish_stats(sp);
#pragma unroll
      for (int g = 0; g < 4; ++g) {
        const f32x4 a = red[g * 64 + lane], b = red[(4 + g) * 64 + lane], c = red[(8 + g) * 64 + lane];
#pragma unroll
        for (int j = 0; j < 4; ++j) acc[4 * g + j] = rs.rs * (((acc[4 * g + j] + a[j]) + (b[j] + c[j])) - rs.mu * cs[g][j]);
      }
      char* rec = KVI + tt * REC;
      *(u32x4*)(rec + lane * 16) = pack8(acc, 0);
      const u32x4 v = pack8(acc, 1);
      const int t16 = r & 15, hh = (t16 >> 2) & 1, jj = ((t16 >> 3) << 2) | (t16 & 3);
      uint16_t* vb = (uint16_t*)(rec + 1024 + (r >> 4) * 528 + hh * 256) + jj;
#pragma unroll
      for (int j = 0; j < 8; ++j) {
        const int d = 8 * (j >> 2) + 4 * h + (j & 3);
        vb[d * 8] = (uint16_t)((j & 1) ? (v[j >> 1] >> 16) : (v[j >> 1] & 0xffffu));
      }
      if (lane < 2) *(u32x4*)(rec + 1024 + 512 + lane * 528) = u32x4{0x3f803f80u, 0x3f803f80u, 0x3f803f80u, 0x3f803f80u};
    }
    return;
  }
  const int b = blockIdx.x - 64;
  const int xcd = b & 7, idx = b >> 3;
  const int tt = xcd * 8 + (idx >> 2), nt = (idx & 3) * 4 + w;
  const unsigned lds0 = (unsigned)(uintptr_t)xl;
#pragma unroll
  for (int i = 0; i < 8; ++i) glds16(XF + ((size_t)tt * 32 + i * 4 + w) * 1024 + lane * 16, lds0 + (i * 4 + w) * 1024);
  u32x4 wf[32];
#pragma unroll
  for (int ks = 0; ks < 32; ++ks) wf[ks] = WQ[(nt * 32 + ks) * 64 + lane];
  f32x2 sp[8];
  load_stats(ST, tt, r, h, sp);
  f32x4 cs[4];
#pragma unroll
  for (int g = 0; g < 4; ++g) cs[g] = *(const f32x4*)(CSQ + nt * 32 + 8 * g + 4 * h);
  WAIT_VM_BAR(44);
  f32x16 acc = zero16();
  const LAS u32x4* ol = (const LAS u32x4*)xl + lane;
#pragma unroll
  for (int ks = 0; ks < 32; ++ks) acc = MFMA(wf[ks], ol[ks * 64], acc);
  const RowStat rs = finish_stats(sp);
  const float sc = rs.rs * (0.25f * LOG2E);
#pragma unroll
  for (int i = 0; i < 16; ++i) acc[i] = sc * (acc[i] - rs.mu * cs[i >> 2][i & 3]);
  QF[(tt * 32 + 2 * nt) * 64 + lane] = pack8(acc, 0);
  QF[(tt * 32 + 2 * nt + 1) * 64 + lane] = pack8(acc, 1);
}

constexpr int ATT_LDS = 130 * 1024 + 8 * 10 * 256;
DI float max16(const f32x16& s) {
  float a = fmaxf(fmaxf(s[0], s[1]), s[2]), b = fmaxf(fmaxf(s[3], s[4]), s[5]);
  a = fmaxf(fmaxf(a, s[6]), s[7]); b = fmaxf(fmaxf(b, s[8]), s[9]);
  a = fmaxf(fmaxf(a, s[10]), s[11]); b = fmaxf(fmaxf(b, s[12]), s[13]);
  a = fmaxf(fmaxf(a, s[14]), s[15]);
  return fmaxf(a, b);
}
#define EXP16(S) _Pragma("unroll") for (int i_ = 0; i_ < 16; ++i_) S[i_] = __builtin_amdgcn_exp2f(S[i_])

__global__ __launch_bounds__(1024, 4) void k_attn(const u32x4* __restrict__ QF, const char* __restrict__ KVI, u32x4* __restrict__ OF) {
  extern __shared__ __attribute__((aligned(16))) char smem[];
  const int tid = threadIdx.x, lane = tid & 63, r = lane & 31, h = lane >> 5;
  const int w = __builtin_amdgcn_readfirstlane(tid >> 6);
  const int half = w >> 3, wq = w & 7;
  const unsigned lds0 = (unsigned)(uintptr_t)smem;
  const int qt = blockIdx.x * 8 + wq;
  {
    const char* src = KVI + half * 66560 + lane * 16;
    const unsigned dst = lds0 + half * 66560;
#pragma unroll
    for (int i = 0; i < 8; ++i) glds16(src + (8 * i + wq) * 1024, dst + (8 * i + wq) * 1024);
    glds16(src + 64 * 1024, dst + 64 * 1024);
  }
  const u32x4 qf = QF[qt * 64 + lane];
  const LAS char* kb = (const LAS char*)smem + half * 66560 + lane * 16;
  const LAS char* vb = (const LAS char*)smem + half * 66560 + 1024 + (r < 16 ? (h * 16 + r) * 16 : 512);
#define KF(t) (*(const LAS u32x4*)(kb + (t) * REC))
#define VF(t, s) (*(const LAS u32x4*)(vb + (t) * REC + (s) * 528))
  WAIT_VM_BAR(9);
  f32x16 o = zero16(), sa, sb, cm;
  float mref;
  {
    sa = MFMA(KF(0), qf, zero16());
    mref = xhalf_max(max16(sa));
#pragma unroll
    for (int i = 0; i < 16; ++i) { cm[i] = -mref; sa[i] -= mref; }
    sb = MFMA(KF(1), qf, cm);
    EXP16(sa);
    o = MFMA(VF(0, 0), pack8(sa, 0), o); o = MFMA(VF(0, 1), pack8(sa, 1), o);
    sa = MFMA(KF(2), qf, cm);
    EXP16(sb);
    o = MFMA(VF(1, 0), pack8(sb, 0), o); o = MFMA(VF(1, 1), pack8(sb, 1), o);
  }
  WAIT_VM_BAR(0);
#pragma unroll 1
  for (int t = 2; t < 30; t += 2) {
    sb = MFMA(KF(t + 1), qf, cm);
    EXP16(sa);
    o = MFMA(VF(t, 0), pack8(sa, 0), o); o = MFMA(VF(t, 1), pack8(sa, 1), o);
    sa = MFMA(KF(t + 2), qf, cm);
    EXP16(sb);
    o = MFMA(VF(t + 1, 0), pack8(sb, 0), o); o = MFMA(VF(t + 1, 1), pack8(sb, 1), o);
  }
  sb = MFMA(KF(31), qf, cm);
  EXP16(sa);
  o = MFMA(VF(30, 0), pack8(sa, 0), o); o = MFMA(VF(30, 1), pack8(sa, 1), o);
  EXP16(sb);
  o = MFMA(VF(31, 0), pack8(sb, 0), o); o = MFMA(VF(31, 1), pack8(sb, 1), o);
  float* cb = (float*)(smem + 130 * 1024) + wq * 640 + lane;
  if (half == 1) {
#pragma unroll
    for (int i = 0; i < 9; ++i) cb[i * 64] = o[i];
    cb[9 * 64] = mref;
  }
  __syncthreads();
  if (half == 1) return;
  const float m2 = cb[9 * 64], mm = fmaxf(mref, m2);
  const float f1 = __builtin_amdgcn_exp2f(mref - mm), f2 = __builtin_amdgcn_exp2f(m2 - mm);
  bool ok = true;
#pragma unroll
  for (int i = 0; i < 9; ++i) {
    const float a = o[i], b = cb[i * 64];
    ok = ok && (fabsf(a) < 3.0e38f) && (fabsf(b) < 3.0e38f);
    o[i] = a * f1 + b * f2;
  }
  if (__builtin_expect(!__all(ok), 0)) {
    const LAS char* kb0 = (const LAS char*)smem + lane * 16;
    const LAS char* vb0 = (const LAS char*)smem + 1024 + (r < 16 ? (h * 16 + r) * 16 : 512);
    float m = -INFINITY;
    o = zero16();
#pragma unroll 1
    for (int t = 0; t < 64; ++t) {
      f32x16 s = MFMA(*(const LAS u32x4*)(kb0 + t * REC), qf, zero16());
      const float mn = fmaxf(m, xhalf_max(max16(s)));
      const float al = __builtin_amdgcn_exp2f(m - mn);
      m = mn;
#pragma unroll
      for (int i = 0; i < 16; ++i) { o[i] *= al; s[i] = __builtin_amdgcn_exp2f(s[i] - mn); }
      o = MFMA(*(const LAS u32x4*)(vb0 + t * REC), pack8(s, 0), o);
      o = MFMA(*(const LAS u32x4*)(vb0 + t * REC + 528), pack8(s, 1), o);
    }
  }
  const float inv = 1.f / o[8];
#pragma unroll
  for (int i = 0; i < 8; ++i) o[i] *= inv;
  OF[qt * 64 + lane] = pack8(o, 0);
#undef KF
#undef VF
}

extern "C" void kernel_launch(void* const* d_in, const int* in_sizes, int n_in, void* d_out, int out_size, void* d_ws,
                              size_t ws_size, hipStream_t stream) {
  const uint16_t* x0 = (const uint16_t*)d_in[0];
  char* ws = (char*)d_ws;
  (void)hipFuncSetAttribute((const void*)k_qkv, hipFuncAttributeMaxDynamicSharedMemorySize, K1_LDS);
  (void)hipFuncSetAttribute((const void*)k_attn, hipFuncAttributeMaxDynamicSharedMemorySize, ATT_LDS);
  k_prep<<<161, 1024, 0, stream>>>(x0, (const uint16_t*)d_in[1], (const uint16_t*)d_in[2], (const uint16_t*)d_in[4],
                                   (const uint16_t*)d_in[5], (const uint16_t*)d_in[6], (const uint16_t*)d_in[7], ws);
  const float* cs = (const float*)(ws + OFF_CS);
  for (int L = 0; L < 2; ++L) {
    char* xa = ws + OFF_XF + (size_t)(2 * L) * 2048 * KB;
    char* xb = xa + 2048 * KB;
    char* xc = xb + 2048 * KB;
    f32x2* sa = (f32x2*)(ws + OFF_ST + (size_t)(2 * L) * 256 * KB);
    f32x2* sb = (f32x2*)(ws + OFF_ST + (size_t)(2 * L + 1) * 256 * KB);
    f32x2* sc = (f32x2*)(ws + OFF_ST + (size_t)(2 * L + 2) * 256 * KB);
    float* acc = (float*)(ws + OFF_ACC) + L * ACC_N;
    k_qkv<<<256, 256, K1_LDS, stream>>>((const u32x4*)xa, sa, ws + OFF_WQKV, cs, (u32x4*)(ws + OFF_PQ), acc);
    k_lin_out<<<256, 256, 0, stream>>>((const u32x4*)(ws + OFF_PQ), acc, (const u32x4*)(ws + OFF_WOA), (const u32x4*)xa,
                                       (u32x4*)xb, sb);
    k_qkv_b<<<320, 256, 0, stream>>>(xb, sb, (const u32x4*)(ws + OFF_WQ), (const u32x4*)(ws + OFF_WKV), cs + 64 * 32,
                                     cs + 80 * 32, (u32x4*)(ws + OFF_QF), ws + OFF_KV);
    k_attn<<<256, 1024, ATT_LDS, stream>>>((const u32x4*)(ws + OFF_QF), ws + OFF_KV, (u32x4*)(ws + OFF_OF));
    if (L == 0)
      k_attn_out<false><<<256, 256, 0, stream>>>(ws + OFF_OF, (const u32x4*)(ws + OFF_WOB), (const u32x4*)xb, (u32x4*)xc, sc, nullptr);
    else
      k_attn_out<true><<<256, 256, 0, stream>>>(ws + OFF_OF, (const u32x4*)(ws + OFF_WOB), (const u32x4*)xb, nullptr, nullptr,
                                                (uint16_t*)d_out);
  }
}
```

```cpp
#include <hip/hip_runtime.h>
#include <hip/hip_bf16.h>
#include <stdint.h>

typedef __attribute__((ext_vector_type(8))) short bf16x8;
typedef __attribute__((ext_vector_type(4))) short s16x4;
typedef __attribute__((ext_vector_type(16))) float f32x16;
typedef __attribute__((ext_vector_type(4))) float f32x4;
typedef __attribute__((ext_vector_type(2))) float f32x2;
typedef __attribute__((ext_vector_type(16))) unsigned u32x16;
typedef __attribute__((ext_vector_type(4))) unsigned u32x4;
typedef __attribute__((ext_vector_type(2))) unsigned u32x2;
typedef __attribute__((ext_vector_type(2))) __bf16 bf16x2_t;
#define LAS __attribute__((address_space(3)))
#define GAS __attribute__((address_space(1)))
typedef GAS unsigned gu32;
#define DI __device__ __forceinline__
#define MFMA(a, b, c) __builtin_amdgcn_mfma_f32_32x32x16_bf16(__builtin_bit_cast(bf16x8, (a)), __builtin_bit_cast(bf16x8, (b)), (c), 0, 0, 0)
#define RLX_AGENT __ATOMIC_RELAXED, __HIP_MEMORY_SCOPE_AGENT

#define STAMP(slot) do {} while (0)
#define STAMP_DECL do {} while (0)
#define STAMP_FLUSH() do {} while (0)

constexpr int SEQ = 2048, DM = 512, NH = 32;
constexpr float LOG2E = 1.4426950408889634f;

constexpr int KB = 1024;
constexpr int OFF_CTR = 0;
constexpr int CTR_A = 0, CTR_C = 256, CTR_B = 512, CTR_D = 640, CTR_E = 768, CTR_TMO = 784, CTR_LINES = 800;
constexpr int OFF_ACC = OFF_CTR + CTR_LINES * 128;
constexpr int ACC_N = NH * 17 * 16;
constexpr int ZERO_BYTES = CTR_LINES * 128 + 2 * ACC_N * 4;
constexpr int OFF_W = 256 * KB;
constexpr int T_WOA = 48, T_WQ = 64, T_WKV = 80, T_WOB = 81;
constexpr int OFF_CS1 = OFF_W + 97 * 32 * KB;
constexpr int OFF_CS3 = OFF_CS1 + 16 * KB;
constexpr int OFF_XF = OFF_CS3 + 8 * KB;
constexpr int OFF_ST = OFF_XF + 4 * 2048 * KB;
constexpr int OFF_PQ = OFF_ST + 4 * 512 * KB;
constexpr int OFF_OF = OFF_PQ + 2 * 2048 * KB;
constexpr int OFF_KV = OFF_OF + 2 * 2048 * KB;
constexpr int WS_END = OFF_KV + 2 * 128 * KB;

constexpr int L_ACT = 0, L_WL = 32 * KB;
constexpr int L_CONST = 128 * KB, L_COMB = 136 * KB;

DI unsigned cvtpk(float lo, float hi) {
  f32x2 v = {lo, hi};
  return __builtin_bit_cast(unsigned, __builtin_convertvector(v, bf16x2_t));
}
DI float bflo(unsigned u) { return __uint_as_float(u << 16); }
DI float bfhi(unsigned u) { return __uint_as_float(u & 0xffff0000u); }
DI float bfround(float x) { return bflo(cvtpk(x, x)); }
DI u32x4 pack8(const f32x16& x, int s) {
  u32x4 p;
  p[0] = cvtpk(x[8 * s + 0], x[8 * s + 1]); p[1] = cvtpk(x[8 * s + 2], x[8 * s + 3]);
  p[2] = cvtpk(x[8 * s + 4], x[8 * s + 5]); p[3] = cvtpk(x[8 * s + 6], x[8 * s + 7]);
  return p;
}
DI float xhalf_sum(float v) {
  auto r = __builtin_amdgcn_permlane32_swap(__float_as_uint(v), __float_as_uint(v), false, false);
  return __uint_as_float(r[0]) + __uint_as_float(r[1]);
}
DI float xhalf_max(float v) {
  auto r = __builtin_amdgcn_permlane32_swap(__float_as_uint(v), __float_as_uint(v), false, false);
  return fmaxf(__uint_as_float(r[0]), __uint_as_float(r[1]));
}
DI f32x16 zero16() { f32x16 z; _Pragma("unroll") for (int i = 0; i < 16; ++i) z[i] = 0.f; return z; }
DI float phi(float y, float rs, float rsl) {
  return fmaxf(__builtin_fmaf(y, rs, 1e-6f), 1e-6f) + __builtin_amdgcn_fmed3f(__builtin_amdgcn_exp2f(y * rsl), 0.f, 1.f);
}

#define GLDS_HEAD "s_nop 4\n\ts_mov_b32 %0, m0\n\ts_mov_b32 m0, %3\n\ts_nop 0\n\t"
#define GLDS_OPS : "=&s"(sv) : "v"(voff), "s"(sbase), "s"(lds_base) : "memory"
template <bool SC1> DI void glds4(const char* sbase, unsigned voff, unsigned lds_base) {
  unsigned sv;
  lds_base = __builtin_amdgcn_readfirstlane(lds_base);
  if (SC1) asm volatile(GLDS_HEAD "global_load_lds_dwordx4 %1, %2 sc1\n\tglobal_load_lds_dwordx4 %1, %2 offset:1024 sc1\n\t"
               "global_load_lds_dwordx4 %1, %2 offset:2048 sc1\n\tglobal_load_lds_dwordx4 %1, %2 offset:3072 sc1\n\ts_mov_b32 m0, %0" GLDS_OPS);
  else asm volatile(GLDS_HEAD "global_load_lds_dwordx4 %1, %2\n\tglobal_load_lds_dwordx4 %1, %2 offset:1024\n\t"
               "global_load_lds_dwordx4 %1, %2 offset:2048\n\tglobal_load_lds_dwordx4 %1, %2 offset:3072\n\ts_mov_b32 m0, %0" GLDS_OPS);
}
DI void glds2(const char* sbase, unsigned voff, unsigned lds_base) {
  unsigned sv;
  lds_base = __builtin_amdgcn_readfirstlane(lds_base);
  asm volatile(GLDS_HEAD "global_load_lds_dwordx4 %1, %2\n\tglobal_load_lds_dwordx4 %1, %2 offset:1024\n\ts_mov_b32 m0, %0" GLDS_OPS);
}
DI void glds1s(const char* sbase, unsigned voff, unsigned lds_base) {
  unsigned sv;
  lds_base = __builtin_amdgcn_readfirstlane(lds_base);
  asm volatile(GLDS_HEAD "global_load_lds_dwordx4 %1, %2 sc1\n\ts_mov_b32 m0, %0" GLDS_OPS);
}
DI void glds1(const char* sbase, unsigned voff, unsigned lds_base) {
  unsigned sv;
  lds_base = __builtin_amdgcn_readfirstlane(lds_base);
  asm volatile(GLDS_HEAD "global_load_lds_dwordx4 %1, %2\n\ts_mov_b32 m0, %0" GLDS_OPS);
}
#define BAR() asm volatile("s_waitcnt lgkmcnt(0)\n\ts_barrier" ::: "memory")
#define DRAIN() asm volatile("s_waitcnt vmcnt(0)" ::: "memory")
#define PIN(x) asm volatile("" : "+v"(x))
#define SBAR() __builtin_amdgcn_sched_barrier(0)

typedef __amdgpu_buffer_rsrc_t rsrc_t;
DI u32x4 ld128(rsrc_t rs, int voff, int soff) { return __builtin_amdgcn_raw_buffer_load_b128(rs, voff, soff, 16); }
DI u32x2 ld64(rsrc_t rs, int voff, int soff) { return __builtin_amdgcn_raw_buffer_load_b64(rs, voff, soff, 16); }
DI u32x4 ldw(rsrc_t rs, int voff, int soff) { return __builtin_amdgcn_raw_buffer_load_b128(rs, voff, soff, 0); }
DI float ldwf(rsrc_t rs, int voff, int soff) { return __uint_as_float(__builtin_amdgcn_raw_buffer_load_b32(rs, voff, soff, 0)); }
DI void st128(rsrc_t rs, int voff, int soff, u32x4 v) { __builtin_amdgcn_raw_buffer_store_b128(v, rs, voff + soff, 0, 16); }
DI void st64(rsrc_t rs, int voff, int soff, u32x2 v) { __builtin_amdgcn_raw_buffer_store_b64(v, rs, voff, soff, 16); }

DI void poll(char* ws, int line0, int n, unsigned want, int lane) {
  gu32* c = (gu32*)(ws + OFF_CTR) + (line0 + (lane < n ? lane : 0)) * 32;
  gu32* tmo = (gu32*)(ws + OFF_CTR) + CTR_TMO * 32;
#pragma clang loop unroll(disable)
  for (unsigned spins = 0;;) {
    if (__all(__hip_atomic_load(c, RLX_AGENT) >= want)) break;
    __builtin_amdgcn_s_sleep(1);
    if (++spins > (1u << 17)) { if (lane == 0) __hip_atomic_store(tmo, 1u, RLX_AGENT); break; }
    if ((spins & 255u) == 0u && __builtin_amdgcn_readfirstlane(__hip_atomic_load(tmo, RLX_AGENT))) break;
  }
  __builtin_amdgcn_fence(__ATOMIC_ACQUIRE, "wavefront");
}
DI void arrive(char* ws, int line0, int stride, int n, int lane) {
  if (lane < n) __hip_atomic_fetch_add((gu32*)(ws + OFF_CTR) + (line0 + lane * stride) * 32, 1u, RLX_AGENT);
}

struct RowStat { float mu, rs; };
DI RowStat row_stats(const LAS char* st, int r, int h) {
  float s = 0.f, q = 0.f;
#pragma unroll
  for (int i = 0; i < 16; ++i) { const f32x2 p = *(const LAS f32x2*)(st + (h * 16 + i) * 256 + r * 8); s += p[0]; q += p[1]; }
  s = xhalf_sum(s); q = xhalf_sum(q);
  const float mean = s * (1.f / DM);
  const float var = fmaxf(q * (1.f / DM) - mean * mean, 0.f);
  const float sd = bfround(__builtin_amdgcn_sqrtf(bfround(bfround(var) + 1e-5f)));
  RowStat o; o.mu = bfround(mean); o.rs = __builtin_amdgcn_rcpf(sd); return o;
}
DI void put16(LAS char* p, const f32x16& a, int lane) {
#pragma unroll
  for (int g = 0; g < 4; ++g) *(LAS f32x4*)(p + g * 1024 + lane * 16) = f32x4{a[4 * g], a[4 * g + 1], a[4 * g + 2], a[4 * g + 3]};
}
DI void add16(const LAS char* p, f32x16& a, int lane) {
#pragma unroll
  for (int g = 0; g < 4; ++g) {
    const f32x4 v = *(const LAS f32x4*)(p + g * 1024 + lane * 16);
#pragma unroll
    for (int j = 0; j < 4; ++j) a[4 * g + j] += v[j];
  }
}
DI void put_hi(LAS char* p, const f32x16& a) {
  *(LAS f32x4*)p = f32x4{a[8], a[9], a[10], a[11]}; *(LAS f32x4*)(p + 1024) = f32x4{a[12], a[13], a[14], a[15]};
}
DI void add_lo(const LAS char* p, f32x16& a) {
  const f32x4 u = *(const LAS f32x4*)p, v = *(const LAS f32x4*)(p + 1024);
#pragma unroll
  for (int j = 0; j < 4; ++j) { a[j] += u[j]; a[4 + j] += v[j]; }
}
DI float max16(const f32x16& s) {
  float a = fmaxf(fmaxf(s[0], s[1]), s[2]), b = fmaxf(fmaxf(s[3], s[4]), s[5]);
  a = fmaxf(fmaxf(a, s[6]), s[7]); b = fmaxf(fmaxf(b, s[8]), s[9]);
  a = fmaxf(fmaxf(a, s[10]), s[11]); b = fmaxf(fmaxf(b, s[12]), s[13]);
  a = fmaxf(fmaxf(a, s[14]), s[15]);
  return fmaxf(a, b);
}

constexpr int PREP_BLOCKS = 322;
__global__ __launch_bounds__(512) void k_prep(const uint16_t* __restrict__ x0, const uint16_t* __restrict__ wqkv,
                                              const uint16_t* __restrict__ woa, const uint16_t* __restrict__ wq,
                                              const uint16_t* __restrict__ wk, const uint16_t* __restrict__ wv,
                                              const uint16_t* __restrict__ wob, char* __restrict__ ws) {
  __shared__ __attribute__((aligned(16))) char tile[256 * 80 + 8 * 32 * 4];
  const int b = blockIdx.x, tid = threadIdx.x, w = tid >> 6, lane = tid & 63, r = lane & 31, h = lane >> 5;
  if (b < 194) {
    const int nt = b >> 1, kb = b & 1, c = tid & 3;
    const uint16_t* src; int ld;
    if (nt < 48) { src = wqkv + nt * 32 + c * 8; ld = 1536; }
    else if (nt == T_WKV) { src = (c < 2 ? wk : wv) + (c & 1) * 8; ld = 16; }
    else { src = (nt < T_WQ ? woa + (nt - T_WOA) * 32 : (nt < T_WKV ? wq + (nt - T_WQ) * 32 : wob + (nt - T_WOB) * 32)) + c * 8; ld = 512; }
    u32x4 v[2];
#pragma unroll
    for (int i = 0; i < 2; ++i) v[i] = *(const u32x4*)(src + (size_t)(kb * 256 + i * 128 + (tid >> 2)) * ld);
#pragma unroll
    for (int i = 0; i < 2; ++i) *(u32x4*)(tile + (i * 128 + (tid >> 2)) * 80 + c * 16) = v[i];
    __syncthreads();
    float* red = (float*)(tile + 256 * 80);
    const int q = (lane & 15) >> 2, p = lane & 3, g1 = (lane >> 4) & 1;
    float s = 0.f;
#pragma unroll
    for (int i = 0; i < 2; ++i) {
      const LAS char* a = (const LAS char*)tile + ((2 * w + i) * 16 + 4 * h + q) * 80 + (16 * g1 + 4 * p) * 2;
      const s16x4 lo = __builtin_amdgcn_ds_read_tr16_b64_v4i16((LAS s16x4*)a);
      const s16x4 hi = __builtin_amdgcn_ds_read_tr16_b64_v4i16((LAS s16x4*)(a + 8 * 80));
      const u32x2 l2 = __builtin_bit_cast(u32x2, lo), h2 = __builtin_bit_cast(u32x2, hi);
      const u32x4 f = {l2[0], l2[1], h2[0], h2[1]};
      *(u32x4*)(ws + OFF_W + (nt * 32 + kb * 16 + 2 * w + i) * 1024 + lane * 16) = f;
#pragma unroll
      for (int j = 0; j < 4; ++j) s += bflo(f[j]) + bfhi(f[j]);
    }
    s = xhalf_sum(s);
    if (h == 0) red[w * 32 + r] = s;
    __syncthreads();
    if (tid < 32) {
      float t = 0.f;
#pragma unroll
      for (int i = 0; i < 8; ++i) t += red[i * 32 + tid];
      if (nt < 48) ((float*)(ws + OFF_CS1))[(nt & 15) * 256 + kb * 128 + (nt >> 4) * 32 + tid] = t;
      else if (nt >= T_WQ && nt < T_WKV) ((float*)(ws + OFF_CS3))[((nt - T_WQ) >> 2) * 512 + kb * 256 + ((nt - T_WQ) & 3) * 32 + tid] = t;
      else if (nt == T_WKV) {
#pragma unroll
        for (int i = 0; i < 4; ++i) ((float*)(ws + OFF_CS3))[i * 512 + kb * 256 + 128 + tid] = t;
      }
    }
  } else {
    const int tt = (b - 194) >> 1, p = ((b - 194) & 1) * 8 + w;
    const uint16_t* row = x0 + (size_t)(tt * 32 + r) * DM + p * 32 + 4 * h;
    u32x2 e[4];
#pragma unroll
    for (int i = 0; i < 4; ++i) e[i] = *(const u32x2*)(row + 8 * i);
#pragma unroll
    for (int i = 0; i < 2; ++i) {
      const u32x4 v = {e[2 * i][0], e[2 * i][1], e[2 * i + 1][0], e[2 * i + 1][1]};
      float s = 0.f, q = 0.f;
#pragma unroll
      for (int j = 0; j < 4; ++j) { const float a = bflo(v[j]), c = bfhi(v[j]); s += a + c; q += a * a + c * c; }
      *(u32x4*)(ws + OFF_XF + ((tt * 32 + 2 * p + i) * 64 + lane) * 16) = v;
      s = xhalf_sum(s); q = xhalf_sum(q);
      if (h == 0) ((f32x2*)(ws + OFF_ST))[(tt * 32 + 2 * p + i) * 32 + r] = f32x2{s, q};
    }
  }
  const int z = (PREP_BLOCKS - 1 - b) * 512 + tid;
  if (z < ZERO_BYTES / 16) ((u32x4*)(ws + OFF_CTR))[z] = u32x4{0u, 0u, 0u, 0u};
}

#define ATTN_ASM \
  "s_nop 4\n\t" \
  "s_mov_b32 %[m0s], m0\n\t" \
  "s_mov_b32 m0, %[m0a]\n\t" \
  "s_nop 0\n\t" \
  "global_load_lds_dwordx4 v95, %[wsb] sc1\n\t" \
  "global_load_lds_dwordx4 v95, %[wsb] offset:1024 sc1\n\t" \
  "s_add_u32 m0, m0, 0x2000\n\t" \
  "v_add_u32 v95, 0x2000, v95\n\t" \
  "global_load_lds_dwordx4 v95, %[wsb] sc1\n\t" \
  "global_load_lds_dwordx4 v95, %[wsb] offset:1024 sc1\n\t" \
  "s_add_u32 m0, m0, 0x2000\n\t" \
  "v_add_u32 v95, 0x2000, v95\n\t" \
  "global_load_lds_dwordx4 v95, %[wsb] sc1\n\t" \
  "global_load_lds_dwordx4 v95, %[wsb] offset:1024 sc1\n\t" \
  "s_add_u32 m0, m0, 0x2000\n\t" \
  "v_add_u32 v95, 0x2000, v95\n\t" \
  "s_waitcnt vmcnt(4)\n\t" \
  "s_barrier\n\t" \
  "ds_read_b128 v[64:67], v92 offset:0\n\t" \
  "ds_read_b128 v[68:71], v93 offset:0\n\t" \
  "ds_read_b128 v[72:75], v93 offset:0\n\t" \
  "s_mov_b32 %[n], 0\n\t" \
  "s_waitcnt lgkmcnt(0)\n\t" \
  "v_mfma_f32_32x32x16_bf16 v[32:47], v[64:67], v[84:87], 0\n\t" \
  "s_nop 7\n\t" \
  "s_nop 7\n\t" \
  "1:\n\t" \
  "v_exp_f32 v32, v32\n\t" \
  "v_exp_f32 v33, v33\n\t" \
  "s_waitcnt lgkmcnt(2)\n\t" \
  "v_exp_f32 v34, v34\n\t" \
  "v_mfma_f32_32x32x16_bf16 v[16:31], v[72:75], v[80:83], v[16:31]\n\t" \
  "ds_read_b128 v[72:75], v93 offset:512\n\t" \
  "v_exp_f32 v35, v35\n\t" \
  "v_cvt_pk_bf16_f32 v76, v32, v33\n\t" \
  "v_exp_f32 v36, v36\n\t" \
  "v_exp_f32 v37, v37\n\t" \
  "v_mfma_f32_32x32x16_bf16 v[48:63], v[64:67], v[88:91], 0\n\t" \
  "ds_read_b128 v[64:67], v92 offset:2048\n\t" \
  "v_cvt_pk_bf16_f32 v77, v34, v35\n\t" \
  "v_exp_f32 v38, v38\n\t" \
  "v_exp_f32 v39, v39\n\t" \
  "v_cvt_pk_bf16_f32 v78, v36, v37\n\t" \
  "v_exp_f32 v40, v40\n\t" \
  "v_exp_f32 v41, v41\n\t" \
  "v_cvt_pk_bf16_f32 v79, v38, v39\n\t" \
  "v_exp_f32 v42, v42\n\t" \
  "v_exp_f32 v43, v43\n\t" \
  "s_waitcnt lgkmcnt(2)\n\t" \
  "v_exp_f32 v44, v44\n\t" \
  "v_mfma_f32_32x32x16_bf16 v[0:15], v[68:71], v[76:79], v[0:15]\n\t" \
  "v_cvt_pk_bf16_f32 v80, v40, v41\n\t" \
  "v_exp_f32 v45, v45\n\t" \
  "v_cvt_pk_bf16_f32 v81, v42, v43\n\t" \
  "v_exp_f32 v46, v46\n\t" \
  "v_exp_f32 v47, v47\n\t" \
  "v_cvt_pk_bf16_f32 v82, v44, v45\n\t" \
  "v_cvt_pk_bf16_f32 v83, v46, v47\n\t" \
  "v_exp_f32 v48, v48\n\t" \
  "v_exp_f32 v49, v49\n\t" \
  "s_waitcnt lgkmcnt(1)\n\t" \
  "v_exp_f32 v50, v50\n\t" \
  "v_mfma_f32_32x32x16_bf16 v[0:15], v[72:75], v[80:83], v[0:15]\n\t" \
  "v_exp_f32 v51, v51\n\t" \
  "v_cvt_pk_bf16_f32 v76, v48, v49\n\t" \
  "v_exp_f32 v52, v52\n\t" \
  "v_exp_f32 v53, v53\n\t" \
  "s_waitcnt lgkmcnt(0)\n\t" \
  "v_exp_f32 v54, v54\n\t" \
  "v_mfma_f32_32x32x16_bf16 v[32:47], v[64:67], v[84:87], 0\n\t" \
  "v_cvt_pk_bf16_f32 v77, v50, v51\n\t" \
  "v_exp_f32 v55, v55\n\t" \
  "v_cvt_pk_bf16_f32 v78, v52, v53\n\t" \
  "v_exp_f32 v56, v56\n\t" \
  "v_exp_f32 v57, v57\n\t" \
  "v_cvt_pk_bf16_f32 v79, v54, v55\n\t" \
  "v_exp_f32 v58, v58\n\t" \
  "v_exp_f32 v59, v59\n\t" \
  "v_mfma_f32_32x32x16_bf16 v[16:31], v[68:71], v[76:79], v[16:31]\n\t" \
  "ds_read_b128 v[68:71], v93 offset:2048\n\t" \
  "v_cvt_pk_bf16_f32 v80, v56, v57\n\t" \
  "v_exp_f32 v60, v60\n\t" \
  "v_exp_f32 v61, v61\n\t" \
  "v_cvt_pk_bf16_f32 v81, v58, v59\n\t" \
  "v_exp_f32 v62, v62\n\t" \
  "v_exp_f32 v63, v63\n\t" \
  "v_cvt_pk_bf16_f32 v82, v60, v61\n\t" \
  "v_cvt_pk_bf16_f32 v83, v62, v63\n\t" \
  "v_exp_f32 v32, v32\n\t" \
  "v_exp_f32 v33, v33\n\t" \
  "s_waitcnt lgkmcnt(2)\n\t" \
  "v_exp_f32 v34, v34\n\t" \
  "v_mfma_f32_32x32x16_bf16 v[16:31], v[72:75], v[80:83], v[16:31]\n\t" \
  "ds_read_b128 v[72:75], v93 offset:2560\n\t" \
  "v_exp_f32 v35, v35\n\t" \
  "v_cvt_pk_bf16_f32 v76, v32, v33\n\t" \
  "v_exp_f32 v36, v36\n\t" \
  "v_exp_f32 v37, v37\n\t" \
  "v_mfma_f32_32x32x16_bf16 v[48:63], v[64:67], v[88:91], 0\n\t" \
  "ds_read_b128 v[64:67], v92 offset:4096\n\t" \
  "v_cvt_pk_bf16_f32 v77, v34, v35\n\t" \
  "v_exp_f32 v38, v38\n\t" \
  "v_exp_f32 v39, v39\n\t" \
  "v_cvt_pk_bf16_f32 v78, v36, v37\n\t" \
  "v_exp_f32 v40, v40\n\t" \
  "v_exp_f32 v41, v41\n\t" \
  "v_cvt_pk_bf16_f32 v79, v38, v39\n\t" \
  "v_exp_f32 v42, v42\n\t" \
  "v_exp_f32 v43, v43\n\t" \
  "s_waitcnt lgkmcnt(2)\n\t" \
  "v_exp_f32 v44, v44\n\t" \
  "v_mfma_f32_32x32x16_bf16 v[0:15], v[68:71], v[76:79], v[0:15]\n\t" \
  "v_cvt_pk_bf16_f32 v80, v40, v41\n\t" \
  "v_exp_f32 v45, v45\n\t" \
  "v_cvt_pk_bf16_f32 v81, v42, v43\n\t" \
  "v_exp_f32 v46, v46\n\t" \
  "v_exp_f32 v47, v47\n\t" \
  "v_cvt_pk_bf16_f32 v82, v44, v45\n\t" \
  "v_cvt_pk_bf16_f32 v83, v46, v47\n\t" \
  "v_exp_f32 v48, v48\n\t" \
  "v_exp_f32 v49, v49\n\t" \
  "s_waitcnt lgkmcnt(1)\n\t" \
  "v_exp_f32 v50, v50\n\t" \
  "v_mfma_f32_32x32x16_bf16 v[0:15], v[72:75], v[80:83], v[0:15]\n\t" \
  "v_exp_f32 v51, v51\n\t" \
  "v_cvt_pk_bf16_f32 v76, v48, v49\n\t" \
  "v_exp_f32 v52, v52\n\t" \
  "v_exp_f32 v53, v53\n\t" \
  "s_waitcnt lgkmcnt(0)\n\t" \
  "v_exp_f32 v54, v54\n\t" \
  "v_mfma_f32_32x32x16_bf16 v[32:47], v[64:67], v[84:87], 0\n\t" \
  "v_cvt_pk_bf16_f32 v77, v50, v51\n\t" \
  "v_exp_f32 v55, v55\n\t" \
  "v_cvt_pk_bf16_f32 v78, v52, v53\n\t" \
  "v_exp_f32 v56, v56\n\t" \
  "v_exp_f32 v57, v57\n\t" \
  "v_cvt_pk_bf16_f32 v79, v54, v55\n\t" \
  "v_exp_f32 v58, v58\n\t" \
  "v_exp_f32 v59, v59\n\t" \
  "v_mfma_f32_32x32x16_bf16 v[16:31], v[68:71], v[76:79], v[16:31]\n\t" \
  "ds_read_b128 v[68:71], v93 offset:4096\n\t" \
  "v_cvt_pk_bf16_f32 v80, v56, v57\n\t" \
  "v_exp_f32 v60, v60\n\t" \
  "v_exp_f32 v61, v61\n\t" \
  "v_cvt_pk_bf16_f32 v81, v58, v59\n\t" \
  "v_exp_f32 v62, v62\n\t" \
  "v_exp_f32 v63, v63\n\t" \
  "v_cvt_pk_bf16_f32 v82, v60, v61\n\t" \
  "v_cvt_pk_bf16_f32 v83, v62, v63\n\t" \
  "v_add_u32 v92, 0x1000, v92\n\t" \
  "v_add_u32 v93, v94, v93\n\t" \
  "s_add_u32 %[n], %[n], 1\n\t" \
  "s_cmp_lt_u32 %[n], 6\n\t" \
  "s_cbranch_scc0 2f\n\t" \
  "s_bitcmp1_b32 0xa, %[n]\n\t" \
  "s_cbranch_scc0 3f\n\t" \
  "s_waitcnt vmcnt(2)\n\t" \
  "s_barrier\n\t" \
  "3:\n\t" \
  "global_load_lds_dwordx4 v95, %[wsb] sc1\n\t" \
  "global_load_lds_dwordx4 v95, %[wsb] offset:1024 sc1\n\t" \
  "s_add_u32 m0, m0, 0x2000\n\t" \
  "v_add_u32 v95, 0x2000, v95\n\t" \
  "s_branch 1b\n\t" \
  "2:\n\t" \
  "s_cmp_eq_u32 %[n], 6\n\t" \
  "s_cbranch_scc1 1b\n\t" \
  "s_cmp_eq_u32 %[n], 7\n\t" \
  "s_cbranch_scc0 17f\n\t" \
  "s_waitcnt vmcnt(0)\n\t" \
  "s_barrier\n\t" \
  "s_mov_b32 m0, %[m0s]\n\t" \
  "global_load_dwordx4 v[128:131], v96, %[wsb]\n\t" \
  "global_load_dwordx4 v[132:135], v96, %[wsb] offset:1024\n\t" \
  "v_add_u32 v96, 0x800, v96\n\t" \
  "global_load_dwordx4 v[192:195], v97, %[wsb] offset:0\n\t" \
  "global_load_dwordx4 v[196:199], v97, %[wsb] offset:1024\n\t" \
  "s_branch 1b\n\t" \
  "17:\n\t" \
  "s_cmp_eq_u32 %[n], 8\n\t" \
  "s_cbranch_scc0 18f\n\t" \
  "global_load_dwordx4 v[136:139], v96, %[wsb]\n\t" \
  "global_load_dwordx4 v[140:143], v96, %[wsb] offset:1024\n\t" \
  "v_add_u32 v96, 0x800, v96\n\t" \
  "global_load_dwordx4 v[200:203], v97, %[wsb] offset:2048\n\t" \
  "global_load_dwordx4 v[204:207], v97, %[wsb] offset:3072\n\t" \
  "v_add_u32 v97, 0x80000, v97\n\t" \
  "s_branch 1b\n\t" \
  "18:\n\t" \
  "s_cmp_eq_u32 %[n], 9\n\t" \
  "s_cbranch_scc0 19f\n\t" \
  "global_load_dwordx4 v[144:147], v96, %[wsb]\n\t" \
  "global_load_dwordx4 v[148:151], v96, %[wsb] offset:1024\n\t" \
  "v_add_u32 v96, 0x800, v96\n\t" \
  "global_load_dwordx4 v[208:211], v97, %[wsb] offset:0\n\t" \
  "global_load_dwordx4 v[212:215], v97, %[wsb] offset:1024\n\t" \
  "s_branch 1b\n\t" \
  "19:\n\t" \
  "s_cmp_eq_u32 %[n], 10\n\t" \
  "s_cbranch_scc0 20f\n\t" \
  "global_load_dwordx4 v[152:155], v96, %[wsb]\n\t" \
  "global_load_dwordx4 v[156:159], v96, %[wsb] offset:1024\n\t" \
  "v_add_u32 v96, 0x800, v96\n\t" \
  "global_load_dwordx4 v[216:219], v97, %[wsb] offset:2048\n\t" \
  "global_load_dwordx4 v[220:223], v97, %[wsb] offset:3072\n\t" \
  "v_add_u32 v97, 0x80000, v97\n\t" \
  "s_branch 1b\n\t" \
  "20:\n\t" \
  "s_cmp_eq_u32 %[n], 11\n\t" \
  "s_cbranch_scc0 21f\n\t" \
  "global_load_dwordx4 v[160:163], v96, %[wsb]\n\t" \
  "global_load_dwordx4 v[164:167], v96, %[wsb] offset:1024\n\t" \
  "v_add_u32 v96, 0x800, v96\n\t" \
  "global_load_dwordx4 v[224:227], v97, %[wsb] offset:0\n\t" \
  "global_load_dwordx4 v[228:231], v97, %[wsb] offset:1024\n\t" \
  "s_branch 1b\n\t" \
  "21:\n\t" \
  "s_cmp_eq_u32 %[n], 12\n\t" \
  "s_cbranch_scc0 22f\n\t" \
  "s_setprio 0\n\t" \
  "global_load_dwordx4 v[168:171], v96, %[wsb]\n\t" \
  "global_load_dwordx4 v[172:175], v96, %[wsb] offset:1024\n\t" \
  "v_add_u32 v96, 0x800, v96\n\t" \
  "global_load_dwordx4 v[232:235], v97, %[wsb] offset:2048\n\t" \
  "global_load_dwordx4 v[236:239], v97, %[wsb] offset:3072\n\t" \
  "v_add_u32 v97, 0x80000, v97\n\t" \
  "s_branch 1b\n\t" \
  "22:\n\t" \
  "s_cmp_eq_u32 %[n], 13\n\t" \
  "s_cbranch_scc0 23f\n\t" \
  "global_load_dwordx4 v[176:179], v96, %[wsb]\n\t" \
  "global_load_dwordx4 v[180:183], v96, %[wsb] offset:1024\n\t" \
  "v_add_u32 v96, 0x800, v96\n\t" \
  "s_branch 1b\n\t" \
  "23:\n\t" \
  "s_cmp_eq_u32 %[n], 14\n\t" \
  "s_cbranch_scc0 24f\n\t" \
  "global_load_dwordx4 v[184:187], v96, %[wsb]\n\t" \
  "global_load_dwordx4 v[188:191], v96, %[wsb] offset:1024\n\t" \
  "v_add_u32 v96, 0x800, v96\n\t" \
  "s_branch 1b\n\t" \
  "24:\n\t" \
  "s_cmp_eq_u32 %[n], 15\n\t" \
  "s_cbranch_scc1 1b\n\t" \
  "s_waitcnt lgkmcnt(0)\n\t" \
  "v_mfma_f32_32x32x16_bf16 v[16:31], v[72:75], v[80:83], v[16:31]\n\t" \
  "s_setprio 0\n\t" \
  "s_waitcnt vmcnt(0)\n\t" \
  "s_nop 7\n\t" \
  "s_nop 7\n\t" \
  ""

template <class W, class H> DI f32x16 gemm_half(const LAS char* act, W wf, H hook) {
  f32x16 acc = zero16();
  u32x4 a[16];
#pragma unroll
  for (int i = 0; i < 4; ++i) a[i] = *(const LAS u32x4*)(act + i * 1024);
#pragma unroll
  for (int i = 0; i < 16; ++i) {
    SBAR();
    if (i + 4 < 16) a[i + 4] = *(const LAS u32x4*)(act + (i + 4) * 1024);
    hook(i);
    acc = MFMA(wf(i), a[i], acc);
  }
  SBAR();
  return acc;
}
DI f32x4 lds2(const LAS char* p, int half) { return *(const LAS f32x4*)p + *(const LAS f32x4*)(p + half); }
DI void resid_epilogue(const f32x16& acc, u32x4& xr, rsrc_t rs, int xoff, int soff, uint16_t* out, bool final_, int tt, int ks, int lane) {
  const int r = lane & 31, h = lane >> 5;
  float s = 0.f, q = 0.f;
  const u32x4 y = pack8(acc, 0);
#pragma unroll
  for (int j = 0; j < 4; ++j) {
    xr[j] = cvtpk(bflo(xr[j]) + bflo(y[j]), bfhi(xr[j]) + bfhi(y[j]));
    const float a = bflo(xr[j]), c = bfhi(xr[j]); s += a + c; q += a * a + c * c;
  }
  if (final_) {
    auto a = __builtin_amdgcn_permlane32_swap(xr[0], xr[2], false, false);
    auto c = __builtin_amdgcn_permlane32_swap(xr[1], xr[3], false, false);
    *(u32x4*)(out + (size_t)(tt * 32 + r) * DM + ks * 16 + 8 * h) = u32x4{a[0], c[0], a[1], c[1]};
  } else {
    st128(rs, lane * 16, xoff + (tt * 32 + ks) * 1024, xr);
    s = xhalf_sum(s); q = xhalf_sum(q);
    if (h == 0) st64(rs, r * 8, soff + (tt * 32 + ks) * 256, u32x2{__float_as_uint(s), __float_as_uint(q)});
  }
}

#define NEARLY 8
__global__ __launch_bounds__(512, 2) void k_mega(char* ws, uint16_t* out) {
  extern __shared__ __attribute__((aligned(16))) char smem_[];
  LAS char* smem = (LAS char*)smem_;
  const unsigned lds0 = (unsigned)(uintptr_t)smem;
  const rsrc_t rs = __builtin_amdgcn_make_buffer_rsrc(ws, 0, WS_END, 0x00020000);
  const int w_ = __builtin_amdgcn_readfirstlane(threadIdx.x >> 6);
#define IDS() int tid = threadIdx.x, w = w_, b = blockIdx.x; asm volatile("" : "+v"(tid)); asm volatile("" : "+s"(w), "+s"(b)); \
  const int lane = tid & 63, r = lane & 31, h = lane >> 5, w4 = w & 3, kh = w >> 2, xcd = b & 7, idx = b >> 3; \
  const int tt = xcd * 8 + (idx >> 2), cb = idx & 3, nt = cb * 4 + w4;                 \
  const int panel = (xcd & 3) * 4 + (idx & 3), hp = (xcd >> 2) * 8 + (idx >> 2);       \
  const unsigned voff = lane * 16, vrot = voff ^ (kh << 8); \
  (void)r; (void)h; (void)tt; (void)nt; (void)panel; (void)hp; (void)voff; (void)vrot
  u32x4 xr;
  STAMP_DECL;
  {
    IDS();
    STAMP(0);
#pragma unroll
    for (int t = 0; t < 3; ++t) glds4<false>(ws, OFF_W + ((t * 16 + hp) * 32 + 4 * w) * 1024 + voff, lds0 + L_WL + (t * 32 + 4 * w) * 1024);
    xr = ld128(rs, voff, OFF_XF + (tt * 32 + 2 * nt + kh) * 1024);
  }

#pragma unroll 1
  for (int L = 0; L < 2; ++L) {
    const int xa = OFF_XF + (2 * L) * 2048 * KB, xb = xa + 2048 * KB, xc = xb + 2048 * KB;
    const int sa_ = OFF_ST + (2 * L) * 512 * KB, sb_ = sa_ + 512 * KB, sc_ = sb_ + 512 * KB;
    const int pqo = OFF_PQ + L * 2048 * KB, ofo = OFF_OF + L * 2048 * KB, kvo = OFF_KV + L * 128 * KB;
    const int acco = OFF_ACC + L * ACC_N * 4;
    u32x4 wp[16];
    {
      IDS();
      const int t1 = panel * 4 + w4;
      constexpr int WIN = 4;
      u32x4 xf[16];
#define XLD(i) xf[i] = ld128(rs, vrot + ((i) & 3) * 1024, xa + (t1 * 32 + kh * 16 + ((i) & ~3)) * 1024)
      XLD(0);
      SBAR();
      glds4<true>(ws, sa_ + t1 * 8192 + kh * 4096 + voff, lds0 + L_ACT + w4 * 8192 + kh * 4096);
      if (w == 4) glds1(ws, OFF_CS1 + hp * 1024 + voff, lds0 + 128 * KB);
#pragma unroll
      for (int i = 1; i < WIN; ++i) XLD(i);
      SBAR();
      PIN(xf[1]);
      BAR();
      STAMP(1 + 16 * L);
      f32x16 aq = zero16(), ak = zero16(), av = zero16();
      const LAS char* wl = smem + L_WL + kh * 16384;
      u32x4 wf[2][3];
#define WLD(i) do { wf[(i) & 1][0] = *(const LAS u32x4*)(wl + vrot + (i) * 1024); wf[(i) & 1][1] = *(const LAS u32x4*)(wl + voff + (32 + (i)) * 1024); \
                    wf[(i) & 1][2] = *(const LAS u32x4*)(wl + voff + (64 + (i)) * 1024); } while (0)
      WLD(0);
      RowStat st;
      LAS float* sst = (LAS float*)(smem + 129 * KB + w * 256);
#pragma unroll
      for (int i = 0; i < 16; ++i) {
        SBAR();
        if (2 * i + WIN < 16) { XLD(2 * i + WIN); XLD(2 * i + WIN + 1); }
        if (i >= 8) {
#pragma unroll
          for (int k = 2 * i - 16; k < 2 * i - 14; ++k) wp[k] = ldw(rs, vrot + (k & 3) * 1024, OFF_W + ((T_WOA + nt) * 32 + kh * 16 + (k & ~3)) * 1024);
        }
        if (i < 15) WLD(i + 1);
        aq = MFMA(wf[i & 1][0], xf[i], aq);
        ak = MFMA(xf[i], wf[i & 1][1], ak);
        av = MFMA(xf[i], wf[i & 1][2], av);
        if (i == 1) {
          st = row_stats(smem + L_ACT + w4 * 8192, r ^ (kh << 4), h);
          if (h == 0) { sst[r] = st.mu; sst[32 + r] = st.rs; }
        }
      }
      SBAR();
      STAMP(2 + 16 * L);
      LAS char* exw = smem + (kh ? L_WL : 136 * KB) + w4 * 6144;
      LAS char* exr = smem + (kh ? 136 * KB : L_WL) + w4 * 6144;
      const LAS char* csl = smem + 128 * KB;
      const float csk = *(const LAS float*)(csl + 128 + r * 4) + *(const LAS float*)(csl + 640 + r * 4);
      const float csv = *(const LAS float*)(csl + 256 + r * 4) + *(const LAS float*)(csl + 768 + r * 4);
      f32x4 cs[2], mu[2], rr[2];
#pragma unroll
      for (int g = 0; g < 2; ++g) {
        cs[g] = lds2(csl + kh * 64 + g * 32 + h * 16, 512);
        mu[g] = *(const LAS f32x4*)(sst + 8 * g + 4 * h); rr[g] = *(const LAS f32x4*)(sst + 32 + 8 * g + 4 * h);
      }
      if (kh == 1) BAR();
      put_hi(exw + vrot, aq); put_hi(exw + 2048 + voff, ak); put_hi(exw + 4096 + voff, av);
      if (kh == 0) BAR();
      BAR();
      STAMP(42);
      add_lo(exr + vrot, aq); add_lo(exr + 2048 + voff, ak); add_lo(exr + 4096 + voff, av);
      const float rsl = st.rs * LOG2E;
#pragma unroll
      for (int g = 0; g < 2; ++g) {
#pragma unroll
        for (int j = 0; j < 4; ++j) {
          ak[4 * g + j] = phi(ak[4 * g + j] - mu[g][j] * csk, rr[g][j], rr[g][j] * LOG2E);
          av[4 * g + j] = rr[g][j] * (av[4 * g + j] - mu[g][j] * csv);
        }
      }
      const u32x4 ones = {0x3f803f80u, 0x3f803f80u, 0x3f803f80u, 0x3f803f80u};
      const u32x4 pk = pack8(ak, 0);
      const f32x16 z = MFMA(pk, pack8(av, 0), zero16());
      const f32x16 zs = MFMA(pk, ones, zero16());
#pragma unroll
      for (int j = 0; j < 8; ++j) aq[j] = phi(aq[j] - st.mu * cs[j >> 2][j & 3], st.rs, rsl);
      st128(rs, vrot, pqo + (t1 * 32 + 2 * hp + kh) * 1024, pack8(aq, 0));
      {
        LAS float* red = (LAS float*)exr + h * 8 + (r & 15) * 16;
        if (r < 16) {
          *(LAS f32x4*)red = f32x4{z[0], z[1], z[2], z[3]}; *(LAS f32x4*)(red + 4) = f32x4{z[4], z[5], z[6], z[7]};
          if (r == 0) { *(LAS f32x4*)(red + 256) = f32x4{zs[0], zs[1], zs[2], zs[3]}; *(LAS f32x4*)(red + 260) = f32x4{zs[4], zs[5], zs[6], zs[7]}; }
          asm volatile("" ::: "memory");
        } else {
          *(LAS f32x4*)(red + 272) = f32x4{z[8], z[9], z[10], z[11]}; *(LAS f32x4*)(red + 276) = f32x4{z[12], z[13], z[14], z[15]};
          if (r == 16) { *(LAS f32x4*)(red + 528) = f32x4{zs[8], zs[9], zs[10], zs[11]}; *(LAS f32x4*)(red + 532) = f32x4{zs[12], zs[13], zs[14], zs[15]}; }
        }
      }
      STAMP(43);
      BAR();
#pragma unroll
      for (int i = 0; i < 2; ++i) {
        const int j = i * 512 + tid;
        if (j < 544) {
          const LAS float* ra = (const LAS float*)(smem + L_WL) + j;
          float t = 0.f;
#pragma unroll
          for (int k = 0; k < 4; ++k) t += ra[k * 1536] + ra[26 * 1024 + k * 1536];
          atomicAdd((float*)(ws + acco) + hp * 544 + j, t);
        }
      }
      STAMP(45);
      DRAIN();
      STAMP(3 + 16 * L);
      BAR();
      if (w == 0) {
        arrive(ws, CTR_A + L * 128 + (idx & 15), 16, 8, lane);
        poll(ws, CTR_A + L * 128 + xcd * 16, 16, 16, lane);
      }
      BAR();
      STAMP(4 + 16 * L);
    }
    u32x4 wkv[8];
    {
      IDS();
      u32x4 pq[4], kv[4][2];
      const int row = r < 16 ? r : 16;
#pragma unroll
      for (int i = 0; i < 4; ++i) {
        kv[i][0] = ld128(rs, row * 64 + h * 32 + i * 1088, acco + w * 4352);
        kv[i][1] = ld128(rs, row * 64 + h * 32 + i * 1088 + 16, acco + w * 4352);
        pq[i] = ld128(rs, voff + i * 1024, pqo + (tt * 32 + w * 4) * 1024);
      }
      SBAR();
      STAMP(46);
#pragma unroll
      for (int i = 0; i < 4; ++i) {
        const f32x4 k0 = __builtin_bit_cast(f32x4, kv[i][0]), k1 = __builtin_bit_cast(f32x4, kv[i][1]);
        const u32x4 a = {cvtpk(k0[0], k0[1]), cvtpk(k0[2], k0[3]), cvtpk(k1[0], k1[1]), cvtpk(k1[2], k1[3])};
        f32x16 d = MFMA(a, pq[i], zero16());
        const float inv = __builtin_amdgcn_rcpf(d[8] + 1e-6f);
#pragma unroll
        for (int j = 0; j < 8; ++j) d[j] *= inv;
        *(LAS u32x4*)(smem + L_ACT + (w * 4 + i) * 1024 + voff) = pack8(d, 0);
      }
      STAMP(50);
      BAR();
      STAMP(5 + 16 * L);
      const int wqo = OFF_W + ((T_WQ + nt) * 32 + kh * 16) * 1024;
      {
        const int wo = cb == 0 && kh == 1 ? OFF_W + (T_WKV * 32 + w4 * 8) * 1024 : WS_END;
#pragma unroll
        for (int i = 0; i < 8; ++i) wkv[i] = ldw(rs, voff + (i & 3) * 1024, wo + (i & 4) * 1024);
      }
      f32x16 acc = gemm_half(smem + L_ACT + kh * 16384 + voff, [&](int i) { return wp[i]; },
                             [&](int i) { if (i >= 1 && i <= NEARLY) wp[i - 1] = ldw(rs, voff + ((i - 1) & 3) * 1024, wqo + ((i - 1) & ~3) * 1024); });
      STAMP(6 + 16 * L);
      put_hi(smem + L_WL + w * 2048 + voff, acc);
      BAR();
      STAMP(51);
      add_lo(smem + L_WL + (w ^ 4) * 2048 + voff, acc);
      resid_epilogue(acc, xr, rs, xb, sb_, out, false, tt, 2 * nt + kh, lane);
      STAMP(52);
      DRAIN();
      STAMP(7 + 16 * L);
      BAR();
      if (w != 0) {
#pragma unroll
        for (int i = NEARLY; i < 16; ++i) wp[i] = ldw(rs, voff + (i & 3) * 1024, wqo + (i & ~3) * 1024);
      }
      SBAR();
      if (w == 0) {
        arrive(ws, CTR_B + L * 64 + tt, 1, 1, lane);
        poll(ws, CTR_B + L * 64 + tt, 1, 4, lane);
      } else if (w == 1) glds2(ws, OFF_CS3 + cb * 2048 + voff, lds0 + 56 * KB);
      BAR();
      STAMP(8 + 16 * L);
    }
    u32x4 qa, qb;
    {
      IDS();
      glds4<true>(ws, xb + (tt * 32 + 4 * w) * 1024 + voff, lds0 + L_ACT + 4 * w * 1024);
      glds1s(ws, sb_ + tt * 8192 + w * 1024 + voff, lds0 + 48 * KB + w * 1024);
      DRAIN();
      BAR();
      STAMP(9 + 16 * L);
      if (w == 0) {
#pragma unroll
        for (int i = NEARLY; i < 16; ++i) wp[i] = ldw(rs, voff + (i & 3) * 1024, OFF_W + ((T_WQ + nt) * 32 + (i & ~3)) * 1024);
      }
      SBAR();
      const LAS char* csl = smem + 56 * KB + h * 16;
      RowStat st;
      if (cb == 0) {
        f32x16 kacc = zero16();
        if (kh == 1) {
          u32x4 a[8];
#pragma unroll
          for (int i = 0; i < 8; ++i) a[i] = *(const LAS u32x4*)(smem + L_ACT + (w4 * 8 + i) * 1024 + voff);
          SBAR();
#pragma unroll
          for (int i = 0; i < 8; ++i) kacc = MFMA(wkv[i], a[i], kacc);
          if (w4 > 0) put16(smem + 60 * KB + (w4 - 1) * 4096, kacc, lane);
        }
        st = row_stats(smem + 48 * KB, r, h);
        BAR();
        if (w == 4) {
          add16(smem + 60 * KB, kacc, lane); add16(smem + 64 * KB, kacc, lane); add16(smem + 68 * KB, kacc, lane);
#pragma unroll
          for (int g = 0; g < 4; ++g) {
            const f32x4 c = lds2(csl + 512 + g * 32, 1024);
#pragma unroll
            for (int j = 0; j < 4; ++j) kacc[4 * g + j] = st.rs * (kacc[4 * g + j] - st.mu * c[j]);
          }
          const u32x4 v = pack8(kacc, 1);
          const int t16 = r & 15, hh = (t16 >> 2) & 1, jj = ((t16 >> 3) << 2) | (t16 & 3);
          LAS uint16_t* vb = (LAS uint16_t*)(smem + 72 * KB + (r >> 4) * 512 + hh * 256) + jj;
#pragma unroll
          for (int j = 0; j < 8; ++j) vb[(8 * (j >> 2) + 4 * h + (j & 3)) * 8] = (uint16_t)((j & 1) ? (v[j >> 1] >> 16) : (v[j >> 1] & 0xffffu));
          st128(rs, voff, kvo + tt * 2048, pack8(kacc, 0));
          st128(rs, voff + 1024, kvo + tt * 2048, *(const LAS u32x4*)(smem + 72 * KB + voff));
          DRAIN();
          arrive(ws, CTR_C + L * 128 + (tt & 15), 16, 8, lane);
          STAMP(53);
        }
      }
      if (cb != 0) st = row_stats(smem + 48 * KB, r, h);
      f32x16 acc = gemm_half(smem + L_ACT + kh * 16384 + voff, [&](int i) { return wp[i]; }, [](int) {});
      put16(smem + 80 * KB + w * 4096, acc, lane);
      BAR();
      add16(smem + 80 * KB + (w ^ 4) * 4096, acc, lane);
      const float sc = st.rs * (0.25f * LOG2E);
#pragma unroll
      for (int g = 0; g < 4; ++g) {
        const f32x4 c = lds2(csl + w4 * 128 + g * 32, 1024);
#pragma unroll
        for (int j = 0; j < 4; ++j) acc[4 * g + j] = sc * (acc[4 * g + j] - st.mu * c[j]);
      }
      qa = pack8(acc, 0); qb = pack8(acc, 1);
      PIN(qa); PIN(qb);
      STAMP(10 + 16 * L);
      if (w == 0) poll(ws, CTR_C + L * 128 + xcd * 16, 16, 4, lane);
      BAR();
      STAMP(11 + 16 * L);
    }
    u32x16 wo0, wo1, wo2, wo3, wn0, wn1, wn2;
    {
      IDS();
      if (tid < 10) {
        const int s5 = tid >> 1, o5 = s5 == 0 ? 0 : (s5 == 1 ? 512 : (s5 == 2 ? 2048 : (s5 == 3 ? 2560 : 4096)));
        const unsigned c = (tid & 1) ? 0u : 0x3f803f80u;
        *(LAS u32x4*)(smem + L_CONST + o5 + (tid & 1) * 16) = u32x4{c, c, c, c};
      }
      asm volatile("s_waitcnt lgkmcnt(0)" ::: "memory");
      f32x16 oa = zero16(), ob = zero16(), sa, sb;
      u32x4 kf, v0, v1, p0, p1 = {0u, 0u, 0u, 0u};
      unsigned ka = lds0 + kh * 65536 + lane * 16;
      unsigned va = r < 16 ? lds0 + kh * 65536 + 1024 + (h * 16 + r) * 16 : lds0 + L_CONST + ((r == 16 || r == 20) ? 0 : 16);
      const unsigned vinc = r < 16 ? 4096u : 0u;
      const unsigned o0 = kh * 65536 + w4 * 2048;
      unsigned ga = kvo + o0 + voff, wo = OFF_W + ((T_WOB + nt) * 32 + kh * 16) * 1024 + vrot, n, m0s;
      unsigned wn = OFF_W + (hp * 32 + 4 * w) * 1024 + voff;
      const u32x4 q0 = kh ? qb : qa, q1 = kh ? qa : qb;
      if (kh == 1) __builtin_amdgcn_s_setprio(1);
      asm volatile(ATTN_ASM
                   : "+{v[0:15]}"(oa), "+{v[16:31]}"(ob), "=&{v[32:47]}"(sa), "=&{v[48:63]}"(sb), "=&{v[64:67]}"(kf), "=&{v[68:71]}"(v0),
                     "=&{v[72:75]}"(v1), "=&{v[76:79]}"(p0), "+{v[80:83]}"(p1), "+{v92}"(ka), "+{v93}"(va), "+{v95}"(ga), "+{v96}"(wo),
                     "=&{v[128:143]}"(wo0), "=&{v[144:159]}"(wo1), "=&{v[160:175]}"(wo2), "=&{v[176:191]}"(wo3),
                     "+{v97}"(wn), "=&{v[192:207]}"(wn0), "=&{v[208:223]}"(wn1), "=&{v[224:239]}"(wn2),
                     [n] "=&s"(n), [m0s] "=&s"(m0s)
                   : "{v[84:87]}"(q0), "{v[88:91]}"(q1), "{v94}"(vinc), [wsb] "s"(ws), [m0a] "s"(__builtin_amdgcn_readfirstlane(lds0 + o0))
                   : "memory", "scc");
      STAMP(12 + 16 * L);
      LAS char* cw = smem + L_COMB + w * 3072 + voff;
      const LAS char* cr = smem + L_COMB + (w ^ 4) * 3072 + voff;
      *(LAS f32x4*)cw = f32x4{ob[0], ob[1], ob[2], ob[3]}; *(LAS f32x4*)(cw + 1024) = f32x4{ob[4], ob[5], ob[6], ob[7]};
      *(LAS float*)(cw + 2048) = ob[8];
      BAR();
      STAMP(54);
      add_lo(cr, oa);
      oa[8] += *(const LAS float*)(cr + 2048);
      float mx = fmaxf(fmaxf(fabsf(oa[0]), fabsf(oa[1])), fabsf(oa[2]));
      mx = fmaxf(fmaxf(mx, fabsf(oa[3])), fabsf(oa[4])); mx = fmaxf(fmaxf(mx, fabsf(oa[5])), fabsf(oa[6]));
      const float sum8 = ((oa[0] + oa[1]) + (oa[2] + oa[3])) + ((oa[4] + oa[5]) + (oa[6] + oa[7]));
      const bool ok = fmaxf(fmaxf(mx, fabsf(oa[7])), oa[8]) < 1.0e36f && oa[8] > 1.0e-27f && sum8 == sum8;
      if (__builtin_expect(!__all(ok), 0)) {
        const LAS char* kb0 = smem + lane * 16;
        const LAS char* vb0 = r < 16 ? smem + 1024 + (h * 16 + r) * 16 : smem + L_CONST + ((r == 16 || r == 20) ? 0 : 16);
        const int vstep = r < 16 ? 2048 : 0;
        float m = -INFINITY;
        oa = zero16();
#pragma unroll 1
        for (int t = 0; t < 64; ++t) {
          f32x16 s = MFMA(*(const LAS u32x4*)(kb0 + t * 2048), q0, zero16());
          const float mn = fmaxf(m, xhalf_max(max16(s)));
          const float al = __builtin_amdgcn_exp2f(m - mn);
          m = mn;
#pragma unroll
          for (int i = 0; i < 16; ++i) { oa[i] *= al; s[i] = __builtin_amdgcn_exp2f(s[i] - mn); }
          oa = MFMA(*(const LAS u32x4*)(vb0 + t * vstep), pack8(s, 0), oa);
          oa = MFMA(*(const LAS u32x4*)(vb0 + t * vstep + 512), pack8(s, 1), oa);
        }
      }
      const float inv = __builtin_amdgcn_rcpf(oa[8]);
#pragma unroll
      for (int i = 0; i < 8; ++i) oa[i] *= inv;
      st128(rs, voff, ofo + (tt * 32 + 2 * nt + kh) * 1024, pack8(oa, 0));
      STAMP(55);
      DRAIN();
      STAMP(56);
      BAR();
      if (w == 0) arrive(ws, CTR_D + L * 64 + tt, 1, 1, lane);
      if (L == 0) {
#pragma unroll
        for (int i = 0; i < 4; ++i) {
          LAS char* d = smem + L_WL + (4 * w + i) * 1024 + voff;
          *(LAS u32x4*)d = u32x4{wn0[4 * i], wn0[4 * i + 1], wn0[4 * i + 2], wn0[4 * i + 3]};
          *(LAS u32x4*)(d + 32768) = u32x4{wn1[4 * i], wn1[4 * i + 1], wn1[4 * i + 2], wn1[4 * i + 3]};
          *(LAS u32x4*)(d + 65536) = u32x4{wn2[4 * i], wn2[4 * i + 1], wn2[4 * i + 2], wn2[4 * i + 3]};
        }
      }
      if (w == 0) poll(ws, CTR_D + L * 64 + tt, 1, 4, lane);
      BAR();
      STAMP(13 + 16 * L);
    }
    {
      IDS();
      glds4<true>(ws, ofo + (tt * 32 + 4 * w) * 1024 + voff, lds0 + L_ACT + 4 * w * 1024);
      DRAIN();
      BAR();
      STAMP(14 + 16 * L);
      f32x16 acc = gemm_half(smem + L_ACT + kh * 16384 + voff, [&](int i) {
        const u32x16& v = i < 4 ? wo0 : (i < 8 ? wo1 : (i < 12 ? wo2 : wo3));
        return u32x4{v[4 * (i & 3)], v[4 * (i & 3) + 1], v[4 * (i & 3) + 2], v[4 * (i & 3) + 3]};
      }, [](int) {});
      put_hi(smem + 136 * KB + w * 2048 + voff, acc);
      BAR();
      add_lo(smem + 136 * KB + (w ^ 4) * 2048 + voff, acc);
      resid_epilogue(acc, xr, rs, xc, sc_, out, L == 1, tt, 2 * nt + kh, lane);
      STAMP(15 + 16 * L);
      if (L == 0) {
        DRAIN();
        BAR();
        if (w == 0) {
          arrive(ws, CTR_E + (tt >> 2), 1, 1, lane);
          poll(ws, CTR_E + panel, 1, 16, lane);
        }
        BAR();
        STAMP(16);
      }
    }
  }
  STAMP(40);
  STAMP_FLUSH();
}

extern "C" void kernel_launch(void* const* d_in, const int* in_sizes, int n_in, void* d_out, int out_size, void* d_ws,
                              size_t ws_size, hipStream_t stream) {
  char* ws = (char*)d_ws;
  (void)hipFuncSetAttribute((const void*)k_mega, hipFuncAttributeMaxDynamicSharedMemorySize, 160 * KB);
  k_prep<<<PREP_BLOCKS, 512, 0, stream>>>((const uint16_t*)d_in[0], (const uint16_t*)d_in[1], (const uint16_t*)d_in[2], (const uint16_t*)d_in[4],
                                          (const uint16_t*)d_in[5], (const uint16_t*)d_in[6], (const uint16_t*)d_in[7], ws);
  k_mega<<<256, 512, 160 * KB, stream>>>(ws, (uint16_t*)d_out);
}
```

```cpp
#include <hip/hip_runtime.h>
#include <hip/hip_bf16.h>
#include <stdint.h>

typedef __attribute__((ext_vector_type(8))) short bf16x8;
typedef __attribute__((ext_vector_type(4))) short s16x4;
typedef __attribute__((ext_vector_type(16))) float f32x16;
typedef __attribute__((ext_vector_type(4))) float f32x4;
typedef __attribute__((ext_vector_type(2))) float f32x2;
typedef __attribute__((ext_vector_type(16))) unsigned u32x16;
typedef __attribute__((ext_vector_type(4))) unsigned u32x4;
typedef __attribute__((ext_vector_type(2))) unsigned u32x2;
typedef __attribute__((ext_vector_type(2))) __bf16 bf16x2_t;
#define LAS __attribute__((address_space(3)))
#define GAS __attribute__((address_space(1)))
typedef GAS unsigned gu32;
#define DI __device__ __forceinline__
#define MFMA(a, b, c) __builtin_amdgcn_mfma_f32_32x32x16_bf16(__builtin_bit_cast(bf16x8, (a)), __builtin_bit_cast(bf16x8, (b)), (c), 0, 0, 0)
#define RLX_AGENT __ATOMIC_RELAXED, __HIP_MEMORY_SCOPE_AGENT

#define STAMP(slot) do {} while (0)
#define STAMP_DECL do {} while (0)
#define STAMP_FLUSH() do {} while (0)

constexpr int SEQ = 2048, DM = 512, NH = 32;
constexpr float LOG2E = 1.4426950408889634f;

constexpr int KB = 1024;
constexpr int OFF_CTR = 0;
constexpr int CTR_A = 0, CTR_C = 256, CTR_B = 512, CTR_D = 640, CTR_E = 768, CTR_TMO = 784, CTR_LINES = 800;
constexpr int OFF_ACC = OFF_CTR + CTR_LINES * 128;
constexpr int ACC_N = NH * 17 * 16;
constexpr int ZERO_BYTES = CTR_LINES * 128 + 2 * ACC_N * 4;
constexpr int OFF_W = 256 * KB;
constexpr int T_WOA = 48, T_WQ = 64, T_WKV = 80, T_WOB = 81;
constexpr int OFF_CS1 = OFF_W + 97 * 32 * KB;
constexpr int OFF_CS3 = OFF_CS1 + 16 * KB;
constexpr int OFF_XF = OFF_CS3 + 8 * KB;
constexpr int OFF_ST = OFF_XF + 4 * 2048 * KB;
constexpr int OFF_PQ = OFF_ST + 4 * 512 * KB;
constexpr int OFF_OF = OFF_PQ + 2 * 2048 * KB;
constexpr int OFF_KV = OFF_OF + 2 * 2048 * KB;
constexpr int WS_END = OFF_KV + 2 * 128 * KB;

constexpr int L_ACT = 0, L_WL = 32 * KB;
constexpr int L_CONST = 128 * KB, L_COMB = 136 * KB;

DI unsigned cvtpk(float lo, float hi) {
  f32x2 v = {lo, hi};
  return __builtin_bit_cast(unsigned, __builtin_convertvector(v, bf16x2_t));
}
DI float bflo(unsigned u) { return __uint_as_float(u << 16); }
DI float bfhi(unsigned u) { return __uint_as_float(u & 0xffff0000u); }
DI float bfround(float x) { return bflo(cvtpk(x, x)); }
DI u32x4 pack8(const f32x16& x, int s) {
  u32x4 p;
  p[0] = cvtpk(x[8 * s + 0], x[8 * s + 1]); p[1] = cvtpk(x[8 * s + 2], x[8 * s + 3]);
  p[2] = cvtpk(x[8 * s + 4], x[8 * s + 5]); p[3] = cvtpk(x[8 * s + 6], x[8 * s + 7]);
  return p;
}
DI float xhalf_sum(float v) {
  auto r = __builtin_amdgcn_permlane32_swap(__float_as_uint(v), __float_as_uint(v), false, false);
  return __uint_as_float(r[0]) + __uint_as_float(r[1]);
}
DI float xhalf_max(float v) {
  auto r = __builtin_amdgcn_permlane32_swap(__float_as_uint(v), __float_as_uint(v), false, false);
  return fmaxf(__uint_as_float(r[0]), __uint_as_float(r[1]));
}
DI f32x16 zero16() { f32x16 z; _Pragma("unroll") for (int i = 0; i < 16; ++i) z[i] = 0.f; return z; }
DI float phi(float y, float rs, float rsl) {
  return fmaxf(__builtin_fmaf(y, rs, 1e-6f), 1e-6f) + __builtin_amdgcn_fmed3f(__builtin_amdgcn_exp2f(y * rsl), 0.f, 1.f);
}

#define GLDS_HEAD "s_nop 4\n\ts_mov_b32 %0, m0\n\ts_mov_b32 m0, %3\n\ts_nop 0\n\t"
#define GLDS_OPS : "=&s"(sv) : "v"(voff), "s"(sbase), "s"(lds_base) : "memory"
template <bool SC1> DI void glds4(const char* sbase, unsigned voff, unsigned lds_base) {
  unsigned sv;
  lds_base = __builtin_amdgcn_readfirstlane(lds_base);
  if (SC1) asm volatile(GLDS_HEAD "global_load_lds_dwordx4 %1, %2 sc1\n\tglobal_load_lds_dwordx4 %1, %2 offset:1024 sc1\n\t"
               "global_load_lds_dwordx4 %1, %2 offset:2048 sc1\n\tglobal_load_lds_dwordx4 %1, %2 offset:3072 sc1\n\ts_mov_b32 m0, %0" GLDS_OPS);
  else asm volatile(GLDS_HEAD "global_load_lds_dwordx4 %1, %2\n\tglobal_load_lds_dwordx4 %1, %2 offset:1024\n\t"
               "global_load_lds_dwordx4 %1, %2 offset:2048\n\tglobal_load_lds_dwordx4 %1, %2 offset:3072\n\ts_mov_b32 m0, %0" GLDS_OPS);
}
DI void glds2(const char* sbase, unsigned voff, unsigned lds_base) {
  unsigned sv;
  lds_base = __builtin_amdgcn_readfirstlane(lds_base);
  asm volatile(GLDS_HEAD "global_load_lds_dwordx4 %1, %2\n\tglobal_load_lds_dwordx4 %1, %2 offset:1024\n\ts_mov_b32 m0, %0" GLDS_OPS);
}
DI void glds1s(const char* sbase, unsigned voff, unsigned lds_base) {
  unsigned sv;
  lds_base = __builtin_amdgcn_readfirstlane(lds_base);
  asm volatile(GLDS_HEAD "global_load_lds_dwordx4 %1, %2 sc1\n\ts_mov_b32 m0, %0" GLDS_OPS);
}
DI void glds1(const char* sbase, unsigned voff, unsigned lds_base) {
  unsigned sv;
  lds_base = __builtin_amdgcn_readfirstlane(lds_base);
  asm volatile(GLDS_HEAD "global_load_lds_dwordx4 %1, %2\n\ts_mov_b32 m0, %0" GLDS_OPS);
}
#define BAR() asm volatile("s_waitcnt lgkmcnt(0)\n\ts_barrier" ::: "memory")
#define DRAIN() asm volatile("s_waitcnt vmcnt(0)" ::: "memory")
#define PIN(x) asm volatile("" : "+v"(x))
#define SBAR() __builtin_amdgcn_sched_barrier(0)

typedef __amdgpu_buffer_rsrc_t rsrc_t;
DI u32x4 ld128(rsrc_t rs, int voff, int soff) { return __builtin_amdgcn_raw_buffer_load_b128(rs, voff, soff, 16); }
DI u32x2 ld64(rsrc_t rs, int voff, int soff) { return __builtin_amdgcn_raw_buffer_load_b64(rs, voff, soff, 16); }
DI u32x4 ldw(rsrc_t rs, int voff, int soff) { return __builtin_amdgcn_raw_buffer_load_b128(rs, voff, soff, 0); }
DI float ldwf(rsrc_t rs, int voff, int soff) { return __uint_as_float(__builtin_amdgcn_raw_buffer_load_b32(rs, voff, soff, 0)); }
DI void st128(rsrc_t rs, int voff, int soff, u32x4 v) { __builtin_amdgcn_raw_buffer_store_b128(v, rs, voff + soff, 0, 16); }
DI void st64(rsrc_t rs, int voff, int soff, u32x2 v) { __builtin_amdgcn_raw_buffer_store_b64(v, rs, voff, soff, 16); }

DI void poll(char* ws, int line0, int n, unsigned want, int lane) {
  gu32* c = (gu32*)(ws + OFF_CTR) + (line0 + (lane < n ? lane : 0)) * 32;
  gu32* tmo = (gu32*)(ws + OFF_CTR) + CTR_TMO * 32;
#pragma clang loop unroll(disable)
  for (unsigned spins = 0;;) {
    if (__all(__hip_atomic_load(c, RLX_AGENT) >= want)) break;
    __builtin_amdgcn_s_sleep(1);
    if (++spins > (1u << 17)) { if (lane == 0) __hip_atomic_store(tmo, 1u, RLX_AGENT); break; }
    if ((spins & 255u) == 0u && __builtin_amdgcn_readfirstlane(__hip_atomic_load(tmo, RLX_AGENT))) break;
  }
  __builtin_amdgcn_fence(__ATOMIC_ACQUIRE, "wavefront");
}
DI void arrive(char* ws, int line0, int stride, int n, int lane) {
  if (lane < n) __hip_atomic_fetch_add((gu32*)(ws + OFF_CTR) + (line0 + lane * stride) * 32, 1u, RLX_AGENT);
}

struct RowStat { float mu, rs; };
DI RowStat row_stats(const LAS char* st, int r, int h) {
  float s = 0.f, q = 0.f;
#pragma unroll
  for (int i = 0; i < 16; ++i) { const f32x2 p = *(const LAS f32x2*)(st + (h * 16 + i) * 256 + r * 8); s += p[0]; q += p[1]; }
  s = xhalf_sum(s); q = xhalf_sum(q);
  const float mean = s * (1.f / DM);
  const float var = fmaxf(q * (1.f / DM) - mean * mean, 0.f);
  const float sd = bfround(__builtin_amdgcn_sqrtf(bfround(bfround(var) + 1e-5f)));
  RowStat o; o.mu = bfround(mean); o.rs = __builtin_amdgcn_rcpf(sd); return o;
}
DI float dot2(unsigned a, unsigned b, float c) {
  return __builtin_amdgcn_fdot2_f32_bf16(__builtin_bit_cast(bf16x2_t, a), __builtin_bit_cast(bf16x2_t, b), c, false);
}
DI float sum8(const u32x4& v) { float c = 0.f; _Pragma("unroll") for (int j = 0; j < 4; ++j) c = dot2(v[j], 0x3f803f80u, c); return c; }
DI float sq8(const u32x4& v) { float c = 0.f; _Pragma("unroll") for (int j = 0; j < 4; ++j) c = dot2(v[j], v[j], c); return c; }
DI void put16(LAS char* p, const f32x16& a, int lane) {
#pragma unroll
  for (int g = 0; g < 4; ++g) *(LAS f32x4*)(p + g * 1024 + lane * 16) = f32x4{a[4 * g], a[4 * g + 1], a[4 * g + 2], a[4 * g + 3]};
}
DI void add16(const LAS char* p, f32x16& a, int lane) {
#pragma unroll
  for (int g = 0; g < 4; ++g) {
    const f32x4 v = *(const LAS f32x4*)(p + g * 1024 + lane * 16);
#pragma unroll
    for (int j = 0; j < 4; ++j) a[4 * g + j] += v[j];
  }
}
DI void put_hi(LAS char* p, const f32x16& a) {
  *(LAS f32x4*)p = f32x4{a[8], a[9], a[10], a[11]}; *(LAS f32x4*)(p + 1024) = f32x4{a[12], a[13], a[14], a[15]};
}
DI void add_lo(const LAS char* p, f32x16& a) {
  const f32x4 u = *(const LAS f32x4*)p, v = *(const LAS f32x4*)(p + 1024);
#pragma unroll
  for (int j = 0; j < 4; ++j) { a[j] += u[j]; a[4 + j] += v[j]; }
}
DI float max16(const f32x16& s) {
  float a = fmaxf(fmaxf(s[0], s[1]), s[2]), b = fmaxf(fmaxf(s[3], s[4]), s[5]);
  a = fmaxf(fmaxf(a, s[6]), s[7]); b = fmaxf(fmaxf(b, s[8]), s[9]);
  a = fmaxf(fmaxf(a, s[10]), s[11]); b = fmaxf(fmaxf(b, s[12]), s[13]);
  a = fmaxf(fmaxf(a, s[14]), s[15]);
  return fmaxf(a, b);
}

constexpr int PREP_BLOCKS = 322;
__global__ __launch_bounds__(512) void k_prep(const uint16_t* __restrict__ x0, const uint16_t* __restrict__ wqkv,
                                              const uint16_t* __restrict__ woa, const uint16_t* __restrict__ wq,
                                              const uint16_t* __restrict__ wk, const uint16_t* __restrict__ wv,
                                              const uint16_t* __restrict__ wob, char* __restrict__ ws) {
  __shared__ __attribute__((aligned(16))) char tile[256 * 80 + 8 * 32 * 4];
  const int b = blockIdx.x, tid = threadIdx.x, w = tid >> 6, lane = tid & 63, r = lane & 31, h = lane >> 5;
  if (b < 194) {
    const int nt = b >> 1, kb = b & 1, c = tid & 3;
    const uint16_t* src; int ld;
    if (nt < 48) { src = wqkv + nt * 32 + c * 8; ld = 1536; }
    else if (nt == T_WKV) { src = (c < 2 ? wk : wv) + (c & 1) * 8; ld = 16; }
    else { src = (nt < T_WQ ? woa + (nt - T_WOA) * 32 : (nt < T_WKV ? wq + (nt - T_WQ) * 32 : wob + (nt - T_WOB) * 32)) + c * 8; ld = 512; }
    u32x4 v[2];
#pragma unroll
    for (int i = 0; i < 2; ++i) v[i] = *(const u32x4*)(src + (size_t)(kb * 256 + i * 128 + (tid >> 2)) * ld);
#pragma unroll
    for (int i = 0; i < 2; ++i) *(u32x4*)(tile + (i * 128 + (tid >> 2)) * 80 + c * 16) = v[i];
    __syncthreads();
    float* red = (float*)(tile + 256 * 80);
    const int q = (lane & 15) >> 2, p = lane & 3, g1 = (lane >> 4) & 1;
    float s = 0.f;
#pragma unroll
    for (int i = 0; i < 2; ++i) {
      const LAS char* a = (const LAS char*)tile + ((2 * w + i) * 16 + 4 * h + q) * 80 + (16 * g1 + 4 * p) * 2;
      const s16x4 lo = __builtin_amdgcn_ds_read_tr16_b64_v4i16((LAS s16x4*)a);
      const s16x4 hi = __builtin_amdgcn_ds_read_tr16_b64_v4i16((LAS s16x4*)(a + 8 * 80));
      const u32x2 l2 = __builtin_bit_cast(u32x2, lo), h2 = __builtin_bit_cast(u32x2, hi);
      const u32x4 f = {l2[0], l2[1], h2[0], h2[1]};
      *(u32x4*)(ws + OFF_W + (nt * 32 + kb * 16 + 2 * w + i) * 1024 + lane * 16) = f;
#pragma unroll
      for (int j = 0; j < 4; ++j) s += bflo(f[j]) + bfhi(f[j]);
    }
    s = xhalf_sum(s);
    if (h == 0) red[w * 32 + r] = s;
    __syncthreads();
    if (tid < 32) {
      float t = 0.f;
#pragma unroll
      for (int i = 0; i < 8; ++i) t += red[i * 32 + tid];
      if (nt < 48) ((float*)(ws + OFF_CS1))[(nt & 15) * 256 + kb * 128 + (nt >> 4) * 32 + tid] = t;
      else if (nt >= T_WQ && nt < T_WKV) ((float*)(ws + OFF_CS3))[((nt - T_WQ) >> 2) * 512 + kb * 256 + ((nt - T_WQ) & 3) * 32 + tid] = t;
      else if (nt == T_WKV) {
#pragma unroll
        for (int i = 0; i < 4; ++i) ((float*)(ws + OFF_CS3))[i * 512 + kb * 256 + 128 + tid] = t;
      }
    }
  } else {
    const int tt = (b - 194) >> 1, p = ((b - 194) & 1) * 8 + w;
    const uint16_t* row = x0 + (size_t)(tt * 32 + r) * DM + p * 32 + 4 * h;
    u32x2 e[4];
#pragma unroll
    for (int i = 0; i < 4; ++i) e[i] = *(const u32x2*)(row + 8 * i);
#pragma unroll
    for (int i = 0; i < 2; ++i) {
      const u32x4 v = {e[2 * i][0], e[2 * i][1], e[2 * i + 1][0], e[2 * i + 1][1]};
      float s = 0.f, q = 0.f;
#pragma unroll
      for (int j = 0; j < 4; ++j) { const float a = bflo(v[j]), c = bfhi(v[j]); s += a + c; q += a * a + c * c; }
      *(u32x4*)(ws + OFF_XF + ((tt * 32 + 2 * p + i) * 64 + lane) * 16) = v;
      s = xhalf_sum(s); q = xhalf_sum(q);
      if (h == 0) ((f32x2*)(ws + OFF_ST))[(tt * 32 + 2 * p + i) * 32 + r] = f32x2{s, q};
    }
  }
  const int z = (PREP_BLOCKS - 1 - b) * 512 + tid;
  if (z < ZERO_BYTES / 16) ((u32x4*)(ws + OFF_CTR))[z] = u32x4{0u, 0u, 0u, 0u};
}

#define ATTN_ASM \
  "s_nop 4\n\t" \
  "s_mov_b32 %[m0s], m0\n\t" \
  "s_mov_b32 m0, %[m0a]\n\t" \
  "s_nop 0\n\t" \
  "global_load_lds_dwordx4 v95, %[wsb] sc1\n\t" \
  "global_load_lds_dwordx4 v95, %[wsb] offset:1024 sc1\n\t" \
  "s_add_u32 m0, m0, 0x2000\n\t" \
  "v_add_u32 v95, 0x2000, v95\n\t" \
  "global_load_lds_dwordx4 v95, %[wsb] sc1\n\t" \
  "global_load_lds_dwordx4 v95, %[wsb] offset:1024 sc1\n\t" \
  "s_add_u32 m0, m0, 0x2000\n\t" \
  "v_add_u32 v95, 0x2000, v95\n\t" \
  "global_load_lds_dwordx4 v95, %[wsb] sc1\n\t" \
  "global_load_lds_dwordx4 v95, %[wsb] offset:1024 sc1\n\t" \
  "s_add_u32 m0, m0, 0x2000\n\t" \
  "v_add_u32 v95, 0x2000, v95\n\t" \
  "s_waitcnt vmcnt(4)\n\t" \
  "s_barrier\n\t" \
  "ds_read_b128 v[64:67], v92 offset:0\n\t" \
  "ds_read_b128 v[68:71], v93 offset:0\n\t" \
  "ds_read_b128 v[72:75], v93 offset:0\n\t" \
  "s_mov_b32 %[n], 0\n\t" \
  "s_waitcnt lgkmcnt(0)\n\t" \
  "v_mfma_f32_32x32x16_bf16 v[32:47], v[64:67], v[84:87], 0\n\t" \
  "s_getpc_b64 s[92:93]\n\t" \
  "Lpc%=:\n\t" \
  "s_add_u32 s90, s92, Lb0%=-Lpc%=\n\t" \
  "s_addc_u32 s91, s93, 0\n\t" \
  "s_cmp_eq_u32 %[kh], 0\n\t" \
  "s_cselect_b32 s94, 0, Lb1%=-Lb0%=\n\t" \
  "s_add_u32 s90, s90, s94\n\t" \
  "s_addc_u32 s91, s91, 0\n\t" \
  "s_add_u32 s92, s92, Ltab%=-Lpc%=\n\t" \
  "s_addc_u32 s93, s93, 0\n\t" \
  "s_setprio 1\n\t" \
  "s_nop 5\n\t" \
  "s_setpc_b64 s[90:91]\n\t" \
  "Lb0%=:\n\t" \
  "s_setprio 2\n\t" \
  "v_exp_f32 v32, v32\n\t" \
  "v_exp_f32 v33, v33\n\t" \
  "s_waitcnt lgkmcnt(2)\n\t" \
  "v_exp_f32 v34, v34\n\t" \
  "v_mfma_f32_32x32x16_bf16 v[16:31], v[72:75], v[80:83], v[16:31]\n\t" \
  "ds_read_b128 v[72:75], v93 offset:512\n\t" \
  "v_exp_f32 v35, v35\n\t" \
  "v_cvt_pk_bf16_f32 v76, v32, v33\n\t" \
  "v_exp_f32 v36, v36\n\t" \
  "v_exp_f32 v37, v37\n\t" \
  "v_mfma_f32_32x32x16_bf16 v[48:63], v[64:67], v[88:91], 0\n\t" \
  "ds_read_b128 v[64:67], v92 offset:2048\n\t" \
  "v_cvt_pk_bf16_f32 v77, v34, v35\n\t" \
  "v_exp_f32 v38, v38\n\t" \
  "v_exp_f32 v39, v39\n\t" \
  "v_cvt_pk_bf16_f32 v78, v36, v37\n\t" \
  "v_exp_f32 v40, v40\n\t" \
  "v_exp_f32 v41, v41\n\t" \
  "v_cvt_pk_bf16_f32 v79, v38, v39\n\t" \
  "v_exp_f32 v42, v42\n\t" \
  "v_exp_f32 v43, v43\n\t" \
  "s_waitcnt lgkmcnt(2)\n\t" \
  "v_exp_f32 v44, v44\n\t" \
  "v_mfma_f32_32x32x16_bf16 v[0:15], v[68:71], v[76:79], v[0:15]\n\t" \
  "v_cvt_pk_bf16_f32 v80, v40, v41\n\t" \
  "v_exp_f32 v45, v45\n\t" \
  "v_cvt_pk_bf16_f32 v81, v42, v43\n\t" \
  "v_exp_f32 v46, v46\n\t" \
  "v_exp_f32 v47, v47\n\t" \
  "v_cvt_pk_bf16_f32 v82, v44, v45\n\t" \
  "v_cvt_pk_bf16_f32 v83, v46, v47\n\t" \
  "v_exp_f32 v48, v48\n\t" \
  "v_exp_f32 v49, v49\n\t" \
  "s_waitcnt lgkmcnt(1)\n\t" \
  "v_exp_f32 v50, v50\n\t" \
  "v_mfma_f32_32x32x16_bf16 v[0:15], v[72:75], v[80:83], v[0:15]\n\t" \
  "v_exp_f32 v51, v51\n\t" \
  "v_cvt_pk_bf16_f32 v76, v48, v49\n\t" \
  "v_exp_f32 v52, v52\n\t" \
  "v_exp_f32 v53, v53\n\t" \
  "s_waitcnt lgkmcnt(0)\n\t" \
  "v_exp_f32 v54, v54\n\t" \
  "v_mfma_f32_32x32x16_bf16 v[32:47], v[64:67], v[84:87], 0\n\t" \
  "v_cvt_pk_bf16_f32 v77, v50, v51\n\t" \
  "v_exp_f32 v55, v55\n\t" \
  "v_cvt_pk_bf16_f32 v78, v52, v53\n\t" \
  "v_exp_f32 v56, v56\n\t" \
  "v_exp_f32 v57, v57\n\t" \
  "v_cvt_pk_bf16_f32 v79, v54, v55\n\t" \
  "v_exp_f32 v58, v58\n\t" \
  "v_exp_f32 v59, v59\n\t" \
  "v_mfma_f32_32x32x16_bf16 v[16:31], v[68:71], v[76:79], v[16:31]\n\t" \
  "ds_read_b128 v[68:71], v93 offset:2048\n\t" \
  "v_cvt_pk_bf16_f32 v80, v56, v57\n\t" \
  "v_exp_f32 v60, v60\n\t" \
  "v_exp_f32 v61, v61\n\t" \
  "v_cvt_pk_bf16_f32 v81, v58, v59\n\t" \
  "v_exp_f32 v62, v62\n\t" \
  "v_exp_f32 v63, v63\n\t" \
  "v_cvt_pk_bf16_f32 v82, v60, v61\n\t" \
  "v_cvt_pk_bf16_f32 v83, v62, v63\n\t" \
  "v_exp_f32 v32, v32\n\t" \
  "v_exp_f32 v33, v33\n\t" \
  "s_waitcnt lgkmcnt(2)\n\t" \
  "v_exp_f32 v34, v34\n\t" \
  "v_mfma_f32_32x32x16_bf16 v[16:31], v[72:75], v[80:83], v[16:31]\n\t" \
  "ds_read_b128 v[72:75], v93 offset:2560\n\t" \
  "v_exp_f32 v35, v35\n\t" \
  "v_cvt_pk_bf16_f32 v76, v32, v33\n\t" \
  "v_exp_f32 v36, v36\n\t" \
  "v_exp_f32 v37, v37\n\t" \
  "v_mfma_f32_32x32x16_bf16 v[48:63], v[64:67], v[88:91], 0\n\t" \
  "ds_read_b128 v[64:67], v92 offset:4096\n\t" \
  "v_cvt_pk_bf16_f32 v77, v34, v35\n\t" \
  "v_exp_f32 v38, v38\n\t" \
  "v_exp_f32 v39, v39\n\t" \
  "v_cvt_pk_bf16_f32 v78, v36, v37\n\t" \
  "v_exp_f32 v40, v40\n\t" \
  "v_exp_f32 v41, v41\n\t" \
  "v_cvt_pk_bf16_f32 v79, v38, v39\n\t" \
  "v_exp_f32 v42, v42\n\t" \
  "v_exp_f32 v43, v43\n\t" \
  "s_waitcnt lgkmcnt(2)\n\t" \
  "v_exp_f32 v44, v44\n\t" \
  "v_mfma_f32_32x32x16_bf16 v[0:15], v[68:71], v[76:79], v[0:15]\n\t" \
  "v_cvt_pk_bf16_f32 v80, v40, v41\n\t" \
  "v_exp_f32 v45, v45\n\t" \
  "v_cvt_pk_bf16_f32 v81, v42, v43\n\t" \
  "v_exp_f32 v46, v46\n\t" \
  "v_exp_f32 v47, v47\n\t" \
  "v_cvt_pk_bf16_f32 v82, v44, v45\n\t" \
  "v_cvt_pk_bf16_f32 v83, v46, v47\n\t" \
  "s_setprio 0\n\t" \
  "v_exp_f32 v48, v48\n\t" \
  "v_exp_f32 v49, v49\n\t" \
  "s_waitcnt lgkmcnt(1)\n\t" \
  "v_exp_f32 v50, v50\n\t" \
  "v_mfma_f32_32x32x16_bf16 v[0:15], v[72:75], v[80:83], v[0:15]\n\t" \
  "v_exp_f32 v51, v51\n\t" \
  "v_cvt_pk_bf16_f32 v76, v48, v49\n\t" \
  "v_exp_f32 v52, v52\n\t" \
  "v_exp_f32 v53, v53\n\t" \
  "s_waitcnt lgkmcnt(0)\n\t" \
  "v_exp_f32 v54, v54\n\t" \
  "v_mfma_f32_32x32x16_bf16 v[32:47], v[64:67], v[84:87], 0\n\t" \
  "v_cvt_pk_bf16_f32 v77, v50, v51\n\t" \
  "v_exp_f32 v55, v55\n\t" \
  "v_cvt_pk_bf16_f32 v78, v52, v53\n\t" \
  "v_exp_f32 v56, v56\n\t" \
  "v_exp_f32 v57, v57\n\t" \
  "v_cvt_pk_bf16_f32 v79, v54, v55\n\t" \
  "v_exp_f32 v58, v58\n\t" \
  "v_exp_f32 v59, v59\n\t" \
  "v_mfma_f32_32x32x16_bf16 v[16:31], v[68:71], v[76:79], v[16:31]\n\t" \
  "ds_read_b128 v[68:71], v93 offset:4096\n\t" \
  "v_cvt_pk_bf16_f32 v80, v56, v57\n\t" \
  "v_exp_f32 v60, v60\n\t" \
  "v_exp_f32 v61, v61\n\t" \
  "v_cvt_pk_bf16_f32 v81, v58, v59\n\t" \
  "v_exp_f32 v62, v62\n\t" \
  "v_exp_f32 v63, v63\n\t" \
  "v_cvt_pk_bf16_f32 v82, v60, v61\n\t" \
  "v_cvt_pk_bf16_f32 v83, v62, v63\n\t" \
  "s_add_u32 %[n], %[n], 1\n\t" \
  "v_add_u32 v92, 0x1000, v92\n\t" \
  "s_lshl_b32 s94, %[n], 6\n\t" \
  "v_add_u32 v93, v94, v93\n\t" \
  "s_add_u32 s94, s92, s94\n\t" \
  "s_addc_u32 s95, s93, 0\n\t" \
  "s_setpc_b64 s[94:95]\n\t" \
  "Lb1%=:\n\t" \
  "v_exp_f32 v32, v32\n\t" \
  "v_exp_f32 v33, v33\n\t" \
  "s_waitcnt lgkmcnt(2)\n\t" \
  "v_exp_f32 v34, v34\n\t" \
  "v_mfma_f32_32x32x16_bf16 v[16:31], v[72:75], v[80:83], v[16:31]\n\t" \
  "ds_read_b128 v[72:75], v93 offset:512\n\t" \
  "v_exp_f32 v35, v35\n\t" \
  "v_cvt_pk_bf16_f32 v76, v32, v33\n\t" \
  "v_exp_f32 v36, v36\n\t" \
  "v_exp_f32 v37, v37\n\t" \
  "v_mfma_f32_32x32x16_bf16 v[48:63], v[64:67], v[88:91], 0\n\t" \
  "ds_read_b128 v[64:67], v92 offset:2048\n\t" \
  "v_cvt_pk_bf16_f32 v77, v34, v35\n\t" \
  "v_exp_f32 v38, v38\n\t" \
  "v_exp_f32 v39, v39\n\t" \
  "v_cvt_pk_bf16_f32 v78, v36, v37\n\t" \
  "v_exp_f32 v40, v40\n\t" \
  "v_exp_f32 v41, v41\n\t" \
  "v_cvt_pk_bf16_f32 v79, v38, v39\n\t" \
  "v_exp_f32 v42, v42\n\t" \
  "v_exp_f32 v43, v43\n\t" \
  "s_waitcnt lgkmcnt(2)\n\t" \
  "v_exp_f32 v44, v44\n\t" \
  "v_mfma_f32_32x32x16_bf16 v[0:15], v[68:71], v[76:79], v[0:15]\n\t" \
  "v_cvt_pk_bf16_f32 v80, v40, v41\n\t" \
  "v_exp_f32 v45, v45\n\t" \
  "v_cvt_pk_bf16_f32 v81, v42, v43\n\t" \
  "v_exp_f32 v46, v46\n\t" \
  "v_exp_f32 v47, v47\n\t" \
  "v_cvt_pk_bf16_f32 v82, v44, v45\n\t" \
  "v_cvt_pk_bf16_f32 v83, v46, v47\n\t" \
  "v_exp_f32 v48, v48\n\t" \
  "v_exp_f32 v49, v49\n\t" \
  "s_waitcnt lgkmcnt(1)\n\t" \
  "v_exp_f32 v50, v50\n\t" \
  "v_mfma_f32_32x32x16_bf16 v[0:15], v[72:75], v[80:83], v[0:15]\n\t" \
  "v_exp_f32 v51, v51\n\t" \
  "v_cvt_pk_bf16_f32 v76, v48, v49\n\t" \
  "v_exp_f32 v52, v52\n\t" \
  "v_exp_f32 v53, v53\n\t" \
  "s_waitcnt lgkmcnt(0)\n\t" \
  "v_exp_f32 v54, v54\n\t" \
  "v_mfma_f32_32x32x16_bf16 v[32:47], v[64:67], v[84:87], 0\n\t" \
  "v_cvt_pk_bf16_f32 v77, v50, v51\n\t" \
  "v_exp_f32 v55, v55\n\t" \
  "v_cvt_pk_bf16_f32 v78, v52, v53\n\t" \
  "v_exp_f32 v56, v56\n\t" \
  "v_exp_f32 v57, v57\n\t" \
  "v_cvt_pk_bf16_f32 v79, v54, v55\n\t" \
  "v_exp_f32 v58, v58\n\t" \
  "v_exp_f32 v59, v59\n\t" \
  "v_mfma_f32_32x32x16_bf16 v[16:31], v[68:71], v[76:79], v[16:31]\n\t" \
  "ds_read_b128 v[68:71], v93 offset:2048\n\t" \
  "v_cvt_pk_bf16_f32 v80, v56, v57\n\t" \
  "v_exp_f32 v60, v60\n\t" \
  "v_exp_f32 v61, v61\n\t" \
  "v_cvt_pk_bf16_f32 v81, v58, v59\n\t" \
  "v_exp_f32 v62, v62\n\t" \
  "v_exp_f32 v63, v63\n\t" \
  "v_cvt_pk_bf16_f32 v82, v60, v61\n\t" \
  "v_cvt_pk_bf16_f32 v83, v62, v63\n\t" \
  "v_exp_f32 v32, v32\n\t" \
  "v_exp_f32 v33, v33\n\t" \
  "s_waitcnt lgkmcnt(2)\n\t" \
  "v_exp_f32 v34, v34\n\t" \
  "v_mfma_f32_32x32x16_bf16 v[16:31], v[72:75], v[80:83], v[16:31]\n\t" \
  "ds_read_b128 v[72:75], v93 offset:2560\n\t" \
  "v_exp_f32 v35, v35\n\t" \
  "v_cvt_pk_bf16_f32 v76, v32, v33\n\t" \
  "v_exp_f32 v36, v36\n\t" \
  "v_exp_f32 v37, v37\n\t" \
  "v_mfma_f32_32x32x16_bf16 v[48:63], v[64:67], v[88:91], 0\n\t" \
  "ds_read_b128 v[64:67], v92 offset:4096\n\t" \
  "v_cvt_pk_bf16_f32 v77, v34, v35\n\t" \
  "v_exp_f32 v38, v38\n\t" \
  "v_exp_f32 v39, v39\n\t" \
  "v_cvt_pk_bf16_f32 v78, v36, v37\n\t" \
  "v_exp_f32 v40, v40\n\t" \
  "v_exp_f32 v41, v41\n\t" \
  "v_cvt_pk_bf16_f32 v79, v38, v39\n\t" \
  "v_exp_f32 v42, v42\n\t" \
  "v_exp_f32 v43, v43\n\t" \
  "s_waitcnt lgkmcnt(2)\n\t" \
  "v_exp_f32 v44, v44\n\t" \
  "v_mfma_f32_32x32x16_bf16 v[0:15], v[68:71], v[76:79], v[0:15]\n\t" \
  "v_cvt_pk_bf16_f32 v80, v40, v41\n\t" \
  "v_exp_f32 v45, v45\n\t" \
  "v_cvt_pk_bf16_f32 v81, v42, v43\n\t" \
  "v_exp_f32 v46, v46\n\t" \
  "v_exp_f32 v47, v47\n\t" \
  "v_cvt_pk_bf16_f32 v82, v44, v45\n\t" \
  "v_cvt_pk_bf16_f32 v83, v46, v47\n\t" \
  "v_exp_f32 v48, v48\n\t" \
  "v_exp_f32 v49, v49\n\t" \
  "s_waitcnt lgkmcnt(1)\n\t" \
  "v_exp_f32 v50, v50\n\t" \
  "v_mfma_f32_32x32x16_bf16 v[0:15], v[72:75], v[80:83], v[0:15]\n\t" \
  "v_exp_f32 v51, v51\n\t" \
  "v_cvt_pk_bf16_f32 v76, v48, v49\n\t" \
  "v_exp_f32 v52, v52\n\t" \
  "v_exp_f32 v53, v53\n\t" \
  "s_waitcnt lgkmcnt(0)\n\t" \
  "v_exp_f32 v54, v54\n\t" \
  "v_mfma_f32_32x32x16_bf16 v[32:47], v[64:67], v[84:87], 0\n\t" \
  "v_cvt_pk_bf16_f32 v77, v50, v51\n\t" \
  "v_exp_f32 v55, v55\n\t" \
  "v_cvt_pk_bf16_f32 v78, v52, v53\n\t" \
  "v_exp_f32 v56, v56\n\t" \
  "v_exp_f32 v57, v57\n\t" \
  "v_cvt_pk_bf16_f32 v79, v54, v55\n\t" \
  "v_exp_f32 v58, v58\n\t" \
  "v_exp_f32 v59, v59\n\t" \
  "v_mfma_f32_32x32x16_bf16 v[16:31], v[68:71], v[76:79], v[16:31]\n\t" \
  "ds_read_b128 v[68:71], v93 offset:4096\n\t" \
  "v_cvt_pk_bf16_f32 v80, v56, v57\n\t" \
  "v_exp_f32 v60, v60\n\t" \
  "v_exp_f32 v61, v61\n\t" \
  "v_cvt_pk_bf16_f32 v81, v58, v59\n\t" \
  "v_exp_f32 v62, v62\n\t" \
  "v_exp_f32 v63, v63\n\t" \
  "v_cvt_pk_bf16_f32 v82, v60, v61\n\t" \
  "v_cvt_pk_bf16_f32 v83, v62, v63\n\t" \
  "s_add_u32 %[n], %[n], 1\n\t" \
  "v_add_u32 v92, 0x1000, v92\n\t" \
  "s_lshl_b32 s94, %[n], 6\n\t" \
  "v_add_u32 v93, v94, v93\n\t" \
  "s_add_u32 s94, s92, s94\n\t" \
  "s_addc_u32 s95, s93, 0\n\t" \
  "s_setpc_b64 s[94:95]\n\t" \
  ".p2align 6\n\t" \
  "Ltab%=:\n\t" \
  "s_nop 0\n\t" \
  ".p2align 6\n\t" \
  "s_waitcnt vmcnt(2)\n\t" \
  "s_barrier\n\t" \
  "global_load_lds_dwordx4 v95, %[wsb] sc1\n\t" \
  "global_load_lds_dwordx4 v95, %[wsb] offset:1024 sc1\n\t" \
  "s_add_u32 m0, m0, 0x2000\n\t" \
  "v_add_u32 v95, 0x2000, v95\n\t" \
  "s_setpc_b64 s[90:91]\n\t" \
  ".p2align 6\n\t" \
  "global_load_lds_dwordx4 v95, %[wsb] sc1\n\t" \
  "global_load_lds_dwordx4 v95, %[wsb] offset:1024 sc1\n\t" \
  "s_add_u32 m0, m0, 0x2000\n\t" \
  "v_add_u32 v95, 0x2000, v95\n\t" \
  "s_setpc_b64 s[90:91]\n\t" \
  ".p2align 6\n\t" \
  "s_waitcnt vmcnt(2)\n\t" \
  "s_barrier\n\t" \
  "global_load_lds_dwordx4 v95, %[wsb] sc1\n\t" \
  "global_load_lds_dwordx4 v95, %[wsb] offset:1024 sc1\n\t" \
  "s_add_u32 m0, m0, 0x2000\n\t" \
  "v_add_u32 v95, 0x2000, v95\n\t" \
  "s_setpc_b64 s[90:91]\n\t" \
  ".p2align 6\n\t" \
  "global_load_lds_dwordx4 v95, %[wsb] sc1\n\t" \
  "global_load_lds_dwordx4 v95, %[wsb] offset:1024 sc1\n\t" \
  "s_add_u32 m0, m0, 0x2000\n\t" \
  "v_add_u32 v95, 0x2000, v95\n\t" \
  "s_setpc_b64 s[90:91]\n\t" \
  ".p2align 6\n\t" \
  "global_load_lds_dwordx4 v95, %[wsb] sc1\n\t" \
  "global_load_lds_dwordx4 v95, %[wsb] offset:1024 sc1\n\t" \
  "s_add_u32 m0, m0, 0x2000\n\t" \
  "v_add_u32 v95, 0x2000, v95\n\t" \
  "s_setpc_b64 s[90:91]\n\t" \
  ".p2align 6\n\t" \
  "s_setpc_b64 s[90:91]\n\t" \
  ".p2align 6\n\t" \
  "s_waitcnt vmcnt(0)\n\t" \
  "s_barrier\n\t" \
  "s_mov_b32 m0, %[m0s]\n\t" \
  "global_load_dwordx4 v[128:131], v96, %[wsb]\n\t" \
  "global_load_dwordx4 v[132:135], v96, %[wsb] offset:1024\n\t" \
  "v_add_u32 v96, 0x800, v96\n\t" \
  "global_load_dwordx4 v[192:195], v97, %[wsb] offset:0\n\t" \
  "global_load_dwordx4 v[196:199], v97, %[wsb] offset:1024\n\t" \
  "s_setpc_b64 s[90:91]\n\t" \
  ".p2align 6\n\t" \
  "global_load_dwordx4 v[136:139], v96, %[wsb]\n\t" \
  "global_load_dwordx4 v[140:143], v96, %[wsb] offset:1024\n\t" \
  "v_add_u32 v96, 0x800, v96\n\t" \
  "global_load_dwordx4 v[200:203], v97, %[wsb] offset:2048\n\t" \
  "global_load_dwordx4 v[204:207], v97, %[wsb] offset:3072\n\t" \
  "v_add_u32 v97, 0x80000, v97\n\t" \
  "s_setpc_b64 s[90:91]\n\t" \
  ".p2align 6\n\t" \
  "global_load_dwordx4 v[144:147], v96, %[wsb]\n\t" \
  "global_load_dwordx4 v[148:151], v96, %[wsb] offset:1024\n\t" \
  "v_add_u32 v96, 0x800, v96\n\t" \
  "global_load_dwordx4 v[208:211], v97, %[wsb] offset:0\n\t" \
  "global_load_dwordx4 v[212:215], v97, %[wsb] offset:1024\n\t" \
  "s_setpc_b64 s[90:91]\n\t" \
  ".p2align 6\n\t" \
  "global_load_dwordx4 v[152:155], v96, %[wsb]\n\t" \
  "global_load_dwordx4 v[156:159], v96, %[wsb] offset:1024\n\t" \
  "v_add_u32 v96, 0x800, v96\n\t" \
  "global_load_dwordx4 v[216:219], v97, %[wsb] offset:2048\n\t" \
  "global_load_dwordx4 v[220:223], v97, %[wsb] offset:3072\n\t" \
  "v_add_u32 v97, 0x80000, v97\n\t" \
  "s_setpc_b64 s[90:91]\n\t" \
  ".p2align 6\n\t" \
  "global_load_dwordx4 v[160:163], v96, %[wsb]\n\t" \
  "global_load_dwordx4 v[164:167], v96, %[wsb] offset:1024\n\t" \
  "v_add_u32 v96, 0x800, v96\n\t" \
  "global_load_dwordx4 v[224:227], v97, %[wsb] offset:0\n\t" \
  "global_load_dwordx4 v[228:231], v97, %[wsb] offset:1024\n\t" \
  "s_setpc_b64 s[90:91]\n\t" \
  ".p2align 6\n\t" \
  "global_load_dwordx4 v[168:171], v96, %[wsb]\n\t" \
  "global_load_dwordx4 v[172:175], v96, %[wsb] offset:1024\n\t" \
  "v_add_u32 v96, 0x800, v96\n\t" \
  "global_load_dwordx4 v[232:235], v97, %[wsb] offset:2048\n\t" \
  "global_load_dwordx4 v[236:239], v97, %[wsb] offset:3072\n\t" \
  "v_add_u32 v97, 0x80000, v97\n\t" \
  "s_setpc_b64 s[90:91]\n\t" \
  ".p2align 6\n\t" \
  "global_load_dwordx4 v[176:179], v96, %[wsb]\n\t" \
  "global_load_dwordx4 v[180:183], v96, %[wsb] offset:1024\n\t" \
  "v_add_u32 v96, 0x800, v96\n\t" \
  "s_setpc_b64 s[90:91]\n\t" \
  ".p2align 6\n\t" \
  "global_load_dwordx4 v[184:187], v96, %[wsb]\n\t" \
  "global_load_dwordx4 v[188:191], v96, %[wsb] offset:1024\n\t" \
  "v_add_u32 v96, 0x800, v96\n\t" \
  "s_setpc_b64 s[90:91]\n\t" \
  ".p2align 6\n\t" \
  "s_setpc_b64 s[90:91]\n\t" \
  ".p2align 6\n\t" \
  "s_waitcnt lgkmcnt(0)\n\t" \
  "v_mfma_f32_32x32x16_bf16 v[16:31], v[72:75], v[80:83], v[16:31]\n\t" \
  "s_setprio 0\n\t" \
  "s_waitcnt vmcnt(0)\n\t" \
  "s_nop 7\n\t" \
  "s_nop 7\n\t" \
  ""

template <class W, class H> DI f32x16 gemm_half(const LAS char* act, W wf, H hook) {
  f32x16 acc = zero16();
  u32x4 a[16];
#pragma unroll
  for (int i = 0; i < 4; ++i) a[i] = *(const LAS u32x4*)(act + i * 1024);
#pragma unroll
  for (int i = 0; i < 16; ++i) {
    SBAR();
    if (i + 4 < 16) a[i + 4] = *(const LAS u32x4*)(act + (i + 4) * 1024);
    hook(i);
    acc = MFMA(wf(i), a[i], acc);
  }
  SBAR();
  return acc;
}
DI f32x4 lds2(const LAS char* p, int half) { return *(const LAS f32x4*)p + *(const LAS f32x4*)(p + half); }
DI void resid_epilogue(const f32x16& acc, u32x4& xr, rsrc_t rs, int xoff, int soff, uint16_t* out, bool final_, int tt, int ks, int lane) {
  const int r = lane & 31, h = lane >> 5;
  const u32x4 y = pack8(acc, 0);
#pragma unroll
  for (int j = 0; j < 4; ++j) xr[j] = cvtpk(bflo(xr[j]) + bflo(y[j]), bfhi(xr[j]) + bfhi(y[j]));
  if (final_) {
    auto a = __builtin_amdgcn_permlane32_swap(xr[0], xr[2], false, false);
    auto c = __builtin_amdgcn_permlane32_swap(xr[1], xr[3], false, false);
    *(u32x4*)(out + (size_t)(tt * 32 + r) * DM + ks * 16 + 8 * h) = u32x4{a[0], c[0], a[1], c[1]};
  } else {
    st128(rs, lane * 16, xoff + (tt * 32 + ks) * 1024, xr);
    const float s = xhalf_sum(sum8(xr)), q = xhalf_sum(sq8(xr));
    if (h == 0) st64(rs, r * 8, soff + (tt * 32 + ks) * 256, u32x2{__float_as_uint(s), __float_as_uint(q)});
  }
}

#define KV_PUB 8
#define NEARLY 8
__global__ __launch_bounds__(512, 2) void k_mega(char* ws, uint16_t* out) {
  extern __shared__ __attribute__((aligned(16))) char smem_[];
  LAS char* smem = (LAS char*)smem_;
  const unsigned lds0 = (unsigned)(uintptr_t)smem;
  const rsrc_t rs = __builtin_amdgcn_make_buffer_rsrc(ws, 0, WS_END, 0x00020000);
  const int w_ = __builtin_amdgcn_readfirstlane(threadIdx.x >> 6);
#define IDS() int tid = threadIdx.x, w = w_, b = blockIdx.x; asm volatile("" : "+v"(tid)); asm volatile("" : "+s"(w), "+s"(b)); \
  const int lane = tid & 63, r = lane & 31, h = lane >> 5, w4 = w & 3, kh = w >> 2, xcd = b & 7, idx = b >> 3; \
  const int tt = xcd * 8 + (idx >> 2), cb = idx & 3, nt = cb * 4 + w4;                 \
  const int panel = (xcd & 3) * 4 + (idx & 3), hp = (xcd >> 2) * 8 + (idx >> 2);       \
  const unsigned voff = lane * 16, vrot = voff ^ (kh << 8); \
  (void)r; (void)h; (void)tt; (void)nt; (void)panel; (void)hp; (void)voff; (void)vrot
  u32x4 xr;
  STAMP_DECL;
  {
    IDS();
    STAMP(0);
#pragma unroll
    for (int t = 0; t < 3; ++t) glds4<false>(ws, OFF_W + ((t * 16 + hp) * 32 + 4 * w) * 1024 + voff, lds0 + L_WL + (t * 32 + 4 * w) * 1024);
    xr = ld128(rs, voff, OFF_XF + (tt * 32 + 2 * nt + kh) * 1024);
  }

#pragma unroll 1
  for (int L = 0; L < 2; ++L) {
    const int xa = OFF_XF + (2 * L) * 2048 * KB, xb = xa + 2048 * KB, xc = xb + 2048 * KB;
    const int sa_ = OFF_ST + (2 * L) * 512 * KB, sb_ = sa_ + 512 * KB, sc_ = sb_ + 512 * KB;
    const int pqo = OFF_PQ + L * 2048 * KB, ofo = OFF_OF + L * 2048 * KB, kvo = OFF_KV + L * 128 * KB;
    const int acco = OFF_ACC + L * ACC_N * 4;
    u32x4 wp[16];
    {
      IDS();
      const int t1 = panel * 4 + w4;
      constexpr int WIN = 4;
      u32x4 xf[16];
#define XLD(i) xf[i] = ld128(rs, vrot + ((i) & 3) * 1024, xa + (t1 * 32 + kh * 16 + ((i) & ~3)) * 1024)
      XLD(0);
      SBAR();
      glds4<true>(ws, sa_ + t1 * 8192 + kh * 4096 + voff, lds0 + L_ACT + w4 * 8192 + kh * 4096);
      if (w == 4) glds1(ws, OFF_CS1 + hp * 1024 + voff, lds0 + 128 * KB);
#pragma unroll
      for (int i = 1; i < WIN; ++i) XLD(i);
      SBAR();
      PIN(xf[1]);
      BAR();
      STAMP(1 + 16 * L);
      f32x16 aq = zero16(), ak = zero16(), av = zero16();
      const LAS char* wl = smem + L_WL + kh * 16384;
      u32x4 wf[2][3];
#define WLD(i) do { wf[(i) & 1][0] = *(const LAS u32x4*)(wl + vrot + (i) * 1024); wf[(i) & 1][1] = *(const LAS u32x4*)(wl + voff + (32 + (i)) * 1024); \
                    wf[(i) & 1][2] = *(const LAS u32x4*)(wl + voff + (64 + (i)) * 1024); } while (0)
      WLD(0);
      RowStat st;
      LAS float* sst = (LAS float*)(smem + 129 * KB + w * 256);
#pragma unroll
      for (int i = 0; i < 16; ++i) {
        SBAR();
        if (2 * i + WIN < 16) { XLD(2 * i + WIN); XLD(2 * i + WIN + 1); }
        if (i >= 8) {
#pragma unroll
          for (int k = 2 * i - 16; k < 2 * i - 14; ++k) wp[k] = ldw(rs, vrot + (k & 3) * 1024, OFF_W + ((T_WOA + nt) * 32 + kh * 16 + (k & ~3)) * 1024);
        }
        if (i < 15) WLD(i + 1);
        aq = MFMA(wf[i & 1][0], xf[i], aq);
        ak = MFMA(xf[i], wf[i & 1][1], ak);
        av = MFMA(xf[i], wf[i & 1][2], av);
        if (i == 1) {
          st = row_stats(smem + L_ACT + w4 * 8192, r ^ (kh << 4), h);
          if (h == 0) { sst[r] = st.mu; sst[32 + r] = st.rs; }
        }
      }
      SBAR();
      STAMP(2 + 16 * L);
      LAS char* exw = smem + (kh ? L_WL : 136 * KB) + w4 * 6144;
      LAS char* exr = smem + (kh ? 136 * KB : L_WL) + w4 * 6144;
      const LAS char* csl = smem + 128 * KB;
      const float csk = *(const LAS float*)(csl + 128 + r * 4) + *(const LAS float*)(csl + 640 + r * 4);
      const float csv = *(const LAS float*)(csl + 256 + r * 4) + *(const LAS float*)(csl + 768 + r * 4);
      f32x4 cs[2], mu[2], rr[2];
#pragma unroll
      for (int g = 0; g < 2; ++g) {
        cs[g] = lds2(csl + kh * 64 + g * 32 + h * 16, 512);
        mu[g] = *(const LAS f32x4*)(sst + 8 * g + 4 * h); rr[g] = *(const LAS f32x4*)(sst + 32 + 8 * g + 4 * h);
      }
      if (kh == 1) BAR();
      put_hi(exw + vrot, aq); put_hi(exw + 2048 + voff, ak); put_hi(exw + 4096 + voff, av);
      if (kh == 0) BAR();
      BAR();
      STAMP(42);
      add_lo(exr + vrot, aq); add_lo(exr + 2048 + voff, ak); add_lo(exr + 4096 + voff, av);
      const float rsl = st.rs * LOG2E;
#pragma unroll
      for (int g = 0; g < 2; ++g) {
#pragma unroll
        for (int j = 0; j < 4; ++j) {
          ak[4 * g + j] = phi(ak[4 * g + j] - mu[g][j] * csk, rr[g][j], rr[g][j] * LOG2E);
          av[4 * g + j] = rr[g][j] * (av[4 * g + j] - mu[g][j] * csv);
        }
      }

      const u32x4 ones = {0x3f803f80u, 0x3f803f80u, 0x3f803f80u, 0x3f803f80u};
      const u32x4 pk = pack8(ak, 0);
      const f32x16 z = MFMA(pk, pack8(av, 0), zero16());
      const f32x16 zs = MFMA(pk, ones, zero16());
#pragma unroll
      for (int j = 0; j < 8; ++j) aq[j] = phi(aq[j] - st.mu * cs[j >> 2][j & 3], st.rs, rsl);
      st128(rs, vrot, pqo + (t1 * 32 + 2 * hp + kh) * 1024, pack8(aq, 0));
      {
        LAS float* red = (LAS float*)exr + h * 8 + (r & 15) * 16;
        if (r < 16) {
          *(LAS f32x4*)red = f32x4{z[0], z[1], z[2], z[3]}; *(LAS f32x4*)(red + 4) = f32x4{z[4], z[5], z[6], z[7]};
          if (r == 0) { *(LAS f32x4*)(red + 256) = f32x4{zs[0], zs[1], zs[2], zs[3]}; *(LAS f32x4*)(red + 260) = f32x4{zs[4], zs[5], zs[6], zs[7]}; }
          asm volatile("" ::: "memory");
        } else {
          *(LAS f32x4*)(red + 272) = f32x4{z[8], z[9], z[10], z[11]}; *(LAS f32x4*)(red + 276) = f32x4{z[12], z[13], z[14], z[15]};
          if (r == 16) { *(LAS f32x4*)(red + 528) = f32x4{zs[8], zs[9], zs[10], zs[11]}; *(LAS f32x4*)(red + 532) = f32x4{zs[12], zs[13], zs[14], zs[15]}; }
        }
      }
      STAMP(43);
      BAR();
#pragma unroll
      for (int i = 0; i < 2; ++i) {
        const int j = i * 512 + tid;
        if (j < 544) {
          const LAS float* ra = (const LAS float*)(smem + L_WL) + j;
          float t = 0.f;
#pragma unroll
          for (int k = 0; k < 4; ++k) t += ra[k * 1536] + ra[26 * 1024 + k * 1536];
          atomicAdd((float*)(ws + acco) + hp * 544 + j, t);
        }
      }
      STAMP(45);
      DRAIN();
      STAMP(3 + 16 * L);
      BAR();
      if (w == 0) {
        arrive(ws, CTR_A + L * 128 + (idx & 15), 16, 8, lane);
        poll(ws, CTR_A + L * 128 + xcd * 16, 16, 16, lane);
      }
      BAR();
      STAMP(4 + 16 * L);
    }
    u32x4 wkv[8];
    {
      IDS();
      u32x4 pq[4], kv[4][2];
      const int row = r < 16 ? r : 16;
#pragma unroll
      for (int i = 0; i < 4; ++i) {
        kv[i][0] = ld128(rs, row * 64 + h * 32 + i * 1088, acco + w * 4352);
        kv[i][1] = ld128(rs, row * 64 + h * 32 + i * 1088 + 16, acco + w * 4352);
        pq[i] = ld128(rs, voff + i * 1024, pqo + (tt * 32 + w * 4) * 1024);
      }
      SBAR();
      STAMP(46);
#pragma unroll
      for (int i = 0; i < 4; ++i) {
        const f32x4 k0 = __builtin_bit_cast(f32x4, kv[i][0]), k1 = __builtin_bit_cast(f32x4, kv[i][1]);
        const u32x4 a = {cvtpk(k0[0], k0[1]), cvtpk(k0[2], k0[3]), cvtpk(k1[0], k1[1]), cvtpk(k1[2], k1[3])};
        f32x16 d = MFMA(a, pq[i], zero16());
        const float inv = __builtin_amdgcn_rcpf(d[8] + 1e-6f);
#pragma unroll
        for (int j = 0; j < 8; ++j) d[j] *= inv;
        *(LAS u32x4*)(smem + L_ACT + (w * 4 + i) * 1024 + voff) = pack8(d, 0);
      }
      const int wqo = OFF_W + ((T_WQ + nt) * 32 + kh * 16) * 1024;
#pragma unroll
      for (int i = 0; i < 8; ++i) asm volatile("" : "=v"(wkv[i]));
      if (cb == 0 && kh == 1) {
#pragma unroll
        for (int i = 0; i < 8; ++i) wkv[i] = ldw(rs, (voff ^ (w4 == 1 ? 256 : 0)) + (i & 3) * 1024, OFF_W + (T_WKV * 32 + w4 * 8 + (i & 4)) * 1024);
      }
      if (w == 1) glds2(ws, OFF_CS3 + cb * 2048 + voff, lds0 + 56 * KB);
      STAMP(50);
      BAR();
      STAMP(5 + 16 * L);
      f32x16 acc = gemm_half(smem + L_ACT + kh * 16384 + voff, [&](int i) { return wp[i]; },
                             [&](int i) { if (i >= 1 && i <= NEARLY) wp[i - 1] = ldw(rs, vrot + ((i - 1) & 3) * 1024, wqo + ((i - 1) & ~3) * 1024); });
      STAMP(6 + 16 * L);
      put_hi(smem + L_WL + w * 2048 + voff, acc);
      BAR();
      STAMP(51);
      add_lo(smem + L_WL + (w ^ 4) * 2048 + voff, acc);
      resid_epilogue(acc, xr, rs, xb, sb_, out, false, tt, 2 * nt + kh, lane);
      STAMP(52);
      DRAIN();
      STAMP(7 + 16 * L);
      BAR();
      if (w != 0) {
#pragma unroll
        for (int i = NEARLY; i < 16; ++i) wp[i] = ldw(rs, vrot + (i & 3) * 1024, wqo + (i & ~3) * 1024);
      }
      SBAR();
      if (w == 0) {
        arrive(ws, CTR_B + L * 64 + tt, 1, 1, lane);
        poll(ws, CTR_B + L * 64 + tt, 1, 4, lane);
      }
      BAR();
      STAMP(8 + 16 * L);
    }
    u32x4 q0;
    {
      IDS();
      glds4<true>(ws, xb + (tt * 32 + 4 * w) * 1024 + voff, lds0 + L_ACT + 4 * w * 1024);
      glds1s(ws, sb_ + tt * 8192 + w * 1024 + voff, lds0 + 48 * KB + w * 1024);
      DRAIN();
      BAR();
      STAMP(9 + 16 * L);
      if (w == 0) {
#pragma unroll
        for (int i = NEARLY; i < 16; ++i) wp[i] = ldw(rs, voff + (i & 3) * 1024, OFF_W + ((T_WQ + nt) * 32 + (i & ~3)) * 1024);
      }
      SBAR();
      const LAS char* csl = smem + 56 * KB + h * 16;
      RowStat st;
      if (cb == 0) {
        f32x16 kacc = zero16();
        if (kh == 1) {
          u32x4 a[8];
#pragma unroll
          for (int i = 0; i < 8; ++i) a[i] = *(const LAS u32x4*)(smem + L_ACT + (w4 * 8 + i) * 1024 + voff);
          SBAR();
#pragma unroll
          for (int i = 0; i < 8; ++i) kacc = MFMA(wkv[i], a[i], kacc);
          st = row_stats(smem + 48 * KB, r, h);
          LAS char* kp = smem + 60 * KB + w4 * 4096 + voff;
          if (w4 != 0) { *(LAS f32x4*)(kp + (w4 == 1 ? 2048 : 0)) = f32x4{kacc[0], kacc[1], kacc[2], kacc[3]}; *(LAS f32x4*)(kp + (w4 == 1 ? 3072 : 1024)) = f32x4{kacc[4], kacc[5], kacc[6], kacc[7]}; }
          put_hi(kp + (w4 == 1 ? 0 : 2048), kacc);
        }
        BAR();
        if (w == 4 || w == 5) {
          const LAS char* kp = smem + 60 * KB + w4 * 2048 + voff;
          add_lo(kp + (w4 ^ 1) * 4096, kacc); add_lo(kp + 8192, kacc); add_lo(kp + 12288, kacc);
#pragma unroll
          for (int g = 0; g < 2; ++g) {
            const f32x4 c = lds2(csl + 512 + w4 * 64 + g * 32, 1024);
#pragma unroll
            for (int j = 0; j < 4; ++j) kacc[4 * g + j] = st.rs * (kacc[4 * g + j] - st.mu * c[j]);
          }
          u32x4 v = pack8(kacc, 0);
          if (w == 5) {
            const int t16 = r & 15, hh = (t16 >> 2) & 1, jj = ((t16 >> 3) << 2) | (t16 & 3);
            LAS uint16_t* vb = (LAS uint16_t*)(smem + 76 * KB + (r >> 4) * 512 + hh * 256) + jj;
#pragma unroll
            for (int j = 0; j < 8; ++j) vb[(8 * (j >> 2) + 4 * h + (j & 3)) * 8] = (uint16_t)((j & 1) ? (v[j >> 1] >> 16) : (v[j >> 1] & 0xffffu));
            v = *(const LAS u32x4*)(smem + 76 * KB + voff);
          }
          st128(rs, voff, kvo + tt * 2048 + w4 * 1024, v);
        }
      }
      f32x16 acc = gemm_half(smem + L_ACT + kh * 16384 + voff, [&](int i) { return wp[i]; },
                             [&](int i) {
        if (i == 2 && !(cb == 0 && kh == 1)) st = row_stats(smem + 48 * KB, r, h);
        if (i == KV_PUB && cb == 0 && (w == 4 || w == 5)) {
          DRAIN();
          arrive(ws, CTR_C + L * 128 + (tt & 15), 16, 8, lane);
          STAMP(53);
        }
      });
      put_hi(smem + 80 * KB + w * 2048 + voff, acc);
      f32x4 c[2];
#pragma unroll
      for (int g = 0; g < 2; ++g) c[g] = lds2(csl + w4 * 128 + kh * 64 + g * 32, 1024);
      BAR();
      add_lo(smem + 80 * KB + (w ^ 4) * 2048 + voff, acc);
      const float sc = st.rs * (0.25f * LOG2E);
#pragma unroll
      for (int j = 0; j < 8; ++j) acc[j] = sc * (acc[j] - st.mu * c[j >> 2][j & 3]);
      q0 = pack8(acc, 0);
      *(LAS u32x4*)(smem + L_COMB + w * 3072 + voff) = q0;
      if (tid >= 64 && tid < 74) {
        const int s5 = (tid - 64) >> 1, o5 = s5 == 0 ? 0 : (s5 == 1 ? 512 : (s5 == 2 ? 2048 : (s5 == 3 ? 2560 : 4096)));
        const unsigned c = (tid & 1) ? 0u : 0x3f803f80u;
        *(LAS u32x4*)(smem + L_CONST + o5 + (tid & 1) * 16) = u32x4{c, c, c, c};
      }
      PIN(q0);
      STAMP(10 + 16 * L);
      if (w == 0) poll(ws, CTR_C + L * 128 + xcd * 16, 16, 8, lane);
      BAR();
      STAMP(11 + 16 * L);
    }
    u32x16 wo0, wo1, wo2, wo3, wn0, wn1, wn2;
    {
      IDS();
      f32x16 oa = zero16(), ob = zero16(), sa, sb;
      u32x4 kf, v0, v1, p0, p1 = {0u, 0u, 0u, 0u};
      unsigned ka = lds0 + kh * 65536 + lane * 16;
      unsigned va = r < 16 ? lds0 + kh * 65536 + 1024 + (h * 16 + r) * 16 : lds0 + L_CONST + ((r == 16 || r == 20) ? 0 : 16);
      const unsigned vinc = r < 16 ? 4096u : 0u;
      const unsigned o0 = kh * 65536 + w4 * 2048;
      unsigned ga = kvo + o0 + voff, wo = OFF_W + ((T_WOB + nt) * 32 + kh * 16) * 1024 + vrot, n, m0s;
      unsigned wn = OFF_W + (hp * 32 + 4 * w) * 1024 + voff;
      const u32x4 q1 = *(const LAS u32x4*)(smem + L_COMB + (w ^ 4) * 3072 + voff);
      asm volatile(ATTN_ASM
                   : "+{v[0:15]}"(oa), "+{v[16:31]}"(ob), "=&{v[32:47]}"(sa), "=&{v[48:63]}"(sb), "=&{v[64:67]}"(kf), "=&{v[68:71]}"(v0),
                     "=&{v[72:75]}"(v1), "=&{v[76:79]}"(p0), "+{v[80:83]}"(p1), "+{v92}"(ka), "+{v93}"(va), "+{v95}"(ga), "+{v96}"(wo),
                     "=&{v[128:143]}"(wo0), "=&{v[144:159]}"(wo1), "=&{v[160:175]}"(wo2), "=&{v[176:191]}"(wo3),
                     "+{v97}"(wn), "=&{v[192:207]}"(wn0), "=&{v[208:223]}"(wn1), "=&{v[224:239]}"(wn2),
                     [n] "=&s"(n), [m0s] "=&s"(m0s)
                   : "{v[84:87]}"(q0), "{v[88:91]}"(q1), "{v94}"(vinc), [wsb] "s"(ws), [kh] "s"(kh), [m0a] "s"(__builtin_amdgcn_readfirstlane(lds0 + o0))
                   : "memory", "scc", "s90", "s91", "s92", "s93", "s94", "s95");
      STAMP(12 + 16 * L);
      LAS char* cw = smem + L_COMB + w * 3072 + voff;
      const LAS char* cr = smem + L_COMB + (w ^ 4) * 3072 + voff;
      *(LAS f32x4*)cw = f32x4{ob[0], ob[1], ob[2], ob[3]}; *(LAS f32x4*)(cw + 1024) = f32x4{ob[4], ob[5], ob[6], ob[7]};
      *(LAS float*)(cw + 2048) = ob[8];
      BAR();
      STAMP(54);
      add_lo(cr, oa);
      oa[8] += *(const LAS float*)(cr + 2048);
      float mx = fmaxf(fmaxf(fabsf(oa[0]), fabsf(oa[1])), fabsf(oa[2]));
      mx = fmaxf(fmaxf(mx, fabsf(oa[3])), fabsf(oa[4])); mx = fmaxf(fmaxf(mx, fabsf(oa[5])), fabsf(oa[6]));
      const float sum8 = ((oa[0] + oa[1]) + (oa[2] + oa[3])) + ((oa[4] + oa[5]) + (oa[6] + oa[7]));
      const bool ok = fmaxf(fmaxf(mx, fabsf(oa[7])), oa[8]) < 1.0e36f && oa[8] > 1.0e-27f && sum8 == sum8;
      if (__builtin_expect(!__all(ok), 0)) {
        const LAS char* kb0 = smem + lane * 16;
        const LAS char* vb0 = r < 16 ? smem + 1024 + (h * 16 + r) * 16 : smem + L_CONST + ((r == 16 || r == 20) ? 0 : 16);
        const int vstep = r < 16 ? 2048 : 0;
        float m = -INFINITY;
        oa = zero16();
#pragma unroll 1
        for (int t = 0; t < 64; ++t) {
          f32x16 s = MFMA(*(const LAS u32x4*)(kb0 + t * 2048), q0, zero16());
          const float mn = fmaxf(m, xhalf_max(max16(s)));
          const float al = __builtin_amdgcn_exp2f(m - mn);
          m = mn;
#pragma unroll
          for (int i = 0; i < 16; ++i) { oa[i] *= al; s[i] = __builtin_amdgcn_exp2f(s[i] - mn); }
          oa = MFMA(*(const LAS u32x4*)(vb0 + t * vstep), pack8(s, 0), oa);
          oa = MFMA(*(const LAS u32x4*)(vb0 + t * vstep + 512), pack8(s, 1), oa);
        }
      }
      const float inv = __builtin_amdgcn_rcpf(oa[8]);
#pragma unroll
      for (int i = 0; i < 8; ++i) oa[i] *= inv;
      st128(rs, voff, ofo + (tt * 32 + 2 * nt + kh) * 1024, pack8(oa, 0));
      STAMP(55);
      DRAIN();
      STAMP(56);
      BAR();
      if (w == 0) arrive(ws, CTR_D + L * 64 + tt, 1, 1, lane);
      if (L == 0) {
#pragma unroll
        for (int i = 0; i < 4; ++i) {
          LAS char* d = smem + L_WL + (4 * w + i) * 1024 + voff;
          *(LAS u32x4*)d = u32x4{wn0[4 * i], wn0[4 * i + 1], wn0[4 * i + 2], wn0[4 * i + 3]};
          *(LAS u32x4*)(d + 32768) = u32x4{wn1[4 * i], wn1[4 * i + 1], wn1[4 * i + 2], wn1[4 * i + 3]};
          *(LAS u32x4*)(d + 65536) = u32x4{wn2[4 * i], wn2[4 * i + 1], wn2[4 * i + 2], wn2[4 * i + 3]};
        }
      }
      if (w == 0) poll(ws, CTR_D + L * 64 + tt, 1, 4, lane);
      BAR();
      STAMP(13 + 16 * L);
    }
    {
      IDS();
      glds4<true>(ws, ofo + (tt * 32 + 4 * w) * 1024 + voff, lds0 + L_ACT + 4 * w * 1024);
      DRAIN();
      BAR();
      STAMP(14 + 16 * L);
      f32x16 acc = gemm_half(smem + L_ACT + kh * 16384 + voff, [&](int i) {
        const u32x16& v = i < 4 ? wo0 : (i < 8 ? wo1 : (i < 12 ? wo2 : wo3));
        return u32x4{v[4 * (i & 3)], v[4 * (i & 3) + 1], v[4 * (i & 3) + 2], v[4 * (i & 3) + 3]};
      }, [](int) {});
      put_hi(smem + 136 * KB + w * 2048 + voff, acc);
      BAR();
      add_lo(smem + 136 * KB + (w ^ 4) * 2048 + voff, acc);
      resid_epilogue(acc, xr, rs, xc, sc_, out, L == 1, tt, 2 * nt + kh, lane);
      STAMP(15 + 16 * L);
      if (L == 0) {
        DRAIN();
        BAR();
        if (w == 0) {
          arrive(ws, CTR_E + (tt >> 2), 1, 1, lane);
          poll(ws, CTR_E + panel, 1, 16, lane);
        }
        BAR();
        STAMP(16);
      }
    }
  }
  STAMP(60);
  STAMP_FLUSH();
}

extern "C" void kernel_launch(void* const* d_in, const int* in_sizes, int n_in, void* d_out, int out_size, void* d_ws,
                              size_t ws_size, hipStream_t stream) {
  char* ws = (char*)d_ws;
  (void)hipFuncSetAttribute((const void*)k_mega, hipFuncAttributeMaxDynamicSharedMemorySize, 160 * KB);
  k_prep<<<PREP_BLOCKS, 512, 0, stream>>>((const uint16_t*)d_in[0], (const uint16_t*)d_in[1], (const uint16_t*)d_in[2], (const uint16_t*)d_in[4],
                                          (const uint16_t*)d_in[5], (const uint16_t*)d_in[6], (const uint16_t*)d_in[7], ws);
  k_mega<<<256, 512, 160 * KB, stream>>>(ws, (uint16_t*)d_out);
}
```

```cpp
#include <hip/hip_runtime.h>
#include <hip/hip_bf16.h>
#include <stdint.h>

typedef __attribute__((ext_vector_type(8))) short bf16x8;
typedef __attribute__((ext_vector_type(4))) short s16x4;
typedef __attribute__((ext_vector_type(16))) float f32x16;
typedef __attribute__((ext_vector_type(4))) float f32x4;
typedef __attribute__((ext_vector_type(2))) float f32x2;
typedef __attribute__((ext_vector_type(16))) unsigned u32x16;
typedef __attribute__((ext_vector_type(4))) unsigned u32x4;
typedef __attribute__((ext_vector_type(2))) unsigned u32x2;
typedef __attribute__((ext_vector_type(2))) __bf16 bf16x2_t;
#define LAS __attribute__((address_space(3)))
#define GAS __attribute__((address_space(1)))
typedef GAS unsigned gu32;
#define DI __device__ __forceinline__
#define MFMA(a, b, c) __builtin_amdgcn_mfma_f32_32x32x16_bf16(__builtin_bit_cast(bf16x8, (a)), __builtin_bit_cast(bf16x8, (b)), (c), 0, 0, 0)
#define RLX_AGENT __ATOMIC_RELAXED, __HIP_MEMORY_SCOPE_AGENT

#define STAMP(slot) do {} while (0)
#define STAMP_DECL do {} while (0)
#define STAMP_FLUSH() do {} while (0)

constexpr int SEQ = 2048, DM = 512, NH = 32;
constexpr float LOG2E = 1.4426950408889634f;

constexpr int KB = 1024;
constexpr int OFF_CTR = 0;
constexpr int CTR_A = 0, CTR_C = 256, CTR_TMO = 784, CTR_LINES = 800;
constexpr int OFF_ACC = OFF_CTR + CTR_LINES * 128;
constexpr int ACC_N = NH * 17 * 16;
constexpr int ZERO_BYTES = CTR_LINES * 128 + 2 * ACC_N * 4;
constexpr int OFF_W = 256 * KB;
constexpr int T_WOA = 48, T_WQ = 64, T_WKV = 80, T_WOB = 81;
constexpr int OFF_CS1 = OFF_W + 97 * 32 * KB;
constexpr int OFF_CS3 = OFF_CS1 + 16 * KB;
constexpr int OFF_XF = OFF_CS3 + 8 * KB;
constexpr int OFF_ST = OFF_XF + 4 * 2048 * KB;
constexpr int OFF_PQ = OFF_ST + 4 * 512 * KB;
constexpr int OFF_OF = OFF_PQ + 2 * 2048 * KB;
constexpr int OFF_KV = OFF_OF + 2 * 2048 * KB;
constexpr int WS_END = OFF_KV + 2 * 128 * KB;

constexpr int L_ACT = 0, L_WL = 32 * KB;
constexpr int L_CONST = 128 * KB, L_COMB = 136 * KB;

DI unsigned cvtpk(float lo, float hi) {
  f32x2 v = {lo, hi};
  return __builtin_bit_cast(unsigned, __builtin_convertvector(v, bf16x2_t));
}
DI float bflo(unsigned u) { return __uint_as_float(u << 16); }
DI float bfhi(unsigned u) { return __uint_as_float(u & 0xffff0000u); }
DI float bfround(float x) { return bflo(cvtpk(x, x)); }
DI u32x4 pack8(const f32x16& x, int s) {
  u32x4 p;
  p[0] = cvtpk(x[8 * s + 0], x[8 * s + 1]); p[1] = cvtpk(x[8 * s + 2], x[8 * s + 3]);
  p[2] = cvtpk(x[8 * s + 4], x[8 * s + 5]); p[3] = cvtpk(x[8 * s + 6], x[8 * s + 7]);
  return p;
}
DI float xhalf_sum(float v) {
  auto r = __builtin_amdgcn_permlane32_swap(__float_as_uint(v), __float_as_uint(v), false, false);
  return __uint_as_float(r[0]) + __uint_as_float(r[1]);
}
DI float xhalf_max(float v) {
  auto r = __builtin_amdgcn_permlane32_swap(__float_as_uint(v), __float_as_uint(v), false, false);
  return fmaxf(__uint_as_float(r[0]), __uint_as_float(r[1]));
}
DI f32x16 zero16() { f32x16 z; _Pragma("unroll") for (int i = 0; i < 16; ++i) z[i] = 0.f; return z; }
DI float phi(float y, float rs, float rsl) {
  return fmaxf(__builtin_fmaf(y, rs, 1e-6f), 1e-6f) + __builtin_amdgcn_fmed3f(__builtin_amdgcn_exp2f(y * rsl), 0.f, 1.f);
}

#define GLDS_HEAD "s_nop 4\n\ts_mov_b32 %0, m0\n\ts_mov_b32 m0, %3\n\ts_nop 0\n\t"
#define GLDS_OPS : "=&s"(sv) : "v"(voff), "s"(sbase), "s"(lds_base) : "memory"
template <bool SC1> DI void glds4(const char* sbase, unsigned voff, unsigned lds_base) {
  unsigned sv;
  lds_base = __builtin_amdgcn_readfirstlane(lds_base);
  if (SC1) asm volatile(GLDS_HEAD "global_load_lds_dwordx4 %1, %2 sc1\n\tglobal_load_lds_dwordx4 %1, %2 offset:1024 sc1\n\t"
               "global_load_lds_dwordx4 %1, %2 offset:2048 sc1\n\tglobal_load_lds_dwordx4 %1, %2 offset:3072 sc1\n\ts_mov_b32 m0, %0" GLDS_OPS);
  else asm volatile(GLDS_HEAD "global_load_lds_dwordx4 %1, %2\n\tglobal_load_lds_dwordx4 %1, %2 offset:1024\n\t"
               "global_load_lds_dwordx4 %1, %2 offset:2048\n\tglobal_load_lds_dwordx4 %1, %2 offset:3072\n\ts_mov_b32 m0, %0" GLDS_OPS);
}
DI void glds2(const char* sbase, unsigned voff, unsigned lds_base) {
  unsigned sv;
  lds_base = __builtin_amdgcn_readfirstlane(lds_base);
  asm volatile(GLDS_HEAD "global_load_lds_dwordx4 %1, %2\n\tglobal_load_lds_dwordx4 %1, %2 offset:1024\n\ts_mov_b32 m0, %0" GLDS_OPS);
}
DI void glds1s(const char* sbase, unsigned voff, unsigned lds_base) {
  unsigned sv;
  lds_base = __builtin_amdgcn_readfirstlane(lds_base);
  asm volatile(GLDS_HEAD "global_load_lds_dwordx4 %1, %2 sc1\n\ts_mov_b32 m0, %0" GLDS_OPS);
}
DI void glds1(const char* sbase, unsigned voff, unsigned lds_base) {
  unsigned sv;
  lds_base = __builtin_amdgcn_readfirstlane(lds_base);
  asm volatile(GLDS_HEAD "global_load_lds_dwordx4 %1, %2\n\ts_mov_b32 m0, %0" GLDS_OPS);
}
#define BAR() asm volatile("s_waitcnt lgkmcnt(0)\n\ts_barrier" ::: "memory")
#define DRAIN() asm volatile("s_waitcnt vmcnt(0)" ::: "memory")
#define PIN(x) asm volatile("" : "+v"(x))
#define SBAR() __builtin_amdgcn_sched_barrier(0)

typedef __amdgpu_buffer_rsrc_t rsrc_t;
DI u32x4 ld128(rsrc_t rs, int voff, int soff) { return __builtin_amdgcn_raw_buffer_load_b128(rs, voff, soff, 16); }
DI u32x2 ld64(rsrc_t rs, int voff, int soff) { return __builtin_amdgcn_raw_buffer_load_b64(rs, voff, soff, 16); }
DI u32x4 ldw(rsrc_t rs, int voff, int soff) { return __builtin_amdgcn_raw_buffer_load_b128(rs, voff, soff, 0); }
DI float ldwf(rsrc_t rs, int voff, int soff) { return __uint_as_float(__builtin_amdgcn_raw_buffer_load_b32(rs, voff, soff, 0)); }
DI void st128(rsrc_t rs, int voff, int soff, u32x4 v) { __builtin_amdgcn_raw_buffer_store_b128(v, rs, voff + soff, 0, 16); }
DI void st64(rsrc_t rs, int voff, int soff, u32x2 v) { __builtin_amdgcn_raw_buffer_store_b64(v, rs, voff, soff, 16); }

DI void poll(char* ws, int line0, int n, unsigned want, int lane) {
  gu32* c = (gu32*)(ws + OFF_CTR) + (line0 + (lane < n ? lane : 0)) * 32;
  gu32* tmo = (gu32*)(ws + OFF_CTR) + CTR_TMO * 32;
#pragma clang loop unroll(disable)
  for (unsigned spins = 0;;) {
    if (__all(__hip_atomic_load(c, RLX_AGENT) >= want)) break;
    __builtin_amdgcn_s_sleep(1);
    if (++spins > (1u << 17)) { if (lane == 0) __hip_atomic_store(tmo, 1u, RLX_AGENT); break; }
    if ((spins & 255u) == 0u && __builtin_amdgcn_readfirstlane(__hip_atomic_load(tmo, RLX_AGENT))) break;
  }
  __builtin_amdgcn_fence(__ATOMIC_ACQUIRE, "wavefront");
}
DI void arrive(char* ws, int line0, int stride, int n, int lane) {
  if (lane < n) __hip_atomic_fetch_add((gu32*)(ws + OFF_CTR) + (line0 + lane * stride) * 32, 1u, RLX_AGENT);
}

constexpr unsigned POISON = 0xffffffffu;
DI u32x4 clean(u32x4 v) { _Pragma("unroll") for (int j = 0; j < 4; ++j) v[j] = min(v[j], POISON - 1u); return v; }
DI unsigned umax4(const u32x4& v, unsigned m) { return max(max(max(v[0], v[1]), m), max(v[2], v[3])); }
#define VERIFY(MAXEXPR, RELOAD) do { _Pragma("clang loop unroll(disable)") for (unsigned sp_ = 0; !__all((MAXEXPR) != POISON) && sp_ < (1u << 16); ++sp_) { \
    __builtin_amdgcn_s_sleep(2); asm volatile("" ::: "memory"); RELOAD; } } while (0)

#define VERIFY_FAST(MAXEXPR, RELOAD) do { if (__builtin_expect(!__all((MAXEXPR) != POISON), 0)) { \
    _Pragma("clang loop unroll(disable)") for (unsigned sp_ = 0; sp_ < (1u << 16); ++sp_) { \
      __builtin_amdgcn_s_sleep(2); asm volatile("" ::: "memory"); RELOAD; __builtin_amdgcn_s_waitcnt(0x0F70); if (__all((MAXEXPR) != POISON)) break; } } } while (0)

struct RowStat { float mu, rs; };
DI RowStat finish_stats(float s, float q) {
  const float mean = s * (1.f / DM);
  const float var = fmaxf(q * (1.f / DM) - mean * mean, 0.f);
  const float sd = bfround(__builtin_amdgcn_sqrtf(bfround(bfround(var) + 1e-5f)));
  RowStat o; o.mu = bfround(mean); o.rs = __builtin_amdgcn_rcpf(sd); return o;
}
DI RowStat row_stats(const LAS char* st, int r, int h) {
  float s = 0.f, q = 0.f;
#pragma unroll
  for (int i = 0; i < 16; ++i) { const f32x2 p = *(const LAS f32x2*)(st + (h * 16 + i) * 256 + r * 8); s += p[0]; q += p[1]; }
  return finish_stats(xhalf_sum(s), xhalf_sum(q));
}
DI float dot2(unsigned a, unsigned b, float c) {
  return __builtin_amdgcn_fdot2_f32_bf16(__builtin_bit_cast(bf16x2_t, a), __builtin_bit_cast(bf16x2_t, b), c, false);
}
DI float sum8(const u32x4& v) { float c = 0.f; _Pragma("unroll") for (int j = 0; j < 4; ++j) c = dot2(v[j], 0x3f803f80u, c); return c; }
DI float sq8(const u32x4& v) { float c = 0.f; _Pragma("unroll") for (int j = 0; j < 4; ++j) c = dot2(v[j], v[j], c); return c; }
DI void put16(LAS char* p, const f32x16& a, int lane) {
#pragma unroll
  for (int g = 0; g < 4; ++g) *(LAS f32x4*)(p + g * 1024 + lane * 16) = f32x4{a[4 * g], a[4 * g + 1], a[4 * g + 2], a[4 * g + 3]};
}
DI void add16(const LAS char* p, f32x16& a, int lane) {
#pragma unroll
  for (int g = 0; g < 4; ++g) {
    const f32x4 v = *(const LAS f32x4*)(p + g * 1024 + lane * 16);
#pragma unroll
    for (int j = 0; j < 4; ++j) a[4 * g + j] += v[j];
  }
}
DI void put_hi(LAS char* p, const f32x16& a) {
  *(LAS f32x4*)p = f32x4{a[8], a[9], a[10], a[11]}; *(LAS f32x4*)(p + 1024) = f32x4{a[12], a[13], a[14], a[15]};
}
DI void add_lo(const LAS char* p, f32x16& a) {
  const f32x4 u = *(const LAS f32x4*)p, v = *(const LAS f32x4*)(p + 1024);
#pragma unroll
  for (int j = 0; j < 4; ++j) { a[j] += u[j]; a[4 + j] += v[j]; }
}
DI float max16(const f32x16& s) {
  float a = fmaxf(fmaxf(s[0], s[1]), s[2]), b = fmaxf(fmaxf(s[3], s[4]), s[5]);
  a = fmaxf(fmaxf(a, s[6]), s[7]); b = fmaxf(fmaxf(b, s[8]), s[9]);
  a = fmaxf(fmaxf(a, s[10]), s[11]); b = fmaxf(fmaxf(b, s[12]), s[13]);
  a = fmaxf(fmaxf(a, s[14]), s[15]);
  return fmaxf(a, b);
}

constexpr int PREP_BLOCKS = 714;
DI u32x4 tr_frag(const LAS char* a, int second) {
  const u32x2 lo = __builtin_bit_cast(u32x2, __builtin_amdgcn_ds_read_tr16_b64_v4i16((LAS s16x4*)a));
  const u32x2 hi = __builtin_bit_cast(u32x2, __builtin_amdgcn_ds_read_tr16_b64_v4i16((LAS s16x4*)(a + second)));
  return u32x4{lo[0], lo[1], hi[0], hi[1]};
}
__global__ __launch_bounds__(256) void k_prep(const uint16_t* __restrict__ x0, const uint16_t* __restrict__ wqkv,
                                              const uint16_t* __restrict__ woa, const uint16_t* __restrict__ wq,
                                              const uint16_t* __restrict__ wk, const uint16_t* __restrict__ wv,
                                              const uint16_t* __restrict__ wob, char* __restrict__ ws) {
  __shared__ __attribute__((aligned(16))) char tile_[9216];
  LAS char* smem = (LAS char*)tile_;
  asm volatile("" :: "s"(x0), "s"(wqkv), "s"(woa), "s"(wq), "s"(wk), "s"(wv), "s"(wob), "s"(ws));
  const rsrc_t rs = __builtin_amdgcn_make_buffer_rsrc(ws, 0, WS_END, 0x00020000);
  const int b = blockIdx.x, tid = threadIdx.x, lane = tid & 63, r = lane & 31, h = lane >> 5;
  const int w = __builtin_amdgcn_readfirstlane(tid >> 6), voff = lane * 16, c8 = tid & 7;
  if (b < 458) {
    const int cu = b < 66 ? ((b >> 1) < 24 ? b >> 1 : ((b >> 1) < 32 ? (b >> 1) + 8 : 48)) : (b - 66) >> 3;
    const uint16_t* src; int ld = 512;
    if (cu < 24) { src = wqkv + cu * 64 + c8 * 8; ld = 1536; }
    else if (cu == 48) { src = ((c8 & 2) ? wv : wk) + (c8 & 1) * 8; ld = 16; }
    else src = (cu < 32 ? woa : (cu < 40 ? wq : wob)) + (cu & 7) * 64 + c8 * 8;
    const int wt = cu < 40 ? 2 * cu : (cu < 48 ? 2 * cu + 1 : T_WKV);
    if (b < 66) {
      const int kb = b & 1;
      u32x4 v[8];
#pragma unroll
      for (int i = 0; i < 8; ++i) v[i] = *(const u32x4*)(src + (size_t)(kb * 256 + i * 32 + (tid >> 3)) * ld);
      f32x4 s0 = {0.f, 0.f, 0.f, 0.f}, s1 = s0;
#pragma unroll
      for (int i = 0; i < 8; ++i) {
        s0 += f32x4{bflo(v[i][0]), bfhi(v[i][0]), bflo(v[i][1]), bfhi(v[i][1])};
        s1 += f32x4{bflo(v[i][2]), bfhi(v[i][2]), bflo(v[i][3]), bfhi(v[i][3])};
      }
      *(LAS f32x4*)(smem + (tid >> 3) * 272 + c8 * 32) = s0; *(LAS f32x4*)(smem + (tid >> 3) * 272 + c8 * 32 + 16) = s1;
      __syncthreads();
      if (w == 0) {
        float t = 0.f;
#pragma unroll
        for (int i = 0; i < 32; ++i) t += *(const LAS float*)(smem + i * 272 + lane * 4);
        float* cs1 = (float*)(ws + OFF_CS1); float* cs3 = (float*)(ws + OFF_CS3);
        if (cu < 24) cs1[((wt & 15) + h) * 256 + kb * 128 + (wt >> 4) * 32 + r] = t;
        else if (cu < 40) cs3[((wt - T_WQ) >> 2) * 512 + kb * 256 + ((wt - T_WQ) & 3) * 32 + lane] = t;
        else if (h == 0) {
#pragma unroll
          for (int i = 0; i < 4; ++i) cs3[i * 512 + kb * 256 + 128 + r] = t;
        }
      }
    } else {
      const int k8 = (b - 66) & 7;
      u32x4 v[2];
#pragma unroll
      for (int i = 0; i < 2; ++i) v[i] = *(const u32x4*)(src + (size_t)(k8 * 64 + i * 32 + (tid >> 3)) * ld);
#pragma unroll
      for (int i = 0; i < 2; ++i) *(LAS u32x4*)(smem + (i * 32 + (tid >> 3)) * 144 + c8 * 16) = v[i];
      __syncthreads();
#pragma unroll
      for (int t = 0; t < 2; ++t) {
        const u32x4 f = tr_frag(smem + (w * 16 + 4 * h + ((lane & 15) >> 2)) * 144 + t * 64 + ((lane >> 4) & 1) * 32 + (lane & 3) * 8, 8 * 144);
        if (t == 0 || cu < 48) st128(rs, voff, OFF_W + ((wt + t) * 32 + k8 * 4 + w) * 1024, f);
      }
    }
  } else {
    const int u = b - 458, tt = u >> 2, kq = u & 3;
    u32x4 v[2];
#pragma unroll
    for (int i = 0; i < 2; ++i) v[i] = *(const u32x4*)(x0 + (size_t)(tt * 32 + i * 16 + (tid >> 4)) * DM + kq * 128 + (tid & 15) * 8);
    if (u * 256 + tid < ZERO_BYTES / 16) st128(rs, (u * 256 + tid) * 16, OFF_CTR, u32x4{0u, 0u, 0u, 0u});
#pragma unroll
    for (int i = 0; i < 2; ++i) *(LAS u32x4*)(smem + (i * 16 + (tid >> 4)) * 272 + (tid & 15) * 16) = v[i];
    __syncthreads();
#pragma unroll
    for (int i = 0; i < 2; ++i) {
      const LAS char* xs = smem + r * 272 + (2 * w + i) * 32 + h * 8;
      const u32x2 e0 = *(const LAS u32x2*)xs, e1 = *(const LAS u32x2*)(xs + 16);
      const u32x4 f = clean(u32x4{e0[0], e0[1], e1[0], e1[1]});
      st128(rs, voff, OFF_XF + (tt * 32 + kq * 8 + 2 * w + i) * 1024, f);
      const float s = xhalf_sum(sum8(f)), q = xhalf_sum(sq8(f));
      if (h == 0) st64(rs, r * 8, OFF_ST + (tt * 32 + kq * 8 + 2 * w + i) * 256, u32x2{min(__float_as_uint(s), POISON - 1u), min(__float_as_uint(q), POISON - 1u)});
    }
  }
}

#define ATTN_ASM \
  "s_nop 4\n\t" \
  "s_mov_b32 %[m0s], m0\n\t" \
  "s_mov_b32 m0, %[m0a]\n\t" \
  "s_nop 0\n\t" \
  "global_load_lds_dwordx4 v95, %[wsb] sc1\n\t" \
  "global_load_lds_dwordx4 v95, %[wsb] offset:1024 sc1\n\t" \
  "s_add_u32 m0, m0, 0x2000\n\t" \
  "v_add_u32 v95, 0x2000, v95\n\t" \
  "global_load_lds_dwordx4 v95, %[wsb] sc1\n\t" \
  "global_load_lds_dwordx4 v95, %[wsb] offset:1024 sc1\n\t" \
  "s_add_u32 m0, m0, 0x2000\n\t" \
  "v_add_u32 v95, 0x2000, v95\n\t" \
  "global_load_lds_dwordx4 v95, %[wsb] sc1\n\t" \
  "global_load_lds_dwordx4 v95, %[wsb] offset:1024 sc1\n\t" \
  "s_add_u32 m0, m0, 0x2000\n\t" \
  "v_add_u32 v95, 0x2000, v95\n\t" \
  "s_waitcnt vmcnt(4)\n\t" \
  "s_barrier\n\t" \
  "ds_read_b128 v[64:67], v92 offset:0\n\t" \
  "ds_read_b128 v[68:71], v93 offset:0\n\t" \
  "ds_read_b128 v[72:75], v93 offset:0\n\t" \
  "s_mov_b32 %[n], 0\n\t" \
  "s_waitcnt lgkmcnt(0)\n\t" \
  "v_mfma_f32_32x32x16_bf16 v[32:47], v[64:67], v[84:87], 0\n\t" \
  "s_getpc_b64 s[92:93]\n\t" \
  "Lpc%=:\n\t" \
  "s_add_u32 s90, s92, Lb0%=-Lpc%=\n\t" \
  "s_addc_u32 s91, s93, 0\n\t" \
  "s_cmp_eq_u32 %[kh], 0\n\t" \
  "s_cselect_b32 s94, 0, Lb1%=-Lb0%=\n\t" \
  "s_add_u32 s90, s90, s94\n\t" \
  "s_addc_u32 s91, s91, 0\n\t" \
  "s_add_u32 s92, s92, Ltab%=-Lpc%=\n\t" \
  "s_addc_u32 s93, s93, 0\n\t" \
  "s_setprio 1\n\t" \
  "s_nop 5\n\t" \
  "s_setpc_b64 s[90:91]\n\t" \
  "Lb0%=:\n\t" \
  "s_setprio 2\n\t" \
  "v_exp_f32 v32, v32\n\t" \
  "v_exp_f32 v33, v33\n\t" \
  "s_waitcnt lgkmcnt(2)\n\t" \
  "v_exp_f32 v34, v34\n\t" \
  "v_mfma_f32_32x32x16_bf16 v[16:31], v[72:75], v[80:83], v[16:31]\n\t" \
  "ds_read_b128 v[72:75], v93 offset:512\n\t" \
  "v_exp_f32 v35, v35\n\t" \
  "v_cvt_pk_bf16_f32 v76, v32, v33\n\t" \
  "v_exp_f32 v36, v36\n\t" \
  "v_exp_f32 v37, v37\n\t" \
  "v_mfma_f32_32x32x16_bf16 v[48:63], v[64:67], v[88:91], 0\n\t" \
  "ds_read_b128 v[64:67], v92 offset:2048\n\t" \
  "v_cvt_pk_bf16_f32 v77, v34, v35\n\t" \
  "v_exp_f32 v38, v38\n\t" \
  "v_exp_f32 v39, v39\n\t" \
  "v_cvt_pk_bf16_f32 v78, v36, v37\n\t" \
  "v_exp_f32 v40, v40\n\t" \
  "v_exp_f32 v41, v41\n\t" \
  "v_cvt_pk_bf16_f32 v79, v38, v39\n\t" \
  "v_exp_f32 v42, v42\n\t" \
  "v_exp_f32 v43, v43\n\t" \
  "s_waitcnt lgkmcnt(2)\n\t" \
  "v_exp_f32 v44, v44\n\t" \
  "v_mfma_f32_32x32x16_bf16 v[0:15], v[68:71], v[76:79], v[0:15]\n\t" \
  "v_cvt_pk_bf16_f32 v80, v40, v41\n\t" \
  "v_exp_f32 v45, v45\n\t" \
  "v_cvt_pk_bf16_f32 v81, v42, v43\n\t" \
  "v_exp_f32 v46, v46\n\t" \
  "v_exp_f32 v47, v47\n\t" \
  "v_cvt_pk_bf16_f32 v82, v44, v45\n\t" \
  "v_cvt_pk_bf16_f32 v83, v46, v47\n\t" \
  "v_exp_f32 v48, v48\n\t" \
  "v_exp_f32 v49, v49\n\t" \
  "s_waitcnt lgkmcnt(1)\n\t" \
  "v_exp_f32 v50, v50\n\t" \
  "v_mfma_f32_32x32x16_bf16 v[0:15], v[72:75], v[80:83], v[0:15]\n\t" \
  "v_exp_f32 v51, v51\n\t" \
  "v_cvt_pk_bf16_f32 v76, v48, v49\n\t" \
  "v_exp_f32 v52, v52\n\t" \
  "v_exp_f32 v53, v53\n\t" \
  "s_waitcnt lgkmcnt(0)\n\t" \
  "v_exp_f32 v54, v54\n\t" \
  "v_mfma_f32_32x32x16_bf16 v[32:47], v[64:67], v[84:87], 0\n\t" \
  "v_cvt_pk_bf16_f32 v77, v50, v51\n\t" \
  "v_exp_f32 v55, v55\n\t" \
  "v_cvt_pk_bf16_f32 v78, v52, v53\n\t" \
  "v_exp_f32 v56, v56\n\t" \
  "v_exp_f32 v57, v57\n\t" \
  "v_cvt_pk_bf16_f32 v79, v54, v55\n\t" \
  "v_exp_f32 v58, v58\n\t" \
  "v_exp_f32 v59, v59\n\t" \
  "v_mfma_f32_32x32x16_bf16 v[16:31], v[68:71], v[76:79], v[16:31]\n\t" \
  "ds_read_b128 v[68:71], v93 offset:2048\n\t" \
  "v_cvt_pk_bf16_f32 v80, v56, v57\n\t" \
  "v_exp_f32 v60, v60\n\t" \
  "v_exp_f32 v61, v61\n\t" \
  "v_cvt_pk_bf16_f32 v81, v58, v59\n\t" \
  "v_exp_f32 v62, v62\n\t" \
  "v_exp_f32 v63, v63\n\t" \
  "v_cvt_pk_bf16_f32 v82, v60, v61\n\t" \
  "v_cvt_pk_bf16_f32 v83, v62, v63\n\t" \
  "v_exp_f32 v32, v32\n\t" \
  "v_exp_f32 v33, v33\n\t" \
  "s_waitcnt lgkmcnt(2)\n\t" \
  "v_exp_f32 v34, v34\n\t" \
  "v_mfma_f32_32x32x16_bf16 v[16:31], v[72:75], v[80:83], v[16:31]\n\t" \
  "ds_read_b128 v[72:75], v93 offset:2560\n\t" \
  "v_exp_f32 v35, v35\n\t" \
  "v_cvt_pk_bf16_f32 v76, v32, v33\n\t" \
  "v_exp_f32 v36, v36\n\t" \
  "v_exp_f32 v37, v37\n\t" \
  "v_mfma_f32_32x32x16_bf16 v[48:63], v[64:67], v[88:91], 0\n\t" \
  "ds_read_b128 v[64:67], v92 offset:4096\n\t" \
  "v_cvt_pk_bf16_f32 v77, v34, v35\n\t" \
  "v_exp_f32 v38, v38\n\t" \
  "v_exp_f32 v39, v39\n\t" \
  "v_cvt_pk_bf16_f32 v78, v36, v37\n\t" \
  "v_exp_f32 v40, v40\n\t" \
  "v_exp_f32 v41, v41\n\t" \
  "v_cvt_pk_bf16_f32 v79, v38, v39\n\t" \
  "v_exp_f32 v42, v42\n\t" \
  "v_exp_f32 v43, v43\n\t" \
  "s_waitcnt lgkmcnt(2)\n\t" \
  "v_exp_f32 v44, v44\n\t" \
  "v_mfma_f32_32x32x16_bf16 v[0:15], v[68:71], v[76:79], v[0:15]\n\t" \
  "v_cvt_pk_bf16_f32 v80, v40, v41\n\t" \
  "v_exp_f32 v45, v45\n\t" \
  "v_cvt_pk_bf16_f32 v81, v42, v43\n\t" \
  "v_exp_f32 v46, v46\n\t" \
  "v_exp_f32 v47, v47\n\t" \
  "v_cvt_pk_bf16_f32 v82, v44, v45\n\t" \
  "v_cvt_pk_bf16_f32 v83, v46, v47\n\t" \
  "s_setprio 0\n\t" \
  "v_exp_f32 v48, v48\n\t" \
  "v_exp_f32 v49, v49\n\t" \
  "s_waitcnt lgkmcnt(1)\n\t" \
  "v_exp_f32 v50, v50\n\t" \
  "v_mfma_f32_32x32x16_bf16 v[0:15], v[72:75], v[80:83], v[0:15]\n\t" \
  "v_exp_f32 v51, v51\n\t" \
  "v_cvt_pk_bf16_f32 v76, v48, v49\n\t" \
  "v_exp_f32 v52, v52\n\t" \
  "v_exp_f32 v53, v53\n\t" \
  "s_waitcnt lgkmcnt(0)\n\t" \
  "v_exp_f32 v54, v54\n\t" \
  "v_mfma_f32_32x32x16_bf16 v[32:47], v[64:67], v[84:87], 0\n\t" \
  "v_cvt_pk_bf16_f32 v77, v50, v51\n\t" \
  "v_exp_f32 v55, v55\n\t" \
  "v_cvt_pk_bf16_f32 v78, v52, v53\n\t" \
  "v_exp_f32 v56, v56\n\t" \
  "v_exp_f32 v57, v57\n\t" \
  "v_cvt_pk_bf16_f32 v79, v54, v55\n\t" \
  "v_exp_f32 v58, v58\n\t" \
  "v_exp_f32 v59, v59\n\t" \
  "v_mfma_f32_32x32x16_bf16 v[16:31], v[68:71], v[76:79], v[16:31]\n\t" \
  "ds_read_b128 v[68:71], v93 offset:4096\n\t" \
  "v_cvt_pk_bf16_f32 v80, v56, v57\n\t" \
  "v_exp_f32 v60, v60\n\t" \
  "v_exp_f32 v61, v61\n\t" \
  "v_cvt_pk_bf16_f32 v81, v58, v59\n\t" \
  "v_exp_f32 v62, v62\n\t" \
  "v_exp_f32 v63, v63\n\t" \
  "v_cvt_pk_bf16_f32 v82, v60, v61\n\t" \
  "v_cvt_pk_bf16_f32 v83, v62, v63\n\t" \
  "s_add_u32 %[n], %[n], 1\n\t" \
  "v_add_u32 v92, 0x1000, v92\n\t" \
  "s_lshl_b32 s94, %[n], 6\n\t" \
  "v_add_u32 v93, v94, v93\n\t" \
  "s_add_u32 s94, s92, s94\n\t" \
  "s_addc_u32 s95, s93, 0\n\t" \
  "s_setpc_b64 s[94:95]\n\t" \
  "Lb1%=:\n\t" \
  "v_exp_f32 v32, v32\n\t" \
  "v_exp_f32 v33, v33\n\t" \
  "s_waitcnt lgkmcnt(2)\n\t" \
  "v_exp_f32 v34, v34\n\t" \
  "v_mfma_f32_32x32x16_bf16 v[16:31], v[72:75], v[80:83], v[16:31]\n\t" \
  "ds_read_b128 v[72:75], v93 offset:512\n\t" \
  "v_exp_f32 v35, v35\n\t" \
  "v_cvt_pk_bf16_f32 v76, v32, v33\n\t" \
  "v_exp_f32 v36, v36\n\t" \
  "v_exp_f32 v37, v37\n\t" \
  "v_mfma_f32_32x32x16_bf16 v[48:63], v[64:67], v[88:91], 0\n\t" \
  "ds_read_b128 v[64:67], v92 offset:2048\n\t" \
  "v_cvt_pk_bf16_f32 v77, v34, v35\n\t" \
  "v_exp_f32 v38, v38\n\t" \
  "v_exp_f32 v39, v39\n\t" \
  "v_cvt_pk_bf16_f32 v78, v36, v37\n\t" \
  "v_exp_f32 v40, v40\n\t" \
  "v_exp_f32 v41, v41\n\t" \
  "v_cvt_pk_bf16_f32 v79, v38, v39\n\t" \
  "v_exp_f32 v42, v42\n\t" \
  "v_exp_f32 v43, v43\n\t" \
  "s_waitcnt lgkmcnt(2)\n\t" \
  "v_exp_f32 v44, v44\n\t" \
  "v_mfma_f32_32x32x16_bf16 v[0:15], v[68:71], v[76:79], v[0:15]\n\t" \
  "v_cvt_pk_bf16_f32 v80, v40, v41\n\t" \
  "v_exp_f32 v45, v45\n\t" \
  "v_cvt_pk_bf16_f32 v81, v42, v43\n\t" \
  "v_exp_f32 v46, v46\n\t" \
  "v_exp_f32 v47, v47\n\t" \
  "v_cvt_pk_bf16_f32 v82, v44, v45\n\t" \
  "v_cvt_pk_bf16_f32 v83, v46, v47\n\t" \
  "v_exp_f32 v48, v48\n\t" \
  "v_exp_f32 v49, v49\n\t" \
  "s_waitcnt lgkmcnt(1)\n\t" \
  "v_exp_f32 v50, v50\n\t" \
  "v_mfma_f32_32x32x16_bf16 v[0:15], v[72:75], v[80:83], v[0:15]\n\t" \
  "v_exp_f32 v51, v51\n\t" \
  "v_cvt_pk_bf16_f32 v76, v48, v49\n\t" \
  "v_exp_f32 v52, v52\n\t" \
  "v_exp_f32 v53, v53\n\t" \
  "s_waitcnt lgkmcnt(0)\n\t" \
  "v_exp_f32 v54, v54\n\t" \
  "v_mfma_f32_32x32x16_bf16 v[32:47], v[64:67], v[84:87], 0\n\t" \
  "v_cvt_pk_bf16_f32 v77, v50, v51\n\t" \
  "v_exp_f32 v55, v55\n\t" \
  "v_cvt_pk_bf16_f32 v78, v52, v53\n\t" \
  "v_exp_f32 v56, v56\n\t" \
  "v_exp_f32 v57, v57\n\t" \
  "v_cvt_pk_bf16_f32 v79, v54, v55\n\t" \
  "v_exp_f32 v58, v58\n\t" \
  "v_exp_f32 v59, v59\n\t" \
  "v_mfma_f32_32x32x16_bf16 v[16:31], v[68:71], v[76:79], v[16:31]\n\t" \
  "ds_read_b128 v[68:71], v93 offset:2048\n\t" \
  "v_cvt_pk_bf16_f32 v80, v56, v57\n\t" \
  "v_exp_f32 v60, v60\n\t" \
  "v_exp_f32 v61, v61\n\t" \
  "v_cvt_pk_bf16_f32 v81, v58, v59\n\t" \
  "v_exp_f32 v62, v62\n\t" \
  "v_exp_f32 v63, v63\n\t" \
  "v_cvt_pk_bf16_f32 v82, v60, v61\n\t" \
  "v_cvt_pk_bf16_f32 v83, v62, v63\n\t" \
  "v_exp_f32 v32, v32\n\t" \
  "v_exp_f32 v33, v33\n\t" \
  "s_waitcnt lgkmcnt(2)\n\t" \
  "v_exp_f32 v34, v34\n\t" \
  "v_mfma_f32_32x32x16_bf16 v[16:31], v[72:75], v[80:83], v[16:31]\n\t" \
  "ds_read_b128 v[72:75], v93 offset:2560\n\t" \
  "v_exp_f32 v35, v35\n\t" \
  "v_cvt_pk_bf16_f32 v76, v32, v33\n\t" \
  "v_exp_f32 v36, v36\n\t" \
  "v_exp_f32 v37, v37\n\t" \
  "v_mfma_f32_32x32x16_bf16 v[48:63], v[64:67], v[88:91], 0\n\t" \
  "ds_read_b128 v[64:67], v92 offset:4096\n\t" \
  "v_cvt_pk_bf16_f32 v77, v34, v35\n\t" \
  "v_exp_f32 v38, v38\n\t" \
  "v_exp_f32 v39, v39\n\t" \
  "v_cvt_pk_bf16_f32 v78, v36, v37\n\t" \
  "v_exp_f32 v40, v40\n\t" \
  "v_exp_f32 v41, v41\n\t" \
  "v_cvt_pk_bf16_f32 v79, v38, v39\n\t" \
  "v_exp_f32 v42, v42\n\t" \
  "v_exp_f32 v43, v43\n\t" \
  "s_waitcnt lgkmcnt(2)\n\t" \
  "v_exp_f32 v44, v44\n\t" \
  "v_mfma_f32_32x32x16_bf16 v[0:15], v[68:71], v[76:79], v[0:15]\n\t" \
  "v_cvt_pk_bf16_f32 v80, v40, v41\n\t" \
  "v_exp_f32 v45, v45\n\t" \
  "v_cvt_pk_bf16_f32 v81, v42, v43\n\t" \
  "v_exp_f32 v46, v46\n\t" \
  "v_exp_f32 v47, v47\n\t" \
  "v_cvt_pk_bf16_f32 v82, v44, v45\n\t" \
  "v_cvt_pk_bf16_f32 v83, v46, v47\n\t" \
  "v_exp_f32 v48, v48\n\t" \
  "v_exp_f32 v49, v49\n\t" \
  "s_waitcnt lgkmcnt(1)\n\t" \
  "v_exp_f32 v50, v50\n\t" \
  "v_mfma_f32_32x32x16_bf16 v[0:15], v[72:75], v[80:83], v[0:15]\n\t" \
  "v_exp_f32 v51, v51\n\t" \
  "v_cvt_pk_bf16_f32 v76, v48, v49\n\t" \
  "v_exp_f32 v52, v52\n\t" \
  "v_exp_f32 v53, v53\n\t" \
  "s_waitcnt lgkmcnt(0)\n\t" \
  "v_exp_f32 v54, v54\n\t" \
  "v_mfma_f32_32x32x16_bf16 v[32:47], v[64:67], v[84:87], 0\n\t" \
  "v_cvt_pk_bf16_f32 v77, v50, v51\n\t" \
  "v_exp_f32 v55, v55\n\t" \
  "v_cvt_pk_bf16_f32 v78, v52, v53\n\t" \
  "v_exp_f32 v56, v56\n\t" \
  "v_exp_f32 v57, v57\n\t" \
  "v_cvt_pk_bf16_f32 v79, v54, v55\n\t" \
  "v_exp_f32 v58, v58\n\t" \
  "v_exp_f32 v59, v59\n\t" \
  "v_mfma_f32_32x32x16_bf16 v[16:31], v[68:71], v[76:79], v[16:31]\n\t" \
  "ds_read_b128 v[68:71], v93 offset:4096\n\t" \
  "v_cvt_pk_bf16_f32 v80, v56, v57\n\t" \
  "v_exp_f32 v60, v60\n\t" \
  "v_exp_f32 v61, v61\n\t" \
  "v_cvt_pk_bf16_f32 v81, v58, v59\n\t" \
  "v_exp_f32 v62, v62\n\t" \
  "v_exp_f32 v63, v63\n\t" \
  "v_cvt_pk_bf16_f32 v82, v60, v61\n\t" \
  "v_cvt_pk_bf16_f32 v83, v62, v63\n\t" \
  "s_add_u32 %[n], %[n], 1\n\t" \
  "v_add_u32 v92, 0x1000, v92\n\t" \
  "s_lshl_b32 s94, %[n], 6\n\t" \
  "v_add_u32 v93, v94, v93\n\t" \
  "s_add_u32 s94, s92, s94\n\t" \
  "s_addc_u32 s95, s93, 0\n\t" \
  "s_setpc_b64 s[94:95]\n\t" \
  ".p2align 6\n\t" \
  "Ltab%=:\n\t" \
  "s_nop 0\n\t" \
  ".p2align 6\n\t" \
  "s_waitcnt vmcnt(2)\n\t" \
  "s_barrier\n\t" \
  "global_load_lds_dwordx4 v95, %[wsb] sc1\n\t" \
  "global_load_lds_dwordx4 v95, %[wsb] offset:1024 sc1\n\t" \
  "s_add_u32 m0, m0, 0x2000\n\t" \
  "v_add_u32 v95, 0x2000, v95\n\t" \
  "s_setpc_b64 s[90:91]\n\t" \
  ".p2align 6\n\t" \
  "global_load_lds_dwordx4 v95, %[wsb] sc1\n\t" \
  "global_load_lds_dwordx4 v95, %[wsb] offset:1024 sc1\n\t" \
  "s_add_u32 m0, m0, 0x2000\n\t" \
  "v_add_u32 v95, 0x2000, v95\n\t" \
  "s_setpc_b64 s[90:91]\n\t" \
  ".p2align 6\n\t" \
  "s_waitcnt vmcnt(2)\n\t" \
  "s_barrier\n\t" \
  "global_load_lds_dwordx4 v95, %[wsb] sc1\n\t" \
  "global_load_lds_dwordx4 v95, %[wsb] offset:1024 sc1\n\t" \
  "s_add_u32 m0, m0, 0x2000\n\t" \
  "v_add_u32 v95, 0x2000, v95\n\t" \
  "s_setpc_b64 s[90:91]\n\t" \
  ".p2align 6\n\t" \
  "global_load_lds_dwordx4 v95, %[wsb] sc1\n\t" \
  "global_load_lds_dwordx4 v95, %[wsb] offset:1024 sc1\n\t" \
  "s_add_u32 m0, m0, 0x2000\n\t" \
  "v_add_u32 v95, 0x2000, v95\n\t" \
  "s_setpc_b64 s[90:91]\n\t" \
  ".p2align 6\n\t" \
  "global_load_lds_dwordx4 v95, %[wsb] sc1\n\t" \
  "global_load_lds_dwordx4 v95, %[wsb] offset:1024 sc1\n\t" \
  "s_add_u32 m0, m0, 0x2000\n\t" \
  "v_add_u32 v95, 0x2000, v95\n\t" \
  "s_setpc_b64 s[90:91]\n\t" \
  ".p2align 6\n\t" \
  "s_setpc_b64 s[90:91]\n\t" \
  ".p2align 6\n\t" \
  "s_waitcnt vmcnt(0)\n\t" \
  "s_barrier\n\t" \
  "s_mov_b32 m0, %[m0s]\n\t" \
  "global_load_dwordx4 v[128:131], v96, %[wsb]\n\t" \
  "global_load_dwordx4 v[132:135], v96, %[wsb] offset:1024\n\t" \
  "v_add_u32 v96, 0x800, v96\n\t" \
  "global_load_dwordx4 v[192:195], v97, %[wsb] offset:0\n\t" \
  "global_load_dwordx4 v[196:199], v97, %[wsb] offset:1024\n\t" \
  "s_setpc_b64 s[90:91]\n\t" \
  ".p2align 6\n\t" \
  "global_load_dwordx4 v[136:139], v96, %[wsb]\n\t" \
  "global_load_dwordx4 v[140:143], v96, %[wsb] offset:1024\n\t" \
  "v_add_u32 v96, 0x800, v96\n\t" \
  "global_load_dwordx4 v[200:203], v97, %[wsb] offset:2048\n\t" \
  "global_load_dwordx4 v[204:207], v97, %[wsb] offset:3072\n\t" \
  "v_add_u32 v97, 0x80000, v97\n\t" \
  "s_setpc_b64 s[90:91]\n\t" \
  ".p2align 6\n\t" \
  "global_load_dwordx4 v[144:147], v96, %[wsb]\n\t" \
  "global_load_dwordx4 v[148:151], v96, %[wsb] offset:1024\n\t" \
  "v_add_u32 v96, 0x800, v96\n\t" \
  "global_load_dwordx4 v[208:211], v97, %[wsb] offset:0\n\t" \
  "global_load_dwordx4 v[212:215], v97, %[wsb] offset:1024\n\t" \
  "s_setpc_b64 s[90:91]\n\t" \
  ".p2align 6\n\t" \
  "global_load_dwordx4 v[152:155], v96, %[wsb]\n\t" \
  "global_load_dwordx4 v[156:159], v96, %[wsb] offset:1024\n\t" \
  "v_add_u32 v96, 0x800, v96\n\t" \
  "global_load_dwordx4 v[216:219], v97, %[wsb] offset:2048\n\t" \
  "global_load_dwordx4 v[220:223], v97, %[wsb] offset:3072\n\t" \
  "v_add_u32 v97, 0x80000, v97\n\t" \
  "s_setpc_b64 s[90:91]\n\t" \
  ".p2align 6\n\t" \
  "global_load_dwordx4 v[160:163], v96, %[wsb]\n\t" \
  "global_load_dwordx4 v[164:167], v96, %[wsb] offset:1024\n\t" \
  "v_add_u32 v96, 0x800, v96\n\t" \
  "global_load_dwordx4 v[224:227], v97, %[wsb] offset:0\n\t" \
  "global_load_dwordx4 v[228:231], v97, %[wsb] offset:1024\n\t" \
  "s_setpc_b64 s[90:91]\n\t" \
  ".p2align 6\n\t" \
  "global_load_dwordx4 v[168:171], v96, %[wsb]\n\t" \
  "global_load_dwordx4 v[172:175], v96, %[wsb] offset:1024\n\t" \
  "v_add_u32 v96, 0x800, v96\n\t" \
  "global_load_dwordx4 v[232:235], v97, %[wsb] offset:2048\n\t" \
  "global_load_dwordx4 v[236:239], v97, %[wsb] offset:3072\n\t" \
  "v_add_u32 v97, 0x80000, v97\n\t" \
  "s_setpc_b64 s[90:91]\n\t" \
  ".p2align 6\n\t" \
  "global_load_dwordx4 v[176:179], v96, %[wsb]\n\t" \
  "global_load_dwordx4 v[180:183], v96, %[wsb] offset:1024\n\t" \
  "v_add_u32 v96, 0x800, v96\n\t" \
  "s_setpc_b64 s[90:91]\n\t" \
  ".p2align 6\n\t" \
  "global_load_dwordx4 v[184:187], v96, %[wsb]\n\t" \
  "global_load_dwordx4 v[188:191], v96, %[wsb] offset:1024\n\t" \
  "v_add_u32 v96, 0x800, v96\n\t" \
  "s_setpc_b64 s[90:91]\n\t" \
  ".p2align 6\n\t" \
  "s_setpc_b64 s[90:91]\n\t" \
  ".p2align 6\n\t" \
  "s_waitcnt lgkmcnt(0)\n\t" \
  "v_mfma_f32_32x32x16_bf16 v[16:31], v[72:75], v[80:83], v[16:31]\n\t" \
  "s_setprio 0\n\t" \
  "s_waitcnt vmcnt(0)\n\t" \
  "s_nop 7\n\t" \
  "s_nop 7\n\t" \
  ""

template <class W, class H> DI f32x16 gemm_half(const LAS char* act, W wf, H hook) {
  f32x16 acc = zero16();
  u32x4 a[16];
#pragma unroll
  for (int i = 0; i < 4; ++i) a[i] = *(const LAS u32x4*)(act + i * 1024);
#pragma unroll
  for (int i = 0; i < 16; ++i) {
    SBAR();
    if (i + 4 < 16) a[i + 4] = *(const LAS u32x4*)(act + (i + 4) * 1024);
    acc = MFMA(wf(i), a[i], acc);
    SBAR();
    hook(i);
  }
  SBAR();
  return acc;
}
DI f32x4 lds2(const LAS char* p, int half) { return *(const LAS f32x4*)p + *(const LAS f32x4*)(p + half); }
DI void resid_epilogue(const f32x16& acc, u32x4& xr, rsrc_t rs, int xoff, int soff, uint16_t* out, bool final_, int tt, int ks, int lane) {
  const int r = lane & 31, h = lane >> 5;
  const u32x4 y = pack8(acc, 0);
#pragma unroll
  for (int j = 0; j < 4; ++j) xr[j] = cvtpk(bflo(xr[j]) + bflo(y[j]), bfhi(xr[j]) + bfhi(y[j]));
  xr = clean(xr);
  if (final_) {
    auto a = __builtin_amdgcn_permlane32_swap(xr[0], xr[2], false, false);
    auto c = __builtin_amdgcn_permlane32_swap(xr[1], xr[3], false, false);
    *(u32x4*)(out + (size_t)(tt * 32 + r) * DM + ks * 16 + 8 * h) = u32x4{a[0], c[0], a[1], c[1]};
  } else {
    st128(rs, lane * 16, xoff + (tt * 32 + ks) * 1024, xr);
    const float s = xhalf_sum(sum8(xr)), q = xhalf_sum(sq8(xr));
    if (h == 0) st64(rs, r * 8, soff + (tt * 32 + ks) * 256, u32x2{min(__float_as_uint(s), POISON - 1u), min(__float_as_uint(q), POISON - 1u)});
  }
}

#define KV_PUB 8
#define B_DRAIN 1
#define D_DRAIN 1
#define E_SLEEP 10
#define NEARLY 8
__global__ __launch_bounds__(512, 2) void k_mega(char* ws, uint16_t* out) {
  extern __shared__ __attribute__((aligned(16))) char smem_[];
  LAS char* smem = (LAS char*)smem_;
  const unsigned lds0 = (unsigned)(uintptr_t)smem;
  const rsrc_t rs = __builtin_amdgcn_make_buffer_rsrc(ws, 0, WS_END, 0x00020000);
  const int w_ = __builtin_amdgcn_readfirstlane(threadIdx.x >> 6);
#define IDS() int tid = threadIdx.x, w = w_, b = blockIdx.x; asm volatile("" : "+v"(tid)); asm volatile("" : "+s"(w), "+s"(b)); \
  const int lane = tid & 63, r = lane & 31, h = lane >> 5, w4 = w & 3, kh = w >> 2, xcd = b & 7, idx = b >> 3; \
  const int tt = xcd * 8 + (idx >> 2), cb = idx & 3, nt = cb * 4 + w4;                 \
  const int panel = (xcd & 3) * 4 + (idx & 3), hp = (xcd >> 2) * 8 + (idx >> 2);       \
  const unsigned voff = lane * 16, vrot = voff ^ (kh << 8); \
  (void)r; (void)h; (void)tt; (void)nt; (void)panel; (void)hp; (void)voff; (void)vrot
  u32x4 xr;
  STAMP_DECL;
  {
    IDS();
    STAMP(0);
#pragma unroll
    for (int t = 0; t < 3; ++t) glds4<false>(ws, OFF_W + ((t * 16 + hp) * 32 + 4 * w) * 1024 + voff, lds0 + L_WL + (t * 32 + 4 * w) * 1024);
    xr = ld128(rs, voff, OFF_XF + (tt * 32 + 2 * nt + kh) * 1024);
  }

#pragma unroll 1
  for (int L = 0; L < 2; ++L) {
    const int xa = OFF_XF + (2 * L) * 2048 * KB, xb = xa + 2048 * KB, xc = xb + 2048 * KB;
    const int sa_ = OFF_ST + (2 * L) * 512 * KB, sb_ = sa_ + 512 * KB, sc_ = sb_ + 512 * KB;
    const int pqo = OFF_PQ + L * 2048 * KB, ofo = OFF_OF + L * 2048 * KB, kvo = OFF_KV + L * 128 * KB;
    const int acco = OFF_ACC + L * ACC_N * 4;
    u32x4 wp[16];
    {
      IDS();
      const int t1 = panel * 4 + w4;
      constexpr int WIN = 4;
      u32x4 xf[16];
#define XLD(i) xf[i] = ld128(rs, vrot + ((i) & 3) * 1024, xa + (t1 * 32 + kh * 16 + ((i) & ~3)) * 1024)
#define XMAX(g) umax4(xf[4 * (g)], umax4(xf[4 * (g) + 1], umax4(xf[4 * (g) + 2], umax4(xf[4 * (g) + 3], 0u))))
      if (w == 4) glds1(ws, OFF_CS1 + hp * 1024 + voff, lds0 + 128 * KB);
#pragma clang loop unroll(disable)
      for (unsigned sp = 0; sp < (1u << 16); ++sp) {
        glds4<true>(ws, sa_ + t1 * 8192 + kh * 4096 + voff, lds0 + L_ACT + w4 * 8192 + kh * 4096);
#pragma unroll
        for (int i = 0; i < WIN; ++i) XLD(i);
        SBAR();
        unsigned m = XMAX(0);
        asm volatile("" : "+v"(m) :: "memory");
        if (L == 0) break;
#pragma unroll
        for (int k = 0; k < 4; ++k) { const u32x4 t = *(const LAS u32x4*)(smem + L_ACT + w4 * 8192 + kh * 4096 + k * 1024 + voff); m = umax4(t, m); }
        if (__all(m != POISON)) break;
        __builtin_amdgcn_s_sleep(2);
      }
      BAR();
      STAMP(1 + 16 * L);
      f32x16 aq = zero16(), ak = zero16(), av = zero16();
      const LAS char* wl = smem + L_WL + kh * 16384;
      u32x4 wf[2][3];
#define WLD(i) do { wf[(i) & 1][0] = *(const LAS u32x4*)(wl + vrot + (i) * 1024); wf[(i) & 1][1] = *(const LAS u32x4*)(wl + voff + (32 + (i)) * 1024); \
                    wf[(i) & 1][2] = *(const LAS u32x4*)(wl + voff + (64 + (i)) * 1024); } while (0)
      WLD(0);
      RowStat st;
      LAS float* sst = (LAS float*)(smem + 129 * KB + w * 256);
#pragma unroll
      for (int i = 0; i < 16; ++i) {
        SBAR();
        if (i < 15) WLD(i + 1);
        aq = MFMA(wf[i & 1][0], xf[i], aq);
        ak = MFMA(xf[i], wf[i & 1][1], ak);
        av = MFMA(xf[i], wf[i & 1][2], av);
        SBAR();
        if (2 * i + WIN < 16) { XLD(2 * i + WIN); XLD(2 * i + WIN + 1); }
        if (i >= 8) {
#pragma unroll
          for (int k = 2 * i - 16; k < 2 * i - 14; ++k) wp[k] = ldw(rs, vrot + (k & 3) * 1024, OFF_W + ((T_WOA + nt) * 32 + kh * 16 + (k & ~3)) * 1024);
        }
        if ((i & 3) == 3 && i < 15 && L == 1) VERIFY_FAST(XMAX((i >> 2) + 1), { XLD(i + 1); XLD(i + 2); XLD(i + 3); XLD(i + 4); });
        if (i == 1) {
          st = row_stats(smem + L_ACT + w4 * 8192, r ^ (kh << 4), h);
          if (h == 0) { sst[r] = st.mu; sst[32 + r] = st.rs; }
        }
      }
      SBAR();
      STAMP(2 + 16 * L);
      LAS char* exw = smem + (kh ? L_WL : 136 * KB) + w4 * 6144;
      LAS char* exr = smem + (kh ? 136 * KB : L_WL) + w4 * 6144;
      const LAS char* csl = smem + 128 * KB;
      const float csk = *(const LAS float*)(csl + 128 + r * 4) + *(const LAS float*)(csl + 640 + r * 4);
      const float csv = *(const LAS float*)(csl + 256 + r * 4) + *(const LAS float*)(csl + 768 + r * 4);
      f32x4 cs[2], mu[2], rr[2];
#pragma unroll
      for (int g = 0; g < 2; ++g) {
        cs[g] = lds2(csl + kh * 64 + g * 32 + h * 16, 512);
        mu[g] = *(const LAS f32x4*)(sst + 8 * g + 4 * h); rr[g] = *(const LAS f32x4*)(sst + 32 + 8 * g + 4 * h);
      }
      {
        const u32x4 pz = {POISON, POISON, POISON, POISON};
        st128(rs, voff, xb + (tt * 32 + 2 * nt + kh) * 1024, pz);
        st128(rs, voff, ofo + (tt * 32 + 2 * nt + kh) * 1024, pz);
        if (lane < 16) st128(rs, voff, sb_ + (tt * 32 + 2 * nt + kh) * 256, pz);
        if (L == 0) {
          st128(rs, voff, xc + (tt * 32 + 2 * nt + kh) * 1024, pz);
          if (lane < 16) st128(rs, voff, sc_ + (tt * 32 + 2 * nt + kh) * 256, pz);
        }
      }
      SBAR();
      if (kh == 1) BAR();
      put_hi(exw + vrot, aq); put_hi(exw + 2048 + voff, ak); put_hi(exw + 4096 + voff, av);
      if (kh == 0) BAR();
      BAR();
      STAMP(42);
      add_lo(exr + vrot, aq); add_lo(exr + 2048 + voff, ak); add_lo(exr + 4096 + voff, av);
      const float rsl = st.rs * LOG2E;
#pragma unroll
      for (int g = 0; g < 2; ++g) {
#pragma unroll
        for (int j = 0; j < 4; ++j) {
          ak[4 * g + j] = phi(ak[4 * g + j] - mu[g][j] * csk, rr[g][j], rr[g][j] * LOG2E);
          av[4 * g + j] = rr[g][j] * (av[4 * g + j] - mu[g][j] * csv);
        }
      }

      const u32x4 ones = {0x3f803f80u, 0x3f803f80u, 0x3f803f80u, 0x3f803f80u};
      const u32x4 pk = pack8(ak, 0);
      const f32x16 z = MFMA(pk, pack8(av, 0), zero16());
      const f32x16 zs = MFMA(pk, ones, zero16());
#pragma unroll
      for (int j = 0; j < 8; ++j) aq[j] = phi(aq[j] - st.mu * cs[j >> 2][j & 3], st.rs, rsl);
      st128(rs, vrot, pqo + (t1 * 32 + 2 * hp + kh) * 1024, pack8(aq, 0));
      {
        LAS float* red = (LAS float*)exr + h * 8 + (r & 15) * 16;
        if (r < 16) {
          *(LAS f32x4*)red = f32x4{z[0], z[1], z[2], z[3]}; *(LAS f32x4*)(red + 4) = f32x4{z[4], z[5], z[6], z[7]};
          if (r == 0) { *(LAS f32x4*)(red + 256) = f32x4{zs[0], zs[1], zs[2], zs[3]}; *(LAS f32x4*)(red + 260) = f32x4{zs[4], zs[5], zs[6], zs[7]}; }
          asm volatile("" ::: "memory");
        } else {
          *(LAS f32x4*)(red + 272) = f32x4{z[8], z[9], z[10], z[11]}; *(LAS f32x4*)(red + 276) = f32x4{z[12], z[13], z[14], z[15]};
          if (r == 16) { *(LAS f32x4*)(red + 528) = f32x4{zs[8], zs[9], zs[10], zs[11]}; *(LAS f32x4*)(red + 532) = f32x4{zs[12], zs[13], zs[14], zs[15]}; }
        }
      }
      STAMP(43);
      BAR();
#pragma unroll
      for (int i = 0; i < 2; ++i) {
        const int j = i * 512 + tid;
        if (j < 544) {
          const LAS float* ra = (const LAS float*)(smem + L_WL) + j;
          float t = 0.f;
#pragma unroll
          for (int k = 0; k < 4; ++k) t += ra[k * 1536] + ra[26 * 1024 + k * 1536];
          atomicAdd((float*)(ws + acco) + hp * 544 + j, t);
        }
      }
      STAMP(45);
      DRAIN();
      STAMP(3 + 16 * L);
      BAR();
      if (w == 0) {
        arrive(ws, CTR_A + L * 128 + (idx & 15), 16, 8, lane);
        poll(ws, CTR_A + L * 128 + xcd * 16, 16, 16, lane);
      }
      BAR();
      STAMP(4 + 16 * L);
    }
    u32x4 wkv[8];
    {
      IDS();
      u32x4 pq[4], kv[4][2];
      const int row = r < 16 ? r : 16;
#pragma unroll
      for (int i = 0; i < 4; ++i) {
        kv[i][0] = ld128(rs, row * 64 + h * 32 + i * 1088, acco + w * 4352);
        kv[i][1] = ld128(rs, row * 64 + h * 32 + i * 1088 + 16, acco + w * 4352);
        pq[i] = ld128(rs, voff + i * 1024, pqo + (tt * 32 + w * 4) * 1024);
      }
      SBAR();
      STAMP(46);
#pragma unroll
      for (int i = 0; i < 4; ++i) {
        const f32x4 k0 = __builtin_bit_cast(f32x4, kv[i][0]), k1 = __builtin_bit_cast(f32x4, kv[i][1]);
        const u32x4 a = {cvtpk(k0[0], k0[1]), cvtpk(k0[2], k0[3]), cvtpk(k1[0], k1[1]), cvtpk(k1[2], k1[3])};
        f32x16 d = MFMA(a, pq[i], zero16());
        const float inv = __builtin_amdgcn_rcpf(d[8] + 1e-6f);
#pragma unroll
        for (int j = 0; j < 8; ++j) d[j] *= inv;
        *(LAS u32x4*)(smem + L_ACT + (w * 4 + i) * 1024 + voff) = pack8(d, 0);
      }
      const int wqo = OFF_W + ((T_WQ + nt) * 32 + kh * 16) * 1024;
#pragma unroll
      for (int i = 0; i < 8; ++i) asm volatile("" : "=v"(wkv[i]));
      if (cb == 0 && kh == 1) {
#pragma unroll
        for (int i = 0; i < 8; ++i) wkv[i] = ldw(rs, (voff ^ (w4 == 1 ? 256 : 0)) + (i & 3) * 1024, OFF_W + (T_WKV * 32 + w4 * 8 + (i & 4)) * 1024);
      }
      if (w == 1) glds2(ws, OFF_CS3 + cb * 2048 + voff, lds0 + 56 * KB);
      STAMP(50);
      BAR();
      STAMP(5 + 16 * L);
      f32x16 acc = gemm_half(smem + L_ACT + kh * 16384 + voff, [&](int i) { return wp[i]; },
                             [&](int i) { if (i < NEARLY) wp[i] = ldw(rs, vrot + (i & 3) * 1024, wqo + (i & ~3) * 1024); });
      STAMP(6 + 16 * L);
      put_hi(smem + L_WL + w * 2048 + voff, acc);
      BAR();
      STAMP(51);
      add_lo(smem + L_WL + (w ^ 4) * 2048 + voff, acc);
      resid_epilogue(acc, xr, rs, xb, sb_, out, false, tt, 2 * nt + kh, lane);
      STAMP(7 + 16 * L);
    }
    u32x4 q0;
    {
      IDS();
      u32x4 xa4[5];
#define X3LD() do { _Pragma("unroll") for (int k = 0; k < 4; ++k) xa4[k] = ld128(rs, voff + k * 1024, xb + (tt * 32 + 4 * w) * 1024); \
                    xa4[4] = ld128(rs, voff, sb_ + tt * 8192 + w * 1024); } while (0)
      if (B_DRAIN) DRAIN();
      if (B_DRAIN > 1) BAR();
      X3LD();
      VERIFY(umax4(xa4[0], umax4(xa4[1], umax4(xa4[2], umax4(xa4[3], umax4(xa4[4], 0u))))), X3LD());
      STAMP(8 + 16 * L);
#pragma unroll
      for (int k = 0; k < 4; ++k) *(LAS u32x4*)(smem + L_ACT + (4 * w + k) * 1024 + voff) = xa4[k];
      *(LAS u32x4*)(smem + 48 * KB + w * 1024 + voff) = xa4[4];
      BAR();
      STAMP(9 + 16 * L);
      const LAS char* csl = smem + 56 * KB + h * 16;
      RowStat st;
      if (cb == 0) {
        f32x16 kacc = zero16();
        if (kh == 1) {
          u32x4 a[8];
#pragma unroll
          for (int i = 0; i < 8; ++i) a[i] = *(const LAS u32x4*)(smem + L_ACT + (w4 * 8 + i) * 1024 + voff);
          SBAR();
#pragma unroll
          for (int i = 0; i < 8; ++i) kacc = MFMA(wkv[i], a[i], kacc);
          st = row_stats(smem + 48 * KB, r, h);
          LAS char* kp = smem + 60 * KB + w4 * 4096 + voff;
          if (w4 != 0) { *(LAS f32x4*)(kp + (w4 == 1 ? 2048 : 0)) = f32x4{kacc[0], kacc[1], kacc[2], kacc[3]}; *(LAS f32x4*)(kp + (w4 == 1 ? 3072 : 1024)) = f32x4{kacc[4], kacc[5], kacc[6], kacc[7]}; }
          put_hi(kp + (w4 == 1 ? 0 : 2048), kacc);
        }
        BAR();
        if (w == 4 || w == 5) {
          const LAS char* kp = smem + 60 * KB + w4 * 2048 + voff;
          add_lo(kp + (w4 ^ 1) * 4096, kacc); add_lo(kp + 8192, kacc); add_lo(kp + 12288, kacc);
#pragma unroll
          for (int g = 0; g < 2; ++g) {
            const f32x4 c = lds2(csl + 512 + w4 * 64 + g * 32, 1024);
#pragma unroll
            for (int j = 0; j < 4; ++j) kacc[4 * g + j] = st.rs * (kacc[4 * g + j] - st.mu * c[j]);
          }
          u32x4 v = pack8(kacc, 0);
          if (w == 5) {
            const int t16 = r & 15, hh = (t16 >> 2) & 1, jj = ((t16 >> 3) << 2) | (t16 & 3);
            LAS uint16_t* vb = (LAS uint16_t*)(smem + 76 * KB + (r >> 4) * 512 + hh * 256) + jj;
#pragma unroll
            for (int j = 0; j < 8; ++j) vb[(8 * (j >> 2) + 4 * h + (j & 3)) * 8] = (uint16_t)((j & 1) ? (v[j >> 1] >> 16) : (v[j >> 1] & 0xffffu));
            v = *(const LAS u32x4*)(smem + 76 * KB + voff);
          }
          st128(rs, voff, kvo + tt * 2048 + w4 * 1024, v);
        }
      }
      f32x16 acc = gemm_half(smem + L_ACT + kh * 16384 + voff, [&](int i) { return wp[i]; },
                             [&](int i) {
        if (i + NEARLY < 16) wp[i + NEARLY] = ldw(rs, vrot + ((i + NEARLY) & 3) * 1024, OFF_W + ((T_WQ + nt) * 32 + kh * 16 + ((i + NEARLY) & ~3)) * 1024);
        if (i == 2 && !(cb == 0 && kh == 1)) st = row_stats(smem + 48 * KB, r, h);
        if (i == KV_PUB && cb == 0 && (w == 4 || w == 5)) {
          DRAIN();
          arrive(ws, CTR_C + L * 128 + (tt & 15), 16, 8, lane);
          STAMP(53);
        }
      });
      put_hi(smem + 80 * KB + w * 2048 + voff, acc);
      f32x4 c[2];
#pragma unroll
      for (int g = 0; g < 2; ++g) c[g] = lds2(csl + w4 * 128 + kh * 64 + g * 32, 1024);
      BAR();
      add_lo(smem + 80 * KB + (w ^ 4) * 2048 + voff, acc);
      const float sc = st.rs * (0.25f * LOG2E);
#pragma unroll
      for (int j = 0; j < 8; ++j) acc[j] = sc * (acc[j] - st.mu * c[j >> 2][j & 3]);
      q0 = pack8(acc, 0);
      *(LAS u32x4*)(smem + L_COMB + w * 3072 + voff) = q0;
      if (tid >= 64 && tid < 74) {
        const int s5 = (tid - 64) >> 1, o5 = s5 == 0 ? 0 : (s5 == 1 ? 512 : (s5 == 2 ? 2048 : (s5 == 3 ? 2560 : 4096)));
        const unsigned c = (tid & 1) ? 0u : 0x3f803f80u;
        *(LAS u32x4*)(smem + L_CONST + o5 + (tid & 1) * 16) = u32x4{c, c, c, c};
      }
      PIN(q0);
      STAMP(10 + 16 * L);
      if (w == 0) poll(ws, CTR_C + L * 128 + xcd * 16, 16, 8, lane);
      BAR();
      STAMP(11 + 16 * L);
    }
    u32x16 wo0, wo1, wo2, wo3, wn0, wn1, wn2;
    {
      IDS();
      f32x16 oa = zero16(), ob = zero16(), sa, sb;
      u32x4 kf, v0, v1, p0, p1 = {0u, 0u, 0u, 0u};
      unsigned ka = lds0 + kh * 65536 + lane * 16;
      unsigned va = r < 16 ? lds0 + kh * 65536 + 1024 + (h * 16 + r) * 16 : lds0 + L_CONST + ((r == 16 || r == 20) ? 0 : 16);
      const unsigned vinc = r < 16 ? 4096u : 0u;
      const unsigned o0 = kh * 65536 + w4 * 2048;
      unsigned ga = kvo + o0 + voff, wo = OFF_W + ((T_WOB + nt) * 32 + kh * 16) * 1024 + vrot, n, m0s;
      unsigned wn = OFF_W + (hp * 32 + 4 * w) * 1024 + voff;
      const u32x4 q1 = *(const LAS u32x4*)(smem + L_COMB + (w ^ 4) * 3072 + voff);
      asm volatile(ATTN_ASM
                   : "+{v[0:15]}"(oa), "+{v[16:31]}"(ob), "=&{v[32:47]}"(sa), "=&{v[48:63]}"(sb), "=&{v[64:67]}"(kf), "=&{v[68:71]}"(v0),
                     "=&{v[72:75]}"(v1), "=&{v[76:79]}"(p0), "+{v[80:83]}"(p1), "+{v92}"(ka), "+{v93}"(va), "+{v95}"(ga), "+{v96}"(wo),
                     "=&{v[128:143]}"(wo0), "=&{v[144:159]}"(wo1), "=&{v[160:175]}"(wo2), "=&{v[176:191]}"(wo3),
                     "+{v97}"(wn), "=&{v[192:207]}"(wn0), "=&{v[208:223]}"(wn1), "=&{v[224:239]}"(wn2),
                     [n] "=&s"(n), [m0s] "=&s"(m0s)
                   : "{v[84:87]}"(q0), "{v[88:91]}"(q1), "{v94}"(vinc), [wsb] "s"(ws), [kh] "s"(kh), [m0a] "s"(__builtin_amdgcn_readfirstlane(lds0 + o0))
                   : "memory", "scc", "s90", "s91", "s92", "s93", "s94", "s95");
      STAMP(12 + 16 * L);
      LAS char* cw = smem + L_COMB + w * 3072 + voff;
      const LAS char* cr = smem + L_COMB + (w ^ 4) * 3072 + voff;
      *(LAS f32x4*)cw = f32x4{ob[0], ob[1], ob[2], ob[3]}; *(LAS f32x4*)(cw + 1024) = f32x4{ob[4], ob[5], ob[6], ob[7]};
      *(LAS float*)(cw + 2048) = ob[8];
      BAR();
      STAMP(54);
      add_lo(cr, oa);
      oa[8] += *(const LAS float*)(cr + 2048);
      float mx = fmaxf(fmaxf(fabsf(oa[0]), fabsf(oa[1])), fabsf(oa[2]));
      mx = fmaxf(fmaxf(mx, fabsf(oa[3])), fabsf(oa[4])); mx = fmaxf(fmaxf(mx, fabsf(oa[5])), fabsf(oa[6]));
      const float sum8 = ((oa[0] + oa[1]) + (oa[2] + oa[3])) + ((oa[4] + oa[5]) + (oa[6] + oa[7]));
      const bool ok = fmaxf(fmaxf(mx, fabsf(oa[7])), oa[8]) < 1.0e36f && oa[8] > 1.0e-27f && sum8 == sum8;
      if (__builtin_expect(!__all(ok), 0)) {
        const LAS char* kb0 = smem + lane * 16;
        const LAS char* vb0 = r < 16 ? smem + 1024 + (h * 16 + r) * 16 : smem + L_CONST + ((r == 16 || r == 20) ? 0 : 16);
        const int vstep = r < 16 ? 2048 : 0;
        float m = -INFINITY;
        oa = zero16();
#pragma unroll 1
        for (int t = 0; t < 64; ++t) {
          f32x16 s = MFMA(*(const LAS u32x4*)(kb0 + t * 2048), q0, zero16());
          const float mn = fmaxf(m, xhalf_max(max16(s)));
          const float al = __builtin_amdgcn_exp2f(m - mn);
          m = mn;
#pragma unroll
          for (int i = 0; i < 16; ++i) { oa[i] *= al; s[i] = __builtin_amdgcn_exp2f(s[i] - mn); }
          oa = MFMA(*(const LAS u32x4*)(vb0 + t * vstep), pack8(s, 0), oa);
          oa = MFMA(*(const LAS u32x4*)(vb0 + t * vstep + 512), pack8(s, 1), oa);
        }
      }
      const float inv = __builtin_amdgcn_rcpf(oa[8]);
#pragma unroll
      for (int i = 0; i < 8; ++i) oa[i] *= inv;
      st128(rs, voff, ofo + (tt * 32 + 2 * nt + kh) * 1024, clean(pack8(oa, 0)));
      STAMP(55);
    }
    {
      IDS();
      u32x4 o4[4];
#define O5LD() do { _Pragma("unroll") for (int k = 0; k < 4; ++k) o4[k] = ld128(rs, voff + k * 1024, ofo + (tt * 32 + 4 * w) * 1024); } while (0)
      if (D_DRAIN) DRAIN();
      BAR();
      O5LD();
      SBAR();
      if (L == 0) {
#pragma unroll
        for (int i = 0; i < 4; ++i) {
          LAS char* d = smem + L_WL + (4 * w + i) * 1024 + voff;
          *(LAS u32x4*)d = u32x4{wn0[4 * i], wn0[4 * i + 1], wn0[4 * i + 2], wn0[4 * i + 3]};
          *(LAS u32x4*)(d + 32768) = u32x4{wn1[4 * i], wn1[4 * i + 1], wn1[4 * i + 2], wn1[4 * i + 3]};
          *(LAS u32x4*)(d + 65536) = u32x4{wn2[4 * i], wn2[4 * i + 1], wn2[4 * i + 2], wn2[4 * i + 3]};
        }
      }
      SBAR();
      VERIFY(umax4(o4[0], umax4(o4[1], umax4(o4[2], umax4(o4[3], 0u)))), O5LD());
      STAMP(13 + 16 * L);
#pragma unroll
      for (int k = 0; k < 4; ++k) *(LAS u32x4*)(smem + L_ACT + (4 * w + k) * 1024 + voff) = o4[k];
      BAR();
      STAMP(14 + 16 * L);
      f32x16 acc = gemm_half(smem + L_ACT + kh * 16384 + voff, [&](int i) {
        const u32x16& v = i < 4 ? wo0 : (i < 8 ? wo1 : (i < 12 ? wo2 : wo3));
        return u32x4{v[4 * (i & 3)], v[4 * (i & 3) + 1], v[4 * (i & 3) + 2], v[4 * (i & 3) + 3]};
      }, [](int) {});
      put_hi(smem + 136 * KB + w * 2048 + voff, acc);
      BAR();
      add_lo(smem + 136 * KB + (w ^ 4) * 2048 + voff, acc);
      resid_epilogue(acc, xr, rs, xc, sc_, out, L == 1, tt, 2 * nt + kh, lane);
      STAMP(15 + 16 * L);
      if (L == 0) {
        DRAIN();
        if (E_SLEEP) __builtin_amdgcn_s_sleep(E_SLEEP);
        STAMP(16);
      }
    }
  }
  STAMP(60);
  STAMP_FLUSH();
}

extern "C" void kernel_launch(void* const* d_in, const int* in_sizes, int n_in, void* d_out, int out_size, void* d_ws,
                              size_t ws_size, hipStream_t stream) {
  char* ws = (char*)d_ws;
  (void)hipFuncSetAttribute((const void*)k_mega, hipFuncAttributeMaxDynamicSharedMemorySize, 160 * KB);
  k_prep<<<PREP_BLOCKS, 256, 0, stream>>>((const uint16_t*)d_in[0], (const uint16_t*)d_in[1], (const uint16_t*)d_in[2], (const uint16_t*)d_in[4],
                                          (const uint16_t*)d_in[5], (const uint16_t*)d_in[6], (const uint16_t*)d_in[7], ws);
  k_mega<<<256, 512, 160 * KB, stream>>>(ws, (uint16_t*)d_out);
}
```

```cpp
#include <hip/hip_runtime.h>
#include <hip/hip_bf16.h>
#include <stdint.h>

typedef __attribute__((ext_vector_type(8))) short bf16x8;
typedef __attribute__((ext_vector_type(4))) short s16x4;
typedef __attribute__((ext_vector_type(16))) float f32x16;
typedef __attribute__((ext_vector_type(4))) float f32x4;
typedef __attribute__((ext_vector_type(2))) float f32x2;
typedef __attribute__((ext_vector_type(16))) unsigned u32x16;
typedef __attribute__((ext_vector_type(4))) unsigned u32x4;
typedef __attribute__((ext_vector_type(2))) unsigned u32x2;
typedef __attribute__((ext_vector_type(2))) __bf16 bf16x2_t;
#define LAS __attribute__((address_space(3)))
#define GAS __attribute__((address_space(1)))
typedef GAS unsigned gu32;
#define DI __device__ __forceinline__
#define MFMA(a, b, c) __builtin_amdgcn_mfma_f32_32x32x16_bf16(__builtin_bit_cast(bf16x8, (a)), __builtin_bit_cast(bf16x8, (b)), (c), 0, 0, 0)
#define RLX_AGENT __ATOMIC_RELAXED, __HIP_MEMORY_SCOPE_AGENT

#define STAMP(slot) do {} while (0)
#define STAMP_DECL do {} while (0)
#define STAMP_FLUSH() do {} while (0)

constexpr int SEQ = 2048, DM = 512, NH = 32;
constexpr float LOG2E = 1.4426950408889634f;

constexpr int KB = 1024;
constexpr int OFF_CTR = 0;
constexpr int CTR_A = 0, CTR_TMO = 256, CTR_LINES = 264;
constexpr int OFF_ACC = OFF_CTR + CTR_LINES * 128;
constexpr int ACC_N = NH * 17 * 16;
constexpr int ZERO_BYTES = CTR_LINES * 128 + 2 * ACC_N * 4;
constexpr int OFF_W = 256 * KB;
constexpr int T_WOA = 48, T_WQ = 64, T_WKV = 80, T_WOB = 81;
constexpr int OFF_CS1 = OFF_W + 97 * 32 * KB;
constexpr int OFF_CS3 = OFF_CS1 + 16 * KB;
constexpr int OFF_XF = OFF_CS3 + 8 * KB;
constexpr int OFF_ST = OFF_XF + 4 * 2048 * KB;
constexpr int OFF_PQ = OFF_ST + 4 * 64 * KB;
constexpr int OFF_OF = OFF_PQ + 2 * 2048 * KB;
constexpr int OFF_KV = OFF_OF + 2 * 2048 * KB;
constexpr int WS_END = OFF_KV + 2 * 128 * KB;

constexpr int L_ACT = 0, L_WL = 32 * KB;
constexpr int L_CONST = 128 * KB, L_COMB = 136 * KB;

DI unsigned cvtpk(float lo, float hi) {
  f32x2 v = {lo, hi};
  return __builtin_bit_cast(unsigned, __builtin_convertvector(v, bf16x2_t));
}
DI float bflo(unsigned u) { return __uint_as_float(u << 16); }
DI float bfhi(unsigned u) { return __uint_as_float(u & 0xffff0000u); }
DI float bfround(float x) { return bflo(cvtpk(x, x)); }
DI u32x4 pack8(const f32x16& x, int s) {
  u32x4 p;
  p[0] = cvtpk(x[8 * s + 0], x[8 * s + 1]); p[1] = cvtpk(x[8 * s + 2], x[8 * s + 3]);
  p[2] = cvtpk(x[8 * s + 4], x[8 * s + 5]); p[3] = cvtpk(x[8 * s + 6], x[8 * s + 7]);
  return p;
}
DI float xhalf_sum(float v) {
  auto r = __builtin_amdgcn_permlane32_swap(__float_as_uint(v), __float_as_uint(v), false, false);
  return __uint_as_float(r[0]) + __uint_as_float(r[1]);
}
DI float xhalf_max(float v) {
  auto r = __builtin_amdgcn_permlane32_swap(__float_as_uint(v), __float_as_uint(v), false, false);
  return fmaxf(__uint_as_float(r[0]), __uint_as_float(r[1]));
}
DI f32x16 zero16() { f32x16 z; _Pragma("unroll") for (int i = 0; i < 16; ++i) z[i] = 0.f; return z; }
DI float phi(float y, float rs, float rsl) {
  return fmaxf(__builtin_fmaf(y, rs, 1e-6f), 1e-6f) + __builtin_amdgcn_fmed3f(__builtin_amdgcn_exp2f(y * rsl), 0.f, 1.f);
}

#define GLDS_HEAD "s_nop 4\n\ts_mov_b32 %0, m0\n\ts_mov_b32 m0, %3\n\ts_nop 0\n\t"
#define GLDS_OPS : "=&s"(sv) : "v"(voff), "s"(sbase), "s"(lds_base) : "memory"
template <bool SC1> DI void glds4(const char* sbase, unsigned voff, unsigned lds_base) {
  unsigned sv;
  lds_base = __builtin_amdgcn_readfirstlane(lds_base);
  if (SC1) asm volatile(GLDS_HEAD "global_load_lds_dwordx4 %1, %2 sc1\n\tglobal_load_lds_dwordx4 %1, %2 offset:1024 sc1\n\t"
               "global_load_lds_dwordx4 %1, %2 offset:2048 sc1\n\tglobal_load_lds_dwordx4 %1, %2 offset:3072 sc1\n\ts_mov_b32 m0, %0" GLDS_OPS);
  else asm volatile(GLDS_HEAD "global_load_lds_dwordx4 %1, %2\n\tglobal_load_lds_dwordx4 %1, %2 offset:1024\n\t"
               "global_load_lds_dwordx4 %1, %2 offset:2048\n\tglobal_load_lds_dwordx4 %1, %2 offset:3072\n\ts_mov_b32 m0, %0" GLDS_OPS);
}
DI void glds2(const char* sbase, unsigned voff, unsigned lds_base) {
  unsigned sv;
  lds_base = __builtin_amdgcn_readfirstlane(lds_base);
  asm volatile(GLDS_HEAD "global_load_lds_dwordx4 %1, %2\n\tglobal_load_lds_dwordx4 %1, %2 offset:1024\n\ts_mov_b32 m0, %0" GLDS_OPS);
}
DI void glds1(const char* sbase, unsigned voff, unsigned lds_base) {
  unsigned sv;
  lds_base = __builtin_amdgcn_readfirstlane(lds_base);
  asm volatile(GLDS_HEAD "global_load_lds_dwordx4 %1, %2\n\ts_mov_b32 m0, %0" GLDS_OPS);
}
#define BAR() asm volatile("s_waitcnt lgkmcnt(0)\n\ts_barrier" ::: "memory")
#define DRAIN() asm volatile("s_waitcnt vmcnt(0)" ::: "memory")
#define PIN(x) asm volatile("" : "+v"(x))
#define SBAR() __builtin_amdgcn_sched_barrier(0)

typedef __amdgpu_buffer_rsrc_t rsrc_t;
DI u32x4 ld128(rsrc_t rs, int voff, int soff) { return __builtin_amdgcn_raw_buffer_load_b128(rs, voff, soff, 16); }
DI u32x2 ld64(rsrc_t rs, int voff, int soff) { return __builtin_amdgcn_raw_buffer_load_b64(rs, voff, soff, 16); }
DI u32x4 ldw(rsrc_t rs, int voff, int soff) { return __builtin_amdgcn_raw_buffer_load_b128(rs, voff, soff, 0); }
DI float ldwf(rsrc_t rs, int voff, int soff) { return __uint_as_float(__builtin_amdgcn_raw_buffer_load_b32(rs, voff, soff, 0)); }
DI void st128(rsrc_t rs, int voff, int soff, u32x4 v) { __builtin_amdgcn_raw_buffer_store_b128(v, rs, voff + soff, 0, 16); }
DI void st64(rsrc_t rs, int voff, int soff, u32x2 v) { __builtin_amdgcn_raw_buffer_store_b64(v, rs, voff, soff, 16); }

DI void poll(char* ws, int line0, int n, unsigned want, int lane) {
  gu32* c = (gu32*)(ws + OFF_CTR) + (line0 + (lane < n ? lane : 0)) * 32;
  gu32* tmo = (gu32*)(ws + OFF_CTR) + CTR_TMO * 32;
#pragma clang loop unroll(disable)
  for (unsigned spins = 0;;) {
    if (__all(__hip_atomic_load(c, RLX_AGENT) >= want)) break;
    __builtin_amdgcn_s_sleep(1);
    if (++spins > (1u << 17)) { if (lane == 0) __hip_atomic_store(tmo, 1u, RLX_AGENT); break; }
    if ((spins & 255u) == 0u && __builtin_amdgcn_readfirstlane(__hip_atomic_load(tmo, RLX_AGENT))) break;
  }
  __builtin_amdgcn_fence(__ATOMIC_ACQUIRE, "wavefront");
}
DI void arrive(char* ws, int line0, int stride, int n, int lane) {
  if (lane < n) __hip_atomic_fetch_add((gu32*)(ws + OFF_CTR) + (line0 + lane * stride) * 32, 1u, RLX_AGENT);
}

constexpr unsigned POISON = 0xffffffffu;
DI u32x4 clean(u32x4 v) { _Pragma("unroll") for (int j = 0; j < 4; ++j) v[j] = min(v[j], POISON - 1u); return v; }
DI unsigned umax4(const u32x4& v, unsigned m) { return max(max(max(v[0], v[1]), m), max(v[2], v[3])); }
#define VERIFY(MAXEXPR, RELOAD) do { _Pragma("clang loop unroll(disable)") for (unsigned sp_ = 0; !__all((MAXEXPR) != POISON) && sp_ < (1u << 16); ++sp_) { \
    __builtin_amdgcn_s_sleep(2); asm volatile("" ::: "memory"); RELOAD; } } while (0)

#define VERIFY_FAST(MAXEXPR, RELOAD) do { if (__builtin_expect(!__all((MAXEXPR) != POISON), 0)) { \
    _Pragma("clang loop unroll(disable)") for (unsigned sp_ = 0; sp_ < (1u << 16); ++sp_) { \
      __builtin_amdgcn_s_sleep(2); asm volatile("" ::: "memory"); RELOAD; __builtin_amdgcn_s_waitcnt(0x0F70); if (__all((MAXEXPR) != POISON)) break; } } } while (0)

struct RowStat { float mu, rs; };
DI RowStat finish_stats(float s, float q) {
  const float mean = s * (1.f / DM);
  const float var = fmaxf(q * (1.f / DM) - mean * mean, 0.f);
  const float sd = bfround(__builtin_amdgcn_sqrtf(bfround(bfround(var) + 1e-5f)));
  RowStat o; o.mu = bfround(mean); o.rs = __builtin_amdgcn_rcpf(sd); return o;
}
DI RowStat row_stats(const u32x4& p) {
  return finish_stats(xhalf_sum(__uint_as_float(p[0]) + __uint_as_float(p[2])), xhalf_sum(__uint_as_float(p[1]) + __uint_as_float(p[3])));
}
DI float dot2(unsigned a, unsigned b, float c) {
  return __builtin_amdgcn_fdot2_f32_bf16(__builtin_bit_cast(bf16x2_t, a), __builtin_bit_cast(bf16x2_t, b), c, false);
}
DI float sum8(const u32x4& v) { float c = 0.f; _Pragma("unroll") for (int j = 0; j < 4; ++j) c = dot2(v[j], 0x3f803f80u, c); return c; }
DI float sq8(const u32x4& v) { float c = 0.f; _Pragma("unroll") for (int j = 0; j < 4; ++j) c = dot2(v[j], v[j], c); return c; }
template <int NW> DI void publish_stats(const LAS char* part, rsrc_t rs, int off, int lane) {
  f32x2 t = {0.f, 0.f};
#pragma unroll
  for (int i = 0; i < NW / 2; ++i) t += *(const LAS f32x2*)(part + ((lane >> 5) * (NW / 2) + i) * 256 + (lane & 31) * 8);
  const float s = xhalf_sum(t[0]), q = xhalf_sum(t[1]);
  if (lane < 32) st64(rs, lane * 32, off, u32x2{min(__float_as_uint(s), POISON - 1u), min(__float_as_uint(q), POISON - 1u)});
}
DI void put16(LAS char* p, const f32x16& a, int lane) {
#pragma unroll
  for (int g = 0; g < 4; ++g) *(LAS f32x4*)(p + g * 1024 + lane * 16) = f32x4{a[4 * g], a[4 * g + 1], a[4 * g + 2], a[4 * g + 3]};
}
DI void add16(const LAS char* p, f32x16& a, int lane) {
#pragma unroll
  for (int g = 0; g < 4; ++g) {
    const f32x4 v = *(const LAS f32x4*)(p + g * 1024 + lane * 16);
#pragma unroll
    for (int j = 0; j < 4; ++j) a[4 * g + j] += v[j];
  }
}
DI void put_hi(LAS char* p, const f32x16& a) {
  *(LAS f32x4*)p = f32x4{a[8], a[9], a[10], a[11]}; *(LAS f32x4*)(p + 1024) = f32x4{a[12], a[13], a[14], a[15]};
}
DI void add_lo(const LAS char* p, f32x16& a) {
  const f32x4 u = *(const LAS f32x4*)p, v = *(const LAS f32x4*)(p + 1024);
#pragma unroll
  for (int j = 0; j < 4; ++j) { a[j] += u[j]; a[4 + j] += v[j]; }
}
DI float max16(const f32x16& s) {
  float a = fmaxf(fmaxf(s[0], s[1]), s[2]), b = fmaxf(fmaxf(s[3], s[4]), s[5]);
  a = fmaxf(fmaxf(a, s[6]), s[7]); b = fmaxf(fmaxf(b, s[8]), s[9]);
  a = fmaxf(fmaxf(a, s[10]), s[11]); b = fmaxf(fmaxf(b, s[12]), s[13]);
  a = fmaxf(fmaxf(a, s[14]), s[15]);
  return fmaxf(a, b);
}

constexpr int PREP_BLOCKS = 714;
DI u32x4 tr_frag(const LAS char* a, int second) {
  const u32x2 lo = __builtin_bit_cast(u32x2, __builtin_amdgcn_ds_read_tr16_b64_v4i16((LAS s16x4*)a));
  const u32x2 hi = __builtin_bit_cast(u32x2, __builtin_amdgcn_ds_read_tr16_b64_v4i16((LAS s16x4*)(a + second)));
  return u32x4{lo[0], lo[1], hi[0], hi[1]};
}
__global__ __launch_bounds__(256) void k_prep(const uint16_t* __restrict__ x0, const uint16_t* __restrict__ wqkv,
                                              const uint16_t* __restrict__ woa, const uint16_t* __restrict__ wq,
                                              const uint16_t* __restrict__ wk, const uint16_t* __restrict__ wv,
                                              const uint16_t* __restrict__ wob, char* __restrict__ ws) {
  __shared__ __attribute__((aligned(16))) char tile_[9728];
  LAS char* smem = (LAS char*)tile_;
  asm volatile("" :: "s"(x0), "s"(wqkv), "s"(woa), "s"(wq), "s"(wk), "s"(wv), "s"(wob), "s"(ws));
  const rsrc_t rs = __builtin_amdgcn_make_buffer_rsrc(ws, 0, WS_END, 0x00020000);
  const int b = blockIdx.x, tid = threadIdx.x, lane = tid & 63, r = lane & 31, h = lane >> 5;
  const int w = __builtin_amdgcn_readfirstlane(tid >> 6), voff = lane * 16, c8 = tid & 7;
  if (b < 458) {
    const int cu = b < 66 ? ((b >> 1) < 24 ? b >> 1 : ((b >> 1) < 32 ? (b >> 1) + 8 : 48)) : (b - 66) >> 3;
    const uint16_t* src; int ld = 512;
    if (cu < 24) { src = wqkv + cu * 64 + c8 * 8; ld = 1536; }
    else if (cu == 48) { src = ((c8 & 2) ? wv : wk) + (c8 & 1) * 8; ld = 16; }
    else src = (cu < 32 ? woa : (cu < 40 ? wq : wob)) + (cu & 7) * 64 + c8 * 8;
    const int wt = cu < 40 ? 2 * cu : (cu < 48 ? 2 * cu + 1 : T_WKV);
    if (b < 66) {
      const int kb = b & 1;
      u32x4 v[8];
#pragma unroll
      for (int i = 0; i < 8; ++i) v[i] = *(const u32x4*)(src + (size_t)(kb * 256 + i * 32 + (tid >> 3)) * ld);
      f32x4 s0 = {0.f, 0.f, 0.f, 0.f}, s1 = s0;
#pragma unroll
      for (int i = 0; i < 8; ++i) {
        s0 += f32x4{bflo(v[i][0]), bfhi(v[i][0]), bflo(v[i][1]), bfhi(v[i][1])};
        s1 += f32x4{bflo(v[i][2]), bfhi(v[i][2]), bflo(v[i][3]), bfhi(v[i][3])};
      }
      *(LAS f32x4*)(smem + (tid >> 3) * 272 + c8 * 32) = s0; *(LAS f32x4*)(smem + (tid >> 3) * 272 + c8 * 32 + 16) = s1;
      __syncthreads();
      if (w == 0) {
        float t = 0.f;
#pragma unroll
        for (int i = 0; i < 32; ++i) t += *(const LAS float*)(smem + i * 272 + lane * 4);
        float* cs1 = (float*)(ws + OFF_CS1); float* cs3 = (float*)(ws + OFF_CS3);
        if (cu < 24) cs1[((wt & 15) + h) * 256 + kb * 128 + (wt >> 4) * 32 + r] = t;
        else if (cu < 40) cs3[((wt - T_WQ) >> 2) * 512 + kb * 256 + ((wt - T_WQ) & 3) * 32 + lane] = t;
        else if (h == 0) {
#pragma unroll
          for (int i = 0; i < 4; ++i) cs3[i * 512 + kb * 256 + 128 + r] = t;
        }
      }
    } else {
      const int k8 = (b - 66) & 7;
      u32x4 v[2];
#pragma unroll
      for (int i = 0; i < 2; ++i) v[i] = *(const u32x4*)(src + (size_t)(k8 * 64 + i * 32 + (tid >> 3)) * ld);
#pragma unroll
      for (int i = 0; i < 2; ++i) *(LAS u32x4*)(smem + (i * 32 + (tid >> 3)) * 144 + c8 * 16) = v[i];
      __syncthreads();
#pragma unroll
      for (int t = 0; t < 2; ++t) {
        const u32x4 f = tr_frag(smem + (w * 16 + 4 * h + ((lane & 15) >> 2)) * 144 + t * 64 + ((lane >> 4) & 1) * 32 + (lane & 3) * 8, 8 * 144);
        if (t == 0 || cu < 48) st128(rs, voff, OFF_W + ((wt + t) * 32 + k8 * 4 + w) * 1024, f);
      }
    }
  } else {
    const int u = b - 458, tt = u >> 2, kq = u & 3;
    u32x4 v[2];
#pragma unroll
    for (int i = 0; i < 2; ++i) v[i] = *(const u32x4*)(x0 + (size_t)(tt * 32 + i * 16 + (tid >> 4)) * DM + kq * 128 + (tid & 15) * 8);
    if (u * 256 + tid < ZERO_BYTES / 16) st128(rs, (u * 256 + tid) * 16, OFF_CTR, u32x4{0u, 0u, 0u, 0u});
#pragma unroll
    for (int i = 0; i < 2; ++i) *(LAS u32x4*)(smem + (i * 16 + (tid >> 4)) * 272 + (tid & 15) * 16) = v[i];
    __syncthreads();
    float s = 0.f, q = 0.f;
#pragma unroll
    for (int i = 0; i < 2; ++i) {
      const LAS char* xs = smem + r * 272 + (2 * w + i) * 32 + h * 8;
      const u32x2 e0 = *(const LAS u32x2*)xs, e1 = *(const LAS u32x2*)(xs + 16);
      const u32x4 f = clean(u32x4{e0[0], e0[1], e1[0], e1[1]});
      st128(rs, voff, OFF_XF + (tt * 32 + kq * 8 + 2 * w + i) * 1024, f);
      s += sum8(f); q += sq8(f);
    }
    s = xhalf_sum(s); q = xhalf_sum(q);
    if (h == 0) *(LAS f32x2*)(smem + 8704 + w * 256 + r * 8) = f32x2{s, q};
    __syncthreads();
    if (w == 0) publish_stats<4>(smem + 8704, rs, OFF_ST + tt * 1024 + kq * 8, lane);
  }
}

#define ATTN_ASM \
  "s_nop 4\n\t" \
  "s_mov_b32 %[m0s], m0\n\t" \
  "s_mov_b32 s96, %[m0a]\n\t" \
  "s_mov_b32 m0, s96\n\t" \
  "s_add_u32 s96, s96, 0x2000\n\t" \
  "global_load_lds_dwordx4 v95, %[wsb] sc1\n\t" \
  "global_load_lds_dwordx4 v95, %[wsb] offset:1024 sc1\n\t" \
  "v_add_u32 v95, 0x2000, v95\n\t" \
  "s_mov_b32 m0, s96\n\t" \
  "s_add_u32 s96, s96, 0x2000\n\t" \
  "global_load_lds_dwordx4 v95, %[wsb] sc1\n\t" \
  "global_load_lds_dwordx4 v95, %[wsb] offset:1024 sc1\n\t" \
  "v_add_u32 v95, 0x2000, v95\n\t" \
  "s_mov_b32 m0, s96\n\t" \
  "s_add_u32 s96, s96, 0x2000\n\t" \
  "global_load_lds_dwordx4 v95, %[wsb] sc1\n\t" \
  "global_load_lds_dwordx4 v95, %[wsb] offset:1024 sc1\n\t" \
  "v_add_u32 v95, 0x2000, v95\n\t" \
  "s_waitcnt vmcnt(2)\n\t" \
  "s_mov_b32 s97, 0\n\t" \
  "Lca%=:\n\t" \
  "ds_read_b128 v[48:51], v98 offset:0\n\t" \
  "ds_read_b128 v[52:55], v98 offset:1024\n\t" \
  "ds_read_b128 v[56:59], v98 offset:8192\n\t" \
  "ds_read_b128 v[60:63], v98 offset:9216\n\t" \
  "s_waitcnt lgkmcnt(0)\n\t" \
  "v_max3_u32 v48, v48, v49, v50\n\t" \
  "v_max3_u32 v48, v51, v52, v48\n\t" \
  "v_max3_u32 v48, v53, v54, v48\n\t" \
  "v_max3_u32 v48, v55, v56, v48\n\t" \
  "v_max3_u32 v48, v57, v58, v48\n\t" \
  "v_max3_u32 v48, v59, v60, v48\n\t" \
  "v_max3_u32 v48, v61, v62, v48\n\t" \
  "v_max3_u32 v48, v63, v50, v48\n\t" \
  "v_cmp_eq_u32 vcc, -1, v48\n\t" \
  "s_nop 1\n\t" \
  "s_cbranch_vccz Loa%=\n\t" \
  "s_add_u32 s97, s97, 1\n\t" \
  "s_cmp_gt_u32 s97, 0xffff\n\t" \
  "s_cbranch_scc1 Loa%=\n\t" \
  "s_sleep 2\n\t" \
  "s_add_u32 m0, %[m0a], 0\n\t" \
  "v_subrev_u32 v48, 24576, v95\n\t" \
  "global_load_lds_dwordx4 v48, %[wsb] sc1\n\t" \
  "global_load_lds_dwordx4 v48, %[wsb] offset:1024 sc1\n\t" \
  "s_add_u32 m0, %[m0a], 8192\n\t" \
  "v_subrev_u32 v48, 16384, v95\n\t" \
  "global_load_lds_dwordx4 v48, %[wsb] sc1\n\t" \
  "global_load_lds_dwordx4 v48, %[wsb] offset:1024 sc1\n\t" \
  "s_waitcnt vmcnt(0)\n\t" \
  "s_branch Lca%=\n\t" \
  "Loa%=:\n\t" \
  "s_barrier\n\t" \
  "ds_read_b128 v[64:67], v92 offset:0\n\t" \
  "ds_read_b128 v[68:71], v93 offset:0\n\t" \
  "ds_read_b128 v[72:75], v93 offset:0\n\t" \
  "s_mov_b32 %[n], 0\n\t" \
  "s_waitcnt lgkmcnt(0)\n\t" \
  "v_mfma_f32_32x32x16_bf16 v[32:47], v[64:67], v[84:87], 0\n\t" \
  "s_getpc_b64 s[92:93]\n\t" \
  "Lpc%=:\n\t" \
  "s_add_u32 s90, s92, Lb0%=-Lpc%=\n\t" \
  "s_addc_u32 s91, s93, 0\n\t" \
  "s_cmp_eq_u32 %[kh], 0\n\t" \
  "s_cselect_b32 s94, 0, Lb1%=-Lb0%=\n\t" \
  "s_add_u32 s90, s90, s94\n\t" \
  "s_addc_u32 s91, s91, 0\n\t" \
  "s_add_u32 s92, s92, Ltab%=-Lpc%=\n\t" \
  "s_addc_u32 s93, s93, 0\n\t" \
  "s_setprio 1\n\t" \
  "s_nop 5\n\t" \
  "s_setpc_b64 s[90:91]\n\t" \
  "Lb0%=:\n\t" \
  "s_setprio 2\n\t" \
  "v_exp_f32 v32, v32\n\t" \
  "v_exp_f32 v33, v33\n\t" \
  "s_waitcnt lgkmcnt(2)\n\t" \
  "v_exp_f32 v34, v34\n\t" \
  "v_mfma_f32_32x32x16_bf16 v[16:31], v[72:75], v[80:83], v[16:31]\n\t" \
  "ds_read_b128 v[72:75], v93 offset:512\n\t" \
  "v_exp_f32 v35, v35\n\t" \
  "v_cvt_pk_bf16_f32 v76, v32, v33\n\t" \
  "v_exp_f32 v36, v36\n\t" \
  "v_exp_f32 v37, v37\n\t" \
  "v_mfma_f32_32x32x16_bf16 v[48:63], v[64:67], v[88:91], 0\n\t" \
  "ds_read_b128 v[64:67], v92 offset:2048\n\t" \
  "v_cvt_pk_bf16_f32 v77, v34, v35\n\t" \
  "v_exp_f32 v38, v38\n\t" \
  "v_exp_f32 v39, v39\n\t" \
  "v_cvt_pk_bf16_f32 v78, v36, v37\n\t" \
  "v_exp_f32 v40, v40\n\t" \
  "v_exp_f32 v41, v41\n\t" \
  "v_cvt_pk_bf16_f32 v79, v38, v39\n\t" \
  "v_exp_f32 v42, v42\n\t" \
  "v_exp_f32 v43, v43\n\t" \
  "s_waitcnt lgkmcnt(2)\n\t" \
  "v_exp_f32 v44, v44\n\t" \
  "v_mfma_f32_32x32x16_bf16 v[0:15], v[68:71], v[76:79], v[0:15]\n\t" \
  "v_cvt_pk_bf16_f32 v80, v40, v41\n\t" \
  "v_exp_f32 v45, v45\n\t" \
  "v_cvt_pk_bf16_f32 v81, v42, v43\n\t" \
  "v_exp_f32 v46, v46\n\t" \
  "v_exp_f32 v47, v47\n\t" \
  "v_cvt_pk_bf16_f32 v82, v44, v45\n\t" \
  "v_cvt_pk_bf16_f32 v83, v46, v47\n\t" \
  "v_exp_f32 v48, v48\n\t" \
  "v_exp_f32 v49, v49\n\t" \
  "s_waitcnt lgkmcnt(1)\n\t" \
  "v_exp_f32 v50, v50\n\t" \
  "v_mfma_f32_32x32x16_bf16 v[0:15], v[72:75], v[80:83], v[0:15]\n\t" \
  "v_exp_f32 v51, v51\n\t" \
  "v_cvt_pk_bf16_f32 v76, v48, v49\n\t" \
  "v_exp_f32 v52, v52\n\t" \
  "v_exp_f32 v53, v53\n\t" \
  "s_waitcnt lgkmcnt(0)\n\t" \
  "v_exp_f32 v54, v54\n\t" \
  "v_mfma_f32_32x32x16_bf16 v[32:47], v[64:67], v[84:87], 0\n\t" \
  "v_cvt_pk_bf16_f32 v77, v50, v51\n\t" \
  "v_exp_f32 v55, v55\n\t" \
  "v_cvt_pk_bf16_f32 v78, v52, v53\n\t" \
  "v_exp_f32 v56, v56\n\t" \
  "v_exp_f32 v57, v57\n\t" \
  "v_cvt_pk_bf16_f32 v79, v54, v55\n\t" \
  "v_exp_f32 v58, v58\n\t" \
  "v_exp_f32 v59, v59\n\t" \
  "v_mfma_f32_32x32x16_bf16 v[16:31], v[68:71], v[76:79], v[16:31]\n\t" \
  "ds_read_b128 v[68:71], v93 offset:2048\n\t" \
  "v_cvt_pk_bf16_f32 v80, v56, v57\n\t" \
  "v_exp_f32 v60, v60\n\t" \
  "v_exp_f32 v61, v61\n\t" \
  "v_cvt_pk_bf16_f32 v81, v58, v59\n\t" \
  "v_exp_f32 v62, v62\n\t" \
  "v_exp_f32 v63, v63\n\t" \
  "v_cvt_pk_bf16_f32 v82, v60, v61\n\t" \
  "v_cvt_pk_bf16_f32 v83, v62, v63\n\t" \
  "v_exp_f32 v32, v32\n\t" \
  "v_exp_f32 v33, v33\n\t" \
  "s_waitcnt lgkmcnt(2)\n\t" \
  "v_exp_f32 v34, v34\n\t" \
  "v_mfma_f32_32x32x16_bf16 v[16:31], v[72:75], v[80:83], v[16:31]\n\t" \
  "ds_read_b128 v[72:75], v93 offset:2560\n\t" \
  "v_exp_f32 v35, v35\n\t" \
  "v_cvt_pk_bf16_f32 v76, v32, v33\n\t" \
  "v_exp_f32 v36, v36\n\t" \
  "v_exp_f32 v37, v37\n\t" \
  "v_mfma_f32_32x32x16_bf16 v[48:63], v[64:67], v[88:91], 0\n\t" \
  "ds_read_b128 v[64:67], v92 offset:4096\n\t" \
  "v_cvt_pk_bf16_f32 v77, v34, v35\n\t" \
  "v_exp_f32 v38, v38\n\t" \
  "v_exp_f32 v39, v39\n\t" \
  "v_cvt_pk_bf16_f32 v78, v36, v37\n\t" \
  "v_exp_f32 v40, v40\n\t" \
  "v_exp_f32 v41, v41\n\t" \
  "v_cvt_pk_bf16_f32 v79, v38, v39\n\t" \
  "v_exp_f32 v42, v42\n\t" \
  "v_exp_f32 v43, v43\n\t" \
  "s_waitcnt lgkmcnt(2)\n\t" \
  "v_exp_f32 v44, v44\n\t" \
  "v_mfma_f32_32x32x16_bf16 v[0:15], v[68:71], v[76:79], v[0:15]\n\t" \
  "v_cvt_pk_bf16_f32 v80, v40, v41\n\t" \
  "v_exp_f32 v45, v45\n\t" \
  "v_cvt_pk_bf16_f32 v81, v42, v43\n\t" \
  "v_exp_f32 v46, v46\n\t" \
  "v_exp_f32 v47, v47\n\t" \
  "v_cvt_pk_bf16_f32 v82, v44, v45\n\t" \
  "v_cvt_pk_bf16_f32 v83, v46, v47\n\t" \
  "s_setprio 0\n\t" \
  "v_exp_f32 v48, v48\n\t" \
  "v_exp_f32 v49, v49\n\t" \
  "s_waitcnt lgkmcnt(1)\n\t" \
  "v_exp_f32 v50, v50\n\t" \
  "v_mfma_f32_32x32x16_bf16 v[0:15], v[72:75], v[80:83], v[0:15]\n\t" \
  "v_exp_f32 v51, v51\n\t" \
  "v_cvt_pk_bf16_f32 v76, v48, v49\n\t" \
  "v_exp_f32 v52, v52\n\t" \
  "v_exp_f32 v53, v53\n\t" \
  "s_waitcnt lgkmcnt(0)\n\t" \
  "v_exp_f32 v54, v54\n\t" \
  "v_mfma_f32_32x32x16_bf16 v[32:47], v[64:67], v[84:87], 0\n\t" \
  "v_cvt_pk_bf16_f32 v77, v50, v51\n\t" \
  "v_exp_f32 v55, v55\n\t" \
  "v_cvt_pk_bf16_f32 v78, v52, v53\n\t" \
  "v_exp_f32 v56, v56\n\t" \
  "v_exp_f32 v57, v57\n\t" \
  "v_cvt_pk_bf16_f32 v79, v54, v55\n\t" \
  "v_exp_f32 v58, v58\n\t" \
  "v_exp_f32 v59, v59\n\t" \
  "v_mfma_f32_32x32x16_bf16 v[16:31], v[68:71], v[76:79], v[16:31]\n\t" \
  "ds_read_b128 v[68:71], v93 offset:4096\n\t" \
  "v_cvt_pk_bf16_f32 v80, v56, v57\n\t" \
  "v_exp_f32 v60, v60\n\t" \
  "v_exp_f32 v61, v61\n\t" \
  "v_cvt_pk_bf16_f32 v81, v58, v59\n\t" \
  "v_exp_f32 v62, v62\n\t" \
  "v_exp_f32 v63, v63\n\t" \
  "v_cvt_pk_bf16_f32 v82, v60, v61\n\t" \
  "v_cvt_pk_bf16_f32 v83, v62, v63\n\t" \
  "s_add_u32 %[n], %[n], 1\n\t" \
  "v_add_u32 v92, 0x1000, v92\n\t" \
  "s_lshl_b32 s94, %[n], 6\n\t" \
  "v_add_u32 v93, v94, v93\n\t" \
  "s_add_u32 s94, s92, s94\n\t" \
  "s_addc_u32 s95, s93, 0\n\t" \
  "s_setpc_b64 s[94:95]\n\t" \
  "Lb1%=:\n\t" \
  "v_exp_f32 v32, v32\n\t" \
  "v_exp_f32 v33, v33\n\t" \
  "s_waitcnt lgkmcnt(2)\n\t" \
  "v_exp_f32 v34, v34\n\t" \
  "v_mfma_f32_32x32x16_bf16 v[16:31], v[72:75], v[80:83], v[16:31]\n\t" \
  "ds_read_b128 v[72:75], v93 offset:512\n\t" \
  "v_exp_f32 v35, v35\n\t" \
  "v_cvt_pk_bf16_f32 v76, v32, v33\n\t" \
  "v_exp_f32 v36, v36\n\t" \
  "v_exp_f32 v37, v37\n\t" \
  "v_mfma_f32_32x32x16_bf16 v[48:63], v[64:67], v[88:91], 0\n\t" \
  "ds_read_b128 v[64:67], v92 offset:2048\n\t" \
  "v_cvt_pk_bf16_f32 v77, v34, v35\n\t" \
  "v_exp_f32 v38, v38\n\t" \
  "v_exp_f32 v39, v39\n\t" \
  "v_cvt_pk_bf16_f32 v78, v36, v37\n\t" \
  "v_exp_f32 v40, v40\n\t" \
  "v_exp_f32 v41, v41\n\t" \
  "v_cvt_pk_bf16_f32 v79, v38, v39\n\t" \
  "v_exp_f32 v42, v42\n\t" \
  "v_exp_f32 v43, v43\n\t" \
  "s_waitcnt lgkmcnt(2)\n\t" \
  "v_exp_f32 v44, v44\n\t" \
  "v_mfma_f32_32x32x16_bf16 v[0:15], v[68:71], v[76:79], v[0:15]\n\t" \
  "v_cvt_pk_bf16_f32 v80, v40, v41\n\t" \
  "v_exp_f32 v45, v45\n\t" \
  "v_cvt_pk_bf16_f32 v81, v42, v43\n\t" \
  "v_exp_f32 v46, v46\n\t" \
  "v_exp_f32 v47, v47\n\t" \
  "v_cvt_pk_bf16_f32 v82, v44, v45\n\t" \
  "v_cvt_pk_bf16_f32 v83, v46, v47\n\t" \
  "v_exp_f32 v48, v48\n\t" \
  "v_exp_f32 v49, v49\n\t" \
  "s_waitcnt lgkmcnt(1)\n\t" \
  "v_exp_f32 v50, v50\n\t" \
  "v_mfma_f32_32x32x16_bf16 v[0:15], v[72:75], v[80:83], v[0:15]\n\t" \
  "v_exp_f32 v51, v51\n\t" \
  "v_cvt_pk_bf16_f32 v76, v48, v49\n\t" \
  "v_exp_f32 v52, v52\n\t" \
  "v_exp_f32 v53, v53\n\t" \
  "s_waitcnt lgkmcnt(0)\n\t" \
  "v_exp_f32 v54, v54\n\t" \
  "v_mfma_f32_32x32x16_bf16 v[32:47], v[64:67], v[84:87], 0\n\t" \
  "v_cvt_pk_bf16_f32 v77, v50, v51\n\t" \
  "v_exp_f32 v55, v55\n\t" \
  "v_cvt_pk_bf16_f32 v78, v52, v53\n\t" \
  "v_exp_f32 v56, v56\n\t" \
  "v_exp_f32 v57, v57\n\t" \
  "v_cvt_pk_bf16_f32 v79, v54, v55\n\t" \
  "v_exp_f32 v58, v58\n\t" \
  "v_exp_f32 v59, v59\n\t" \
  "v_mfma_f32_32x32x16_bf16 v[16:31], v[68:71], v[76:79], v[16:31]\n\t" \
  "ds_read_b128 v[68:71], v93 offset:2048\n\t" \
  "v_cvt_pk_bf16_f32 v80, v56, v57\n\t" \
  "v_exp_f32 v60, v60\n\t" \
  "v_exp_f32 v61, v61\n\t" \
  "v_cvt_pk_bf16_f32 v81, v58, v59\n\t" \
  "v_exp_f32 v62, v62\n\t" \
  "v_exp_f32 v63, v63\n\t" \
  "v_cvt_pk_bf16_f32 v82, v60, v61\n\t" \
  "v_cvt_pk_bf16_f32 v83, v62, v63\n\t" \
  "v_exp_f32 v32, v32\n\t" \
  "v_exp_f32 v33, v33\n\t" \
  "s_waitcnt lgkmcnt(2)\n\t" \
  "v_exp_f32 v34, v34\n\t" \
  "v_mfma_f32_32x32x16_bf16 v[16:31], v[72:75], v[80:83], v[16:31]\n\t" \
  "ds_read_b128 v[72:75], v93 offset:2560\n\t" \
  "v_exp_f32 v35, v35\n\t" \
  "v_cvt_pk_bf16_f32 v76, v32, v33\n\t" \
  "v_exp_f32 v36, v36\n\t" \
  "v_exp_f32 v37, v37\n\t" \
  "v_mfma_f32_32x32x16_bf16 v[48:63], v[64:67], v[88:91], 0\n\t" \
  "ds_read_b128 v[64:67], v92 offset:4096\n\t" \
  "v_cvt_pk_bf16_f32 v77, v34, v35\n\t" \
  "v_exp_f32 v38, v38\n\t" \
  "v_exp_f32 v39, v39\n\t" \
  "v_cvt_pk_bf16_f32 v78, v36, v37\n\t" \
  "v_exp_f32 v40, v40\n\t" \
  "v_exp_f32 v41, v41\n\t" \
  "v_cvt_pk_bf16_f32 v79, v38, v39\n\t" \
  "v_exp_f32 v42, v42\n\t" \
  "v_exp_f32 v43, v43\n\t" \
  "s_waitcnt lgkmcnt(2)\n\t" \
  "v_exp_f32 v44, v44\n\t" \
  "v_mfma_f32_32x32x16_bf16 v[0:15], v[68:71], v[76:79], v[0:15]\n\t" \
  "v_cvt_pk_bf16_f32 v80, v40, v41\n\t" \
  "v_exp_f32 v45, v45\n\t" \
  "v_cvt_pk_bf16_f32 v81, v42, v43\n\t" \
  "v_exp_f32 v46, v46\n\t" \
  "v_exp_f32 v47, v47\n\t" \
  "v_cvt_pk_bf16_f32 v82, v44, v45\n\t" \
  "v_cvt_pk_bf16_f32 v83, v46, v47\n\t" \
  "v_exp_f32 v48, v48\n\t" \
  "v_exp_f32 v49, v49\n\t" \
  "s_waitcnt lgkmcnt(1)\n\t" \
  "v_exp_f32 v50, v50\n\t" \
  "v_mfma_f32_32x32x16_bf16 v[0:15], v[72:75], v[80:83], v[0:15]\n\t" \
  "v_exp_f32 v51, v51\n\t" \
  "v_cvt_pk_bf16_f32 v76, v48, v49\n\t" \
  "v_exp_f32 v52, v52\n\t" \
  "v_exp_f32 v53, v53\n\t" \
  "s_waitcnt lgkmcnt(0)\n\t" \
  "v_exp_f32 v54, v54\n\t" \
  "v_mfma_f32_32x32x16_bf16 v[32:47], v[64:67], v[84:87], 0\n\t" \
  "v_cvt_pk_bf16_f32 v77, v50, v51\n\t" \
  "v_exp_f32 v55, v55\n\t" \
  "v_cvt_pk_bf16_f32 v78, v52, v53\n\t" \
  "v_exp_f32 v56, v56\n\t" \
  "v_exp_f32 v57, v57\n\t" \
  "v_cvt_pk_bf16_f32 v79, v54, v55\n\t" \
  "v_exp_f32 v58, v58\n\t" \
  "v_exp_f32 v59, v59\n\t" \
  "v_mfma_f32_32x32x16_bf16 v[16:31], v[68:71], v[76:79], v[16:31]\n\t" \
  "ds_read_b128 v[68:71], v93 offset:4096\n\t" \
  "v_cvt_pk_bf16_f32 v80, v56, v57\n\t" \
  "v_exp_f32 v60, v60\n\t" \
  "v_exp_f32 v61, v61\n\t" \
  "v_cvt_pk_bf16_f32 v81, v58, v59\n\t" \
  "v_exp_f32 v62, v62\n\t" \
  "v_exp_f32 v63, v63\n\t" \
  "v_cvt_pk_bf16_f32 v82, v60, v61\n\t" \
  "v_cvt_pk_bf16_f32 v83, v62, v63\n\t" \
  "s_add_u32 %[n], %[n], 1\n\t" \
  "v_add_u32 v92, 0x1000, v92\n\t" \
  "s_lshl_b32 s94, %[n], 6\n\t" \
  "v_add_u32 v93, v94, v93\n\t" \
  "s_add_u32 s94, s92, s94\n\t" \
  "s_addc_u32 s95, s93, 0\n\t" \
  "s_setpc_b64 s[94:95]\n\t" \
  ".p2align 6\n\t" \
  "Ltab%=:\n\t" \
  "s_nop 0\n\t" \
  ".p2align 6\n\t" \
  "s_branch Lh1%=\n\t" \
  ".p2align 6\n\t" \
  "s_branch Lh2%=\n\t" \
  ".p2align 6\n\t" \
  "s_barrier\n\t" \
  "s_mov_b32 m0, s96\n\t" \
  "s_add_u32 s96, s96, 0x2000\n\t" \
  "global_load_lds_dwordx4 v95, %[wsb] sc1\n\t" \
  "global_load_lds_dwordx4 v95, %[wsb] offset:1024 sc1\n\t" \
  "v_add_u32 v95, 0x2000, v95\n\t" \
  "s_setpc_b64 s[90:91]\n\t" \
  ".p2align 6\n\t" \
  "s_branch Lh4%=\n\t" \
  ".p2align 6\n\t" \
  "s_branch Lh5%=\n\t" \
  ".p2align 6\n\t" \
  "s_setpc_b64 s[90:91]\n\t" \
  ".p2align 6\n\t" \
  "s_barrier\n\t" \
  "s_mov_b32 m0, %[m0s]\n\t" \
  "global_load_dwordx4 v[128:131], v96, %[wsb]\n\t" \
  "global_load_dwordx4 v[132:135], v96, %[wsb] offset:1024\n\t" \
  "v_add_u32 v96, 0x800, v96\n\t" \
  "global_load_dwordx4 v[192:195], v97, %[wsb] offset:0\n\t" \
  "global_load_dwordx4 v[196:199], v97, %[wsb] offset:1024\n\t" \
  "s_setpc_b64 s[90:91]\n\t" \
  ".p2align 6\n\t" \
  "global_load_dwordx4 v[136:139], v96, %[wsb]\n\t" \
  "global_load_dwordx4 v[140:143], v96, %[wsb] offset:1024\n\t" \
  "v_add_u32 v96, 0x800, v96\n\t" \
  "global_load_dwordx4 v[200:203], v97, %[wsb] offset:2048\n\t" \
  "global_load_dwordx4 v[204:207], v97, %[wsb] offset:3072\n\t" \
  "v_add_u32 v97, 0x80000, v97\n\t" \
  "s_setpc_b64 s[90:91]\n\t" \
  ".p2align 6\n\t" \
  "global_load_dwordx4 v[144:147], v96, %[wsb]\n\t" \
  "global_load_dwordx4 v[148:151], v96, %[wsb] offset:1024\n\t" \
  "v_add_u32 v96, 0x800, v96\n\t" \
  "global_load_dwordx4 v[208:211], v97, %[wsb] offset:0\n\t" \
  "global_load_dwordx4 v[212:215], v97, %[wsb] offset:1024\n\t" \
  "s_setpc_b64 s[90:91]\n\t" \
  ".p2align 6\n\t" \
  "global_load_dwordx4 v[152:155], v96, %[wsb]\n\t" \
  "global_load_dwordx4 v[156:159], v96, %[wsb] offset:1024\n\t" \
  "v_add_u32 v96, 0x800, v96\n\t" \
  "global_load_dwordx4 v[216:219], v97, %[wsb] offset:2048\n\t" \
  "global_load_dwordx4 v[220:223], v97, %[wsb] offset:3072\n\t" \
  "v_add_u32 v97, 0x80000, v97\n\t" \
  "s_setpc_b64 s[90:91]\n\t" \
  ".p2align 6\n\t" \
  "global_load_dwordx4 v[160:163], v96, %[wsb]\n\t" \
  "global_load_dwordx4 v[164:167], v96, %[wsb] offset:1024\n\t" \
  "v_add_u32 v96, 0x800, v96\n\t" \
  "global_load_dwordx4 v[224:227], v97, %[wsb] offset:0\n\t" \
  "global_load_dwordx4 v[228:231], v97, %[wsb] offset:1024\n\t" \
  "s_setpc_b64 s[90:91]\n\t" \
  ".p2align 6\n\t" \
  "global_load_dwordx4 v[168:171], v96, %[wsb]\n\t" \
  "global_load_dwordx4 v[172:175], v96, %[wsb] offset:1024\n\t" \
  "v_add_u32 v96, 0x800, v96\n\t" \
  "global_load_dwordx4 v[232:235], v97, %[wsb] offset:2048\n\t" \
  "global_load_dwordx4 v[236:239], v97, %[wsb] offset:3072\n\t" \
  "v_add_u32 v97, 0x80000, v97\n\t" \
  "s_setpc_b64 s[90:91]\n\t" \
  ".p2align 6\n\t" \
  "global_load_dwordx4 v[176:179], v96, %[wsb]\n\t" \
  "global_load_dwordx4 v[180:183], v96, %[wsb] offset:1024\n\t" \
  "v_add_u32 v96, 0x800, v96\n\t" \
  "s_setpc_b64 s[90:91]\n\t" \
  ".p2align 6\n\t" \
  "global_load_dwordx4 v[184:187], v96, %[wsb]\n\t" \
  "global_load_dwordx4 v[188:191], v96, %[wsb] offset:1024\n\t" \
  "v_add_u32 v96, 0x800, v96\n\t" \
  "s_setpc_b64 s[90:91]\n\t" \
  ".p2align 6\n\t" \
  "s_setpc_b64 s[90:91]\n\t" \
  ".p2align 6\n\t" \
  "s_branch Lend%=\n\t" \
  "Lh1%=:\n\t" \
  "s_waitcnt vmcnt(0)\n\t" \
  "s_mov_b32 s97, 0\n\t" \
  "Lch1%=:\n\t" \
  "ds_read_b128 v[48:51], v98 offset:16384\n\t" \
  "ds_read_b128 v[52:55], v98 offset:17408\n\t" \
  "s_waitcnt lgkmcnt(0)\n\t" \
  "v_max3_u32 v48, v48, v49, v50\n\t" \
  "v_max3_u32 v48, v51, v52, v48\n\t" \
  "v_max3_u32 v48, v53, v54, v48\n\t" \
  "v_max3_u32 v48, v55, v50, v48\n\t" \
  "v_cmp_eq_u32 vcc, -1, v48\n\t" \
  "s_nop 1\n\t" \
  "s_cbranch_vccz Loh1%=\n\t" \
  "s_add_u32 s97, s97, 1\n\t" \
  "s_cmp_gt_u32 s97, 0xffff\n\t" \
  "s_cbranch_scc1 Loh1%=\n\t" \
  "s_sleep 2\n\t" \
  "s_add_u32 m0, %[m0a], 16384\n\t" \
  "v_subrev_u32 v48, 8192, v95\n\t" \
  "global_load_lds_dwordx4 v48, %[wsb] sc1\n\t" \
  "global_load_lds_dwordx4 v48, %[wsb] offset:1024 sc1\n\t" \
  "s_waitcnt vmcnt(0)\n\t" \
  "s_branch Lch1%=\n\t" \
  "Loh1%=:\n\t" \
  "s_mov_b32 m0, s96\n\t" \
  "s_add_u32 s96, s96, 0x2000\n\t" \
  "global_load_lds_dwordx4 v95, %[wsb] sc1\n\t" \
  "global_load_lds_dwordx4 v95, %[wsb] offset:1024 sc1\n\t" \
  "v_add_u32 v95, 0x2000, v95\n\t" \
  "s_mov_b32 m0, s96\n\t" \
  "s_add_u32 s96, s96, 0x2000\n\t" \
  "global_load_lds_dwordx4 v95, %[wsb] sc1\n\t" \
  "global_load_lds_dwordx4 v95, %[wsb] offset:1024 sc1\n\t" \
  "v_add_u32 v95, 0x2000, v95\n\t" \
  "s_setpc_b64 s[90:91]\n\t" \
  "Lh2%=:\n\t" \
  "s_waitcnt vmcnt(0)\n\t" \
  "s_mov_b32 s97, 0\n\t" \
  "Lch2%=:\n\t" \
  "ds_read_b128 v[48:51], v98 offset:24576\n\t" \
  "ds_read_b128 v[52:55], v98 offset:25600\n\t" \
  "ds_read_b128 v[56:59], v98 offset:32768\n\t" \
  "ds_read_b128 v[60:63], v98 offset:33792\n\t" \
  "s_waitcnt lgkmcnt(0)\n\t" \
  "v_max3_u32 v48, v48, v49, v50\n\t" \
  "v_max3_u32 v48, v51, v52, v48\n\t" \
  "v_max3_u32 v48, v53, v54, v48\n\t" \
  "v_max3_u32 v48, v55, v56, v48\n\t" \
  "v_max3_u32 v48, v57, v58, v48\n\t" \
  "v_max3_u32 v48, v59, v60, v48\n\t" \
  "v_max3_u32 v48, v61, v62, v48\n\t" \
  "v_max3_u32 v48, v63, v50, v48\n\t" \
  "v_cmp_eq_u32 vcc, -1, v48\n\t" \
  "s_nop 1\n\t" \
  "s_cbranch_vccz Loh2%=\n\t" \
  "s_add_u32 s97, s97, 1\n\t" \
  "s_cmp_gt_u32 s97, 0xffff\n\t" \
  "s_cbranch_scc1 Loh2%=\n\t" \
  "s_sleep 2\n\t" \
  "s_add_u32 m0, %[m0a], 24576\n\t" \
  "v_subrev_u32 v48, 16384, v95\n\t" \
  "global_load_lds_dwordx4 v48, %[wsb] sc1\n\t" \
  "global_load_lds_dwordx4 v48, %[wsb] offset:1024 sc1\n\t" \
  "s_add_u32 m0, %[m0a], 32768\n\t" \
  "v_subrev_u32 v48, 8192, v95\n\t" \
  "global_load_lds_dwordx4 v48, %[wsb] sc1\n\t" \
  "global_load_lds_dwordx4 v48, %[wsb] offset:1024 sc1\n\t" \
  "s_waitcnt vmcnt(0)\n\t" \
  "s_branch Lch2%=\n\t" \
  "Loh2%=:\n\t" \
  "s_mov_b32 m0, s96\n\t" \
  "s_add_u32 s96, s96, 0x2000\n\t" \
  "global_load_lds_dwordx4 v95, %[wsb] sc1\n\t" \
  "global_load_lds_dwordx4 v95, %[wsb] offset:1024 sc1\n\t" \
  "v_add_u32 v95, 0x2000, v95\n\t" \
  "s_mov_b32 m0, s96\n\t" \
  "s_add_u32 s96, s96, 0x2000\n\t" \
  "global_load_lds_dwordx4 v95, %[wsb] sc1\n\t" \
  "global_load_lds_dwordx4 v95, %[wsb] offset:1024 sc1\n\t" \
  "v_add_u32 v95, 0x2000, v95\n\t" \
  "s_setpc_b64 s[90:91]\n\t" \
  "Lh4%=:\n\t" \
  "s_waitcnt vmcnt(2)\n\t" \
  "s_mov_b32 s97, 0\n\t" \
  "Lch4%=:\n\t" \
  "ds_read_b128 v[48:51], v98 offset:40960\n\t" \
  "ds_read_b128 v[52:55], v98 offset:41984\n\t" \
  "ds_read_b128 v[56:59], v98 offset:49152\n\t" \
  "ds_read_b128 v[60:63], v98 offset:50176\n\t" \
  "s_waitcnt lgkmcnt(0)\n\t" \
  "v_max3_u32 v48, v48, v49, v50\n\t" \
  "v_max3_u32 v48, v51, v52, v48\n\t" \
  "v_max3_u32 v48, v53, v54, v48\n\t" \
  "v_max3_u32 v48, v55, v56, v48\n\t" \
  "v_max3_u32 v48, v57, v58, v48\n\t" \
  "v_max3_u32 v48, v59, v60, v48\n\t" \
  "v_max3_u32 v48, v61, v62, v48\n\t" \
  "v_max3_u32 v48, v63, v50, v48\n\t" \
  "v_cmp_eq_u32 vcc, -1, v48\n\t" \
  "s_nop 1\n\t" \
  "s_cbranch_vccz Loh4%=\n\t" \
  "s_add_u32 s97, s97, 1\n\t" \
  "s_cmp_gt_u32 s97, 0xffff\n\t" \
  "s_cbranch_scc1 Loh4%=\n\t" \
  "s_sleep 2\n\t" \
  "s_add_u32 m0, %[m0a], 40960\n\t" \
  "v_subrev_u32 v48, 24576, v95\n\t" \
  "global_load_lds_dwordx4 v48, %[wsb] sc1\n\t" \
  "global_load_lds_dwordx4 v48, %[wsb] offset:1024 sc1\n\t" \
  "s_add_u32 m0, %[m0a], 49152\n\t" \
  "v_subrev_u32 v48, 16384, v95\n\t" \
  "global_load_lds_dwordx4 v48, %[wsb] sc1\n\t" \
  "global_load_lds_dwordx4 v48, %[wsb] offset:1024 sc1\n\t" \
  "s_waitcnt vmcnt(0)\n\t" \
  "s_branch Lch4%=\n\t" \
  "Loh4%=:\n\t" \
  "s_setpc_b64 s[90:91]\n\t" \
  "Lh5%=:\n\t" \
  "s_waitcnt vmcnt(0)\n\t" \
  "s_mov_b32 s97, 0\n\t" \
  "Lch5%=:\n\t" \
  "ds_read_b128 v[48:51], v98 offset:57344\n\t" \
  "ds_read_b128 v[52:55], v98 offset:58368\n\t" \
  "s_waitcnt lgkmcnt(0)\n\t" \
  "v_max3_u32 v48, v48, v49, v50\n\t" \
  "v_max3_u32 v48, v51, v52, v48\n\t" \
  "v_max3_u32 v48, v53, v54, v48\n\t" \
  "v_max3_u32 v48, v55, v50, v48\n\t" \
  "v_cmp_eq_u32 vcc, -1, v48\n\t" \
  "s_nop 1\n\t" \
  "s_cbranch_vccz Loh5%=\n\t" \
  "s_add_u32 s97, s97, 1\n\t" \
  "s_cmp_gt_u32 s97, 0xffff\n\t" \
  "s_cbranch_scc1 Loh5%=\n\t" \
  "s_sleep 2\n\t" \
  "s_add_u32 m0, %[m0a], 57344\n\t" \
  "v_subrev_u32 v48, 8192, v95\n\t" \
  "global_load_lds_dwordx4 v48, %[wsb] sc1\n\t" \
  "global_load_lds_dwordx4 v48, %[wsb] offset:1024 sc1\n\t" \
  "s_waitcnt vmcnt(0)\n\t" \
  "s_branch Lch5%=\n\t" \
  "Loh5%=:\n\t" \
  "s_setpc_b64 s[90:91]\n\t" \
  "Lend%=:\n\t" \
  "s_waitcnt lgkmcnt(0)\n\t" \
  "v_mfma_f32_32x32x16_bf16 v[16:31], v[72:75], v[80:83], v[16:31]\n\t" \
  "s_setprio 0\n\t" \
  "s_waitcnt vmcnt(0)\n\t" \
  "s_nop 7\n\t" \
  "s_nop 7\n\t" \
  ""

template <class W, class H> DI f32x16 gemm_half(const LAS char* act, W wf, H hook) {
  f32x16 acc = zero16();
  u32x4 a[16];
#pragma unroll
  for (int i = 0; i < 4; ++i) a[i] = *(const LAS u32x4*)(act + i * 1024);
#pragma unroll
  for (int i = 0; i < 16; ++i) {
    SBAR();
    if (i + 4 < 16) a[i + 4] = *(const LAS u32x4*)(act + (i + 4) * 1024);
    acc = MFMA(wf(i), a[i], acc);
    SBAR();
    hook(i);
  }
  SBAR();
  return acc;
}
DI f32x4 lds2(const LAS char* p, int half) { return *(const LAS f32x4*)p + *(const LAS f32x4*)(p + half); }
DI void resid_epilogue(const f32x16& acc, u32x4& xr, rsrc_t rs, int xoff, LAS char* part, uint16_t* out, bool final_, int tt, int ks, int lane) {
  const int r = lane & 31, h = lane >> 5;
  const u32x4 y = pack8(acc, 0);
#pragma unroll
  for (int j = 0; j < 4; ++j) xr[j] = cvtpk(bflo(xr[j]) + bflo(y[j]), bfhi(xr[j]) + bfhi(y[j]));
  xr = clean(xr);
  if (final_) {
    auto a = __builtin_amdgcn_permlane32_swap(xr[0], xr[2], false, false);
    auto c = __builtin_amdgcn_permlane32_swap(xr[1], xr[3], false, false);
    *(u32x4*)(out + (size_t)(tt * 32 + r) * DM + ks * 16 + 8 * h) = u32x4{a[0], c[0], a[1], c[1]};
  } else {
    st128(rs, lane * 16, xoff + (tt * 32 + ks) * 1024, xr);
    const float s = xhalf_sum(sum8(xr)), q = xhalf_sum(sq8(xr));
    if (h == 0) *(LAS f32x2*)(part + r * 8) = f32x2{s, q};
  }
}

#define B_DRAIN 1
#define D_DRAIN 1
#define E_SLEEP 10
#define C_SLEEP 0
#define NEARLY 12
__global__ __launch_bounds__(512, 2) void k_mega(char* ws, uint16_t* out) {
  extern __shared__ __attribute__((aligned(16))) char smem_[];
  LAS char* smem = (LAS char*)smem_;
  const unsigned lds0 = (unsigned)(uintptr_t)smem;
  const rsrc_t rs = __builtin_amdgcn_make_buffer_rsrc(ws, 0, WS_END, 0x00020000);
  const int w_ = __builtin_amdgcn_readfirstlane(threadIdx.x >> 6);
#define IDS() int tid = threadIdx.x, w = w_, b = blockIdx.x; asm volatile("" : "+v"(tid)); asm volatile("" : "+s"(w), "+s"(b)); \
  const int lane = tid & 63, r = lane & 31, h = lane >> 5, w4 = w & 3, kh = w >> 2, xcd = b & 7, idx = b >> 3; \
  const int tt = xcd * 8 + (idx >> 2), cb = idx & 3, nt = cb * 4 + w4;                 \
  const int panel = (xcd & 3) * 4 + (idx & 3), hp = (xcd >> 2) * 8 + (idx >> 2);       \
  const unsigned voff = lane * 16, vrot = voff ^ (kh << 8); \
  (void)r; (void)h; (void)tt; (void)nt; (void)panel; (void)hp; (void)voff; (void)vrot
  u32x4 xr;
  STAMP_DECL;
  {
    IDS();
    STAMP(0);
#pragma unroll
    for (int t = 0; t < 3; ++t) glds4<false>(ws, OFF_W + ((t * 16 + hp) * 32 + 4 * w) * 1024 + voff, lds0 + L_WL + (t * 32 + 4 * w) * 1024);
    xr = ld128(rs, voff, OFF_XF + (tt * 32 + 2 * nt + kh) * 1024);
  }

#pragma unroll 1
  for (int L = 0; L < 2; ++L) {
    const int xa = OFF_XF + (2 * L) * 2048 * KB, xb = xa + 2048 * KB, xc = xb + 2048 * KB;
    const int sa_ = OFF_ST + (2 * L) * 64 * KB, sb_ = sa_ + 64 * KB, sc_ = sb_ + 64 * KB;
    const int pqo = OFF_PQ + L * 2048 * KB, ofo = OFF_OF + L * 2048 * KB, kvo = OFF_KV + L * 128 * KB;
    const int acco = OFF_ACC + L * ACC_N * 4;
    u32x4 wp[16];
    {
      IDS();
      const int t1 = panel * 4 + w4;
      constexpr int WIN = 4;
      u32x4 xf[16];
#define XLD(i) xf[i] = ld128(rs, vrot + ((i) & 3) * 1024, xa + (t1 * 32 + kh * 16 + ((i) & ~3)) * 1024)
#define XMAX(g) umax4(xf[4 * (g)], umax4(xf[4 * (g) + 1], umax4(xf[4 * (g) + 2], umax4(xf[4 * (g) + 3], 0u))))
      if (w == 4) glds1(ws, OFF_CS1 + hp * 1024 + voff, lds0 + 128 * KB);
      u32x4 sg;
#define X1LD() do { _Pragma("unroll") for (int i = 0; i < WIN; ++i) XLD(i); } while (0)
#define S1LD() sg = ld128(rs, (r ^ (kh << 4)) * 32 + h * 16, sa_ + t1 * 1024)
      X1LD();
      SBAR();
      VERIFY(XMAX(0), X1LD());
      BAR();
      STAMP(1 + 16 * L);
      f32x16 aq = zero16(), ak = zero16(), av = zero16();
      const LAS char* wl = smem + L_WL + kh * 16384;
      u32x4 wf[2][3];
#define WLD(i) do { wf[(i) & 1][0] = *(const LAS u32x4*)(wl + vrot + (i) * 1024); wf[(i) & 1][1] = *(const LAS u32x4*)(wl + voff + (32 + (i)) * 1024); \
                    wf[(i) & 1][2] = *(const LAS u32x4*)(wl + voff + (64 + (i)) * 1024); } while (0)
      WLD(0);
      RowStat st;
      LAS float* sst = (LAS float*)(smem + 129 * KB + w * 256);
#pragma unroll
      for (int i = 0; i < 16; ++i) {
        SBAR();
        if (i < 15) WLD(i + 1);
        aq = MFMA(wf[i & 1][0], xf[i], aq);
        ak = MFMA(xf[i], wf[i & 1][1], ak);
        av = MFMA(xf[i], wf[i & 1][2], av);
        SBAR();
        if (2 * i + WIN < 16) { XLD(2 * i + WIN); XLD(2 * i + WIN + 1); }
        if (i >= 8) {
#pragma unroll
          for (int k = 2 * i - 16; k < 2 * i - 14; ++k) wp[k] = ldw(rs, vrot + (k & 3) * 1024, OFF_W + ((T_WOA + nt) * 32 + kh * 16 + (k & ~3)) * 1024);
        }
        if ((i & 3) == 3 && i < 15 && L == 1) VERIFY_FAST(XMAX((i >> 2) + 1), { XLD(i + 1); XLD(i + 2); XLD(i + 3); XLD(i + 4); });
        if (i == 6) S1LD();
        if (i == 12) {
          VERIFY_FAST(umax4(sg, 0u), S1LD());
          st = row_stats(sg);
          if (h == 0) { sst[r] = st.mu; sst[32 + r] = st.rs; }
        }
      }
      SBAR();
      STAMP(2 + 16 * L);
      LAS char* exw = smem + (kh ? L_WL : 136 * KB) + w4 * 6144;
      LAS char* exr = smem + (kh ? 136 * KB : L_WL) + w4 * 6144;
      const LAS char* csl = smem + 128 * KB;
      const float csk = *(const LAS float*)(csl + 128 + r * 4) + *(const LAS float*)(csl + 640 + r * 4);
      const float csv = *(const LAS float*)(csl + 256 + r * 4) + *(const LAS float*)(csl + 768 + r * 4);
      f32x4 cs[2], mu[2], rr[2];
#pragma unroll
      for (int g = 0; g < 2; ++g) {
        cs[g] = lds2(csl + kh * 64 + g * 32 + h * 16, 512);
        mu[g] = *(const LAS f32x4*)(sst + 8 * g + 4 * h); rr[g] = *(const LAS f32x4*)(sst + 32 + 8 * g + 4 * h);
      }
      {
        const u32x4 pz = {POISON, POISON, POISON, POISON};
        st128(rs, voff, xb + (tt * 32 + 2 * nt + kh) * 1024, pz);
        st128(rs, voff, ofo + (tt * 32 + 2 * nt + kh) * 1024, pz);
        if (w == 2 && lane < 32) st64(rs, lane * 32, sb_ + tt * 1024 + cb * 8, u32x2{POISON, POISON});
        if (cb == 0 && w < 2) st128(rs, voff, kvo + tt * 2048 + w * 1024, pz);
        if (L == 0) {
          st128(rs, voff, xc + (tt * 32 + 2 * nt + kh) * 1024, pz);
          if (w == 3 && lane < 32) st64(rs, lane * 32, sc_ + tt * 1024 + cb * 8, u32x2{POISON, POISON});
        }
      }
      SBAR();
      if (kh == 1) BAR();
      put_hi(exw + vrot, aq); put_hi(exw + 2048 + voff, ak); put_hi(exw + 4096 + voff, av);
      if (kh == 0) BAR();
      BAR();
      STAMP(42);
      add_lo(exr + vrot, aq); add_lo(exr + 2048 + voff, ak); add_lo(exr + 4096 + voff, av);
      const float rsl = st.rs * LOG2E;
#pragma unroll
      for (int g = 0; g < 2; ++g) {
#pragma unroll
        for (int j = 0; j < 4; ++j) {
          ak[4 * g + j] = phi(ak[4 * g + j] - mu[g][j] * csk, rr[g][j], rr[g][j] * LOG2E);
          av[4 * g + j] = rr[g][j] * (av[4 * g + j] - mu[g][j] * csv);
        }
      }

      const u32x4 ones = {0x3f803f80u, 0x3f803f80u, 0x3f803f80u, 0x3f803f80u};
      const u32x4 pk = pack8(ak, 0);
      const f32x16 z = MFMA(pk, pack8(av, 0), zero16());
      const f32x16 zs = MFMA(pk, ones, zero16());
#pragma unroll
      for (int j = 0; j < 8; ++j) aq[j] = phi(aq[j] - st.mu * cs[j >> 2][j & 3], st.rs, rsl);
      st128(rs, vrot, pqo + (t1 * 32 + 2 * hp + kh) * 1024, pack8(aq, 0));
      {
        LAS float* red = (LAS float*)exr + h * 8 + (r & 15) * 16;
        if (r < 16) {
          *(LAS f32x4*)red = f32x4{z[0], z[1], z[2], z[3]}; *(LAS f32x4*)(red + 4) = f32x4{z[4], z[5], z[6], z[7]};
          if (r == 0) { *(LAS f32x4*)(red + 256) = f32x4{zs[0], zs[1], zs[2], zs[3]}; *(LAS f32x4*)(red + 260) = f32x4{zs[4], zs[5], zs[6], zs[7]}; }
          asm volatile("" ::: "memory");
        } else {
          *(LAS f32x4*)(red + 272) = f32x4{z[8], z[9], z[10], z[11]}; *(LAS f32x4*)(red + 276) = f32x4{z[12], z[13], z[14], z[15]};
          if (r == 16) { *(LAS f32x4*)(red + 528) = f32x4{zs[8], zs[9], zs[10], zs[11]}; *(LAS f32x4*)(red + 532) = f32x4{zs[12], zs[13], zs[14], zs[15]}; }
        }
      }
      STAMP(43);
      BAR();
#pragma unroll
      for (int i = 0; i < 2; ++i) {
        const int j = i * 512 + tid;
        if (j < 544) {
          const LAS float* ra = (const LAS float*)(smem + L_WL) + j;
          float t = 0.f;
#pragma unroll
          for (int k = 0; k < 4; ++k) t += ra[k * 1536] + ra[26 * 1024 + k * 1536];
          atomicAdd((float*)(ws + acco) + hp * 544 + j, t);
        }
      }
      STAMP(45);
      DRAIN();
      STAMP(3 + 16 * L);
      BAR();
      if (w == 0) {
        arrive(ws, CTR_A + L * 128 + (idx & 15), 16, 8, lane);
        poll(ws, CTR_A + L * 128 + xcd * 16, 16, 16, lane);
      }
      BAR();
      STAMP(4 + 16 * L);
    }
    u32x4 wkv[8];
    {
      IDS();
      u32x4 pq[4], kv[4][2];
      const int row = r < 16 ? r : 16;
#pragma unroll
      for (int i = 0; i < 4; ++i) {
        kv[i][0] = ld128(rs, row * 64 + h * 32 + i * 1088, acco + w * 4352);
        kv[i][1] = ld128(rs, row * 64 + h * 32 + i * 1088 + 16, acco + w * 4352);
        pq[i] = ld128(rs, voff + i * 1024, pqo + (tt * 32 + w * 4) * 1024);
      }
      SBAR();
      STAMP(46);
#pragma unroll
      for (int i = 0; i < 4; ++i) {
        const f32x4 k0 = __builtin_bit_cast(f32x4, kv[i][0]), k1 = __builtin_bit_cast(f32x4, kv[i][1]);
        const u32x4 a = {cvtpk(k0[0], k0[1]), cvtpk(k0[2], k0[3]), cvtpk(k1[0], k1[1]), cvtpk(k1[2], k1[3])};
        f32x16 d = MFMA(a, pq[i], zero16());
        const float inv = __builtin_amdgcn_rcpf(d[8] + 1e-6f);
#pragma unroll
        for (int j = 0; j < 8; ++j) d[j] *= inv;
        *(LAS u32x4*)(smem + L_ACT + (w * 4 + i) * 1024 + voff) = pack8(d, 0);
      }
      const int wqo = OFF_W + ((T_WQ + nt) * 32 + kh * 16) * 1024;
#pragma unroll
      for (int i = 0; i < 8; ++i) asm volatile("" : "=v"(wkv[i]));
      if (cb == 0 && kh == 1) {
#pragma unroll
        for (int i = 0; i < 8; ++i) wkv[i] = ldw(rs, (voff ^ (w4 == 1 ? 256 : 0)) + (i & 3) * 1024, OFF_W + (T_WKV * 32 + w4 * 8 + (i & 4)) * 1024);
      }
      if (w == 1) glds2(ws, OFF_CS3 + cb * 2048 + voff, lds0 + 56 * KB);
      STAMP(50);
      BAR();
      STAMP(5 + 16 * L);
      f32x16 acc = gemm_half(smem + L_ACT + kh * 16384 + voff, [&](int i) { return wp[i]; },
                             [&](int i) { if (i < NEARLY) wp[i] = ldw(rs, vrot + (i & 3) * 1024, wqo + (i & ~3) * 1024); });
      STAMP(6 + 16 * L);
      put_hi(smem + L_WL + w * 2048 + voff, acc);
      BAR();
      STAMP(51);
      add_lo(smem + L_WL + (w ^ 4) * 2048 + voff, acc);
      resid_epilogue(acc, xr, rs, xb, smem + 48 * KB + w * 256, out, false, tt, 2 * nt + kh, lane);
      BAR();
      if (w == 0) publish_stats<8>(smem + 48 * KB, rs, sb_ + tt * 1024 + cb * 8, lane);
      STAMP(7 + 16 * L);
    }
    u32x4 q0;
    {
      IDS();
      u32x4 xa4[4];
#define X3LD() do { _Pragma("unroll") for (int k = 0; k < 4; ++k) xa4[k] = ld128(rs, voff + k * 1024, xb + (tt * 32 + 4 * w) * 1024); } while (0)
      if (B_DRAIN) DRAIN();
      if (B_DRAIN > 1) BAR();
      X3LD();
      VERIFY(umax4(xa4[0], umax4(xa4[1], umax4(xa4[2], umax4(xa4[3], 0u)))), X3LD());
      u32x4 sg = ld128(rs, r * 32 + h * 16, sb_ + tt * 1024);
      STAMP(8 + 16 * L);
#pragma unroll
      for (int k = 0; k < 4; ++k) *(LAS u32x4*)(smem + L_ACT + (4 * w + k) * 1024 + voff) = xa4[k];
      BAR();
      STAMP(9 + 16 * L);
#pragma unroll
      for (int i = NEARLY; i < 16; ++i) wp[i] = ldw(rs, vrot + (i & 3) * 1024, OFF_W + ((T_WQ + nt) * 32 + kh * 16 + (i & ~3)) * 1024);
      SBAR();
      const LAS char* csl = smem + 56 * KB + h * 16;
      RowStat st;
#define S3GET() do { VERIFY_FAST(umax4(sg, 0u), sg = ld128(rs, r * 32 + h * 16, sb_ + tt * 1024)); st = row_stats(sg); } while (0)
      if (cb == 0 && kh == 1) {
        f32x16 kacc = zero16();
        __builtin_amdgcn_s_setprio(2);
        {
          u32x4 a[8];
#pragma unroll
          for (int i = 0; i < 8; ++i) a[i] = *(const LAS u32x4*)(smem + L_ACT + (w4 * 8 + i) * 1024 + voff);
          SBAR();
#pragma unroll
          for (int i = 0; i < 8; ++i) kacc = MFMA(wkv[i], a[i], kacc);
          S3GET();
          LAS char* kp = smem + 60 * KB + w4 * 4096 + voff;
          if (w4 != 0) { *(LAS f32x4*)(kp + (w4 == 1 ? 2048 : 0)) = f32x4{kacc[0], kacc[1], kacc[2], kacc[3]}; *(LAS f32x4*)(kp + (w4 == 1 ? 3072 : 1024)) = f32x4{kacc[4], kacc[5], kacc[6], kacc[7]}; }
          put_hi(kp + (w4 == 1 ? 0 : 2048), kacc);
        }
        BAR();
        if (w4 < 2) {
          const LAS char* kp = smem + 60 * KB + w4 * 2048 + voff;
          add_lo(kp + (w4 ^ 1) * 4096, kacc); add_lo(kp + 8192, kacc); add_lo(kp + 12288, kacc);
#pragma unroll
          for (int g = 0; g < 2; ++g) {
            const f32x4 c = lds2(csl + 512 + w4 * 64 + g * 32, 1024);
#pragma unroll
            for (int j = 0; j < 4; ++j) kacc[4 * g + j] = st.rs * (kacc[4 * g + j] - st.mu * c[j]);
          }
          u32x4 v = pack8(kacc, 0);
          if (w == 5) {
            const int t16 = r & 15, hh = (t16 >> 2) & 1, jj = ((t16 >> 3) << 2) | (t16 & 3);
            LAS uint16_t* vb = (LAS uint16_t*)(smem + 76 * KB + (r >> 4) * 512 + hh * 256) + jj;
#pragma unroll
            for (int j = 0; j < 8; ++j) vb[(8 * (j >> 2) + 4 * h + (j & 3)) * 8] = (uint16_t)((j & 1) ? (v[j >> 1] >> 16) : (v[j >> 1] & 0xffffu));
            v = *(const LAS u32x4*)(smem + 76 * KB + voff);
          }
          st128(rs, voff, kvo + tt * 2048 + w4 * 1024, clean(v));
        }
        __builtin_amdgcn_s_setprio(0);
      }
      f32x16 acc = gemm_half(smem + L_ACT + kh * 16384 + voff, [&](int i) { return wp[i]; },
                             [&](int i) {
        if (i == 6 && cb == 0 && kh == 0) asm volatile("s_barrier" ::: "memory");
        if (i == 10 && !(cb == 0 && kh == 1)) S3GET();
      });
      put_hi(smem + 80 * KB + w * 2048 + voff, acc);
      f32x4 c[2];
#pragma unroll
      for (int g = 0; g < 2; ++g) c[g] = lds2(csl + w4 * 128 + kh * 64 + g * 32, 1024);
      BAR();
      add_lo(smem + 80 * KB + (w ^ 4) * 2048 + voff, acc);
      const float sc = st.rs * (0.25f * LOG2E);
#pragma unroll
      for (int j = 0; j < 8; ++j) acc[j] = sc * (acc[j] - st.mu * c[j >> 2][j & 3]);
      q0 = pack8(acc, 0);
      *(LAS u32x4*)(smem + L_COMB + w * 3072 + voff) = q0;
      if (tid >= 64 && tid < 74) {
        const int s5 = (tid - 64) >> 1, o5 = s5 == 0 ? 0 : (s5 == 1 ? 512 : (s5 == 2 ? 2048 : (s5 == 3 ? 2560 : 4096)));
        const unsigned c = (tid & 1) ? 0u : 0x3f803f80u;
        *(LAS u32x4*)(smem + L_CONST + o5 + (tid & 1) * 16) = u32x4{c, c, c, c};
      }
      PIN(q0);
      STAMP(10 + 16 * L);
      if (C_SLEEP) __builtin_amdgcn_s_sleep(C_SLEEP);
      BAR();
      STAMP(11 + 16 * L);
    }
    u32x16 wo0, wo1, wo2, wo3, wn0, wn1, wn2;
    {
      IDS();
      f32x16 oa = zero16(), ob = zero16(), sa, sb;
      u32x4 kf, v0, v1, p0, p1 = {0u, 0u, 0u, 0u};
      unsigned ka = lds0 + kh * 65536 + lane * 16;
      unsigned va = r < 16 ? lds0 + kh * 65536 + 1024 + (h * 16 + r) * 16 : lds0 + L_CONST + ((r == 16 || r == 20) ? 0 : 16);
      const unsigned vinc = r < 16 ? 4096u : 0u;
      const unsigned o0 = kh * 65536 + w4 * 2048;
      unsigned ga = kvo + o0 + voff, wo = OFF_W + ((T_WOB + nt) * 32 + kh * 16) * 1024 + vrot, n, m0s;
      unsigned wn = OFF_W + (hp * 32 + 4 * w) * 1024 + voff;
      const u32x4 q1 = *(const LAS u32x4*)(smem + L_COMB + (w ^ 4) * 3072 + voff);
      asm volatile(ATTN_ASM
                   : "+{v[0:15]}"(oa), "+{v[16:31]}"(ob), "=&{v[32:47]}"(sa), "=&{v[48:63]}"(sb), "=&{v[64:67]}"(kf), "=&{v[68:71]}"(v0),
                     "=&{v[72:75]}"(v1), "=&{v[76:79]}"(p0), "+{v[80:83]}"(p1), "+{v92}"(ka), "+{v93}"(va), "+{v95}"(ga), "+{v96}"(wo),
                     "=&{v[128:143]}"(wo0), "=&{v[144:159]}"(wo1), "=&{v[160:175]}"(wo2), "=&{v[176:191]}"(wo3),
                     "+{v97}"(wn), "=&{v[192:207]}"(wn0), "=&{v[208:223]}"(wn1), "=&{v[224:239]}"(wn2),
                     [n] "=&s"(n), [m0s] "=&s"(m0s)
                   : "{v[84:87]}"(q0), "{v[88:91]}"(q1), "{v94}"(vinc), "{v98}"(lds0 + o0 + voff), [wsb] "s"(ws), [kh] "s"(kh), [m0a] "s"(__builtin_amdgcn_readfirstlane(lds0 + o0))
                   : "memory", "scc", "vcc", "s90", "s91", "s92", "s93", "s94", "s95", "s96", "s97");
      STAMP(12 + 16 * L);
      LAS char* cw = smem + L_COMB + w * 3072 + voff;
      const LAS char* cr = smem + L_COMB + (w ^ 4) * 3072 + voff;
      *(LAS f32x4*)cw = f32x4{ob[0], ob[1], ob[2], ob[3]}; *(LAS f32x4*)(cw + 1024) = f32x4{ob[4], ob[5], ob[6], ob[7]};
      *(LAS float*)(cw + 2048) = ob[8];
      BAR();
      STAMP(54);
      add_lo(cr, oa);
      oa[8] += *(const LAS float*)(cr + 2048);
      float mx = fmaxf(fmaxf(fabsf(oa[0]), fabsf(oa[1])), fabsf(oa[2]));
      mx = fmaxf(fmaxf(mx, fabsf(oa[3])), fabsf(oa[4])); mx = fmaxf(fmaxf(mx, fabsf(oa[5])), fabsf(oa[6]));
      const float sum8 = ((oa[0] + oa[1]) + (oa[2] + oa[3])) + ((oa[4] + oa[5]) + (oa[6] + oa[7]));
      const bool ok = fmaxf(fmaxf(mx, fabsf(oa[7])), oa[8]) < 1.0e36f && oa[8] > 1.0e-27f && sum8 == sum8;
      if (__builtin_expect(!__all(ok), 0)) {
        const LAS char* kb0 = smem + lane * 16;
        const LAS char* vb0 = r < 16 ? smem + 1024 + (h * 16 + r) * 16 : smem + L_CONST + ((r == 16 || r == 20) ? 0 : 16);
        const int vstep = r < 16 ? 2048 : 0;
        float m = -INFINITY;
        oa = zero16();
#pragma unroll 1
        for (int t = 0; t < 64; ++t) {
          f32x16 s = MFMA(*(const LAS u32x4*)(kb0 + t * 2048), q0, zero16());
          const float mn = fmaxf(m, xhalf_max(max16(s)));
          const float al = __builtin_amdgcn_exp2f(m - mn);
          m = mn;
#pragma unroll
          for (int i = 0; i < 16; ++i) { oa[i] *= al; s[i] = __builtin_amdgcn_exp2f(s[i] - mn); }
          oa = MFMA(*(const LAS u32x4*)(vb0 + t * vstep), pack8(s, 0), oa);
          oa = MFMA(*(const LAS u32x4*)(vb0 + t * vstep + 512), pack8(s, 1), oa);
        }
      }
      const float inv = __builtin_amdgcn_rcpf(oa[8]);
#pragma unroll
      for (int i = 0; i < 8; ++i) oa[i] *= inv;
      st128(rs, voff, ofo + (tt * 32 + 2 * nt + kh) * 1024, clean(pack8(oa, 0)));
      STAMP(55);
    }
    {
      IDS();
      u32x4 o4[4];
#define O5LD() do { _Pragma("unroll") for (int k = 0; k < 4; ++k) o4[k] = ld128(rs, voff + k * 1024, ofo + (tt * 32 + 4 * w) * 1024); } while (0)
      if (D_DRAIN) DRAIN();
      BAR();
      O5LD();
      SBAR();
      if (L == 0) {
#pragma unroll
        for (int i = 0; i < 4; ++i) {
          LAS char* d = smem + L_WL + (4 * w + i) * 1024 + voff;
          *(LAS u32x4*)d = u32x4{wn0[4 * i], wn0[4 * i + 1], wn0[4 * i + 2], wn0[4 * i + 3]};
          *(LAS u32x4*)(d + 32768) = u32x4{wn1[4 * i], wn1[4 * i + 1], wn1[4 * i + 2], wn1[4 * i + 3]};
          *(LAS u32x4*)(d + 65536) = u32x4{wn2[4 * i], wn2[4 * i + 1], wn2[4 * i + 2], wn2[4 * i + 3]};
        }
      }
      SBAR();
      VERIFY(umax4(o4[0], umax4(o4[1], umax4(o4[2], umax4(o4[3], 0u)))), O5LD());
      STAMP(13 + 16 * L);
#pragma unroll
      for (int k = 0; k < 4; ++k) *(LAS u32x4*)(smem + L_ACT + (4 * w + k) * 1024 + voff) = o4[k];
      BAR();
      STAMP(14 + 16 * L);
      f32x16 acc = gemm_half(smem + L_ACT + kh * 16384 + voff, [&](int i) {
        const u32x16& v = i < 4 ? wo0 : (i < 8 ? wo1 : (i < 12 ? wo2 : wo3));
        return u32x4{v[4 * (i & 3)], v[4 * (i & 3) + 1], v[4 * (i & 3) + 2], v[4 * (i & 3) + 3]};
      }, [](int) {});
      put_hi(smem + 136 * KB + w * 2048 + voff, acc);
      BAR();
      add_lo(smem + 136 * KB + (w ^ 4) * 2048 + voff, acc);
      resid_epilogue(acc, xr, rs, xc, smem + 152 * KB + w * 256, out, L == 1, tt, 2 * nt + kh, lane);
      STAMP(15 + 16 * L);
      if (L == 0) {
        BAR();
        if (w == 0) publish_stats<8>(smem + 152 * KB, rs, sc_ + tt * 1024 + cb * 8, lane);
        DRAIN();
        if (E_SLEEP) __builtin_amdgcn_s_sleep(E_SLEEP);
        STAMP(16);
      }
    }
  }
  STAMP(60);
  STAMP_FLUSH();
}

extern "C" void kernel_launch(void* const* d_in, const int* in_sizes, int n_in, void* d_out, int out_size, void* d_ws,
                              size_t ws_size, hipStream_t stream) {
  char* ws = (char*)d_ws;
  (void)hipFuncSetAttribute((const void*)k_mega, hipFuncAttributeMaxDynamicSharedMemorySize, 160 * KB);
  k_prep<<<PREP_BLOCKS, 256, 0, stream>>>((const uint16_t*)d_in[0], (const uint16_t*)d_in[1], (const uint16_t*)d_in[2], (const uint16_t*)d_in[4],
                                          (const uint16_t*)d_in[5], (const uint16_t*)d_in[6], (const uint16_t*)d_in[7], ws);
  k_mega<<<256, 512, 160 * KB, stream>>>(ws, (uint16_t*)d_out);
}
```

```cpp
#include <hip/hip_runtime.h>
#include <hip/hip_bf16.h>
#include <stdint.h>

typedef __attribute__((ext_vector_type(8))) short bf16x8;
typedef __attribute__((ext_vector_type(4))) short s16x4;
typedef __attribute__((ext_vector_type(16))) float f32x16;
typedef __attribute__((ext_vector_type(4))) float f32x4;
typedef __attribute__((ext_vector_type(2))) float f32x2;
typedef __attribute__((ext_vector_type(16))) unsigned u32x16;
typedef __attribute__((ext_vector_type(4))) unsigned u32x4;
typedef __attribute__((ext_vector_type(2))) unsigned u32x2;
typedef __attribute__((ext_vector_type(2))) __bf16 bf16x2_t;
#define LAS __attribute__((address_space(3)))
#define GAS __attribute__((address_space(1)))
typedef GAS unsigned gu32;
#define DI __device__ __forceinline__
#define MFMA(a, b, c) __builtin_amdgcn_mfma_f32_32x32x16_bf16(__builtin_bit_cast(bf16x8, (a)), __builtin_bit_cast(bf16x8, (b)), (c), 0, 0, 0)
#define RLX_AGENT __ATOMIC_RELAXED, __HIP_MEMORY_SCOPE_AGENT

#define STAMP(slot) do {} while (0)
#define STAMP_DECL do {} while (0)
#define STAMP_FLUSH() do {} while (0)

constexpr int DM = 512, NH = 32;
constexpr float LOG2E = 1.4426950408889634f;

constexpr int KB = 1024;
constexpr int OFF_CTR = 0;
constexpr int CTR_A = 0, CTR_TMO = 256, CTR_LINES = 264;
constexpr int OFF_ACC = OFF_CTR + CTR_LINES * 128;
constexpr int ACC_N = NH * 17 * 16;
constexpr int ZERO_BYTES = CTR_LINES * 128 + 2 * ACC_N * 4;
constexpr int OFF_W = 256 * KB;
constexpr int T_WOA = 48, T_WQ = 64, T_WKV = 80, T_WOB = 81;
constexpr int OFF_CS1 = OFF_W + 97 * 32 * KB;
constexpr int OFF_CS3 = OFF_CS1 + 16 * KB;
constexpr int OFF_XF = OFF_CS3 + 8 * KB;
constexpr int OFF_ST = OFF_XF + 4 * 2048 * KB;
constexpr int OFF_PQ = OFF_ST + 4 * 64 * KB;
constexpr int OFF_OF = OFF_PQ + 2 * 2048 * KB;
constexpr int OFF_KV = OFF_OF + 2 * 2048 * KB;
constexpr int WS_END = OFF_KV + 2 * 128 * KB;

constexpr int L_ACT = 0, L_WL = 32 * KB;
constexpr int L_CONST = 128 * KB, L_COMB = 136 * KB;

DI unsigned cvtpk(float lo, float hi) {
  f32x2 v = {lo, hi};
  return __builtin_bit_cast(unsigned, __builtin_convertvector(v, bf16x2_t));
}
DI float bflo(unsigned u) { return __uint_as_float(u << 16); }
DI float bfhi(unsigned u) { return __uint_as_float(u & 0xffff0000u); }
DI float bfround(float x) { return bflo(cvtpk(x, x)); }
DI u32x4 pack8(const f32x16& x, int s) {
  u32x4 p;
  p[0] = cvtpk(x[8 * s + 0], x[8 * s + 1]); p[1] = cvtpk(x[8 * s + 2], x[8 * s + 3]);
  p[2] = cvtpk(x[8 * s + 4], x[8 * s + 5]); p[3] = cvtpk(x[8 * s + 6], x[8 * s + 7]);
  return p;
}
DI float xhalf_sum(float v) {
  auto r = __builtin_amdgcn_permlane32_swap(__float_as_uint(v), __float_as_uint(v), false, false);
  return __uint_as_float(r[0]) + __uint_as_float(r[1]);
}
DI float xhalf_max(float v) {
  auto r = __builtin_amdgcn_permlane32_swap(__float_as_uint(v), __float_as_uint(v), false, false);
  return fmaxf(__uint_as_float(r[0]), __uint_as_float(r[1]));
}
DI f32x16 zero16() { f32x16 z; _Pragma("unroll") for (int i = 0; i < 16; ++i) z[i] = 0.f; return z; }
DI float phi(float y, float rs, float rsl) {
  return fmaxf(__builtin_fmaf(y, rs, 1e-6f), 1e-6f) + __builtin_amdgcn_fmed3f(__builtin_amdgcn_exp2f(y * rsl), 0.f, 1.f);
}

#define GLDS_HEAD "s_nop 4\n\ts_mov_b32 %0, m0\n\ts_mov_b32 m0, %3\n\ts_nop 0\n\t"
#define GLDS_OPS : "=&s"(sv) : "v"(voff), "s"(sbase), "s"(lds_base) : "memory"
DI void glds4(const char* sbase, unsigned voff, unsigned lds_base) {
  unsigned sv;
  lds_base = __builtin_amdgcn_readfirstlane(lds_base);
  asm volatile(GLDS_HEAD "global_load_lds_dwordx4 %1, %2\n\tglobal_load_lds_dwordx4 %1, %2 offset:1024\n\t"
               "global_load_lds_dwordx4 %1, %2 offset:2048\n\tglobal_load_lds_dwordx4 %1, %2 offset:3072\n\ts_mov_b32 m0, %0" GLDS_OPS);
}
DI void glds2(const char* sbase, unsigned voff, unsigned lds_base) {
  unsigned sv;
  lds_base = __builtin_amdgcn_readfirstlane(lds_base);
  asm volatile(GLDS_HEAD "global_load_lds_dwordx4 %1, %2\n\tglobal_load_lds_dwordx4 %1, %2 offset:1024\n\ts_mov_b32 m0, %0" GLDS_OPS);
}
DI void glds1(const char* sbase, unsigned voff, unsigned lds_base) {
  unsigned sv;
  lds_base = __builtin_amdgcn_readfirstlane(lds_base);
  asm volatile(GLDS_HEAD "global_load_lds_dwordx4 %1, %2\n\ts_mov_b32 m0, %0" GLDS_OPS);
}
#define BAR() asm volatile("s_waitcnt lgkmcnt(0)\n\ts_barrier" ::: "memory")
#define DRAIN() asm volatile("s_waitcnt vmcnt(0)" ::: "memory")
#define PIN(x) asm volatile("" : "+v"(x))
#define SBAR() __builtin_amdgcn_sched_barrier(0)

typedef __amdgpu_buffer_rsrc_t rsrc_t;
DI u32x4 ld128(rsrc_t rs, int voff, int soff) { return __builtin_amdgcn_raw_buffer_load_b128(rs, voff, soff, 16); }
DI u32x4 ldw(rsrc_t rs, int voff, int soff) { return __builtin_amdgcn_raw_buffer_load_b128(rs, voff, soff, 0); }
DI void st128(rsrc_t rs, int voff, int soff, u32x4 v) { __builtin_amdgcn_raw_buffer_store_b128(v, rs, voff + soff, 0, 16); }
DI void st64(rsrc_t rs, int voff, int soff, u32x2 v) { __builtin_amdgcn_raw_buffer_store_b64(v, rs, voff, soff, 16); }

DI void poll(char* ws, int line0, int n, unsigned want, int lane) {
  gu32* c = (gu32*)(ws + OFF_CTR) + (line0 + (lane < n ? lane : 0)) * 32;
  gu32* tmo = (gu32*)(ws + OFF_CTR) + CTR_TMO * 32;
#pragma clang loop unroll(disable)
  for (unsigned spins = 0;;) {
    if (__all(__hip_atomic_load(c, RLX_AGENT) >= want)) break;
    __builtin_amdgcn_s_sleep(1);
    if (++spins > (1u << 17)) { if (lane == 0) __hip_atomic_store(tmo, 1u, RLX_AGENT); break; }
    if ((spins & 255u) == 0u && __builtin_amdgcn_readfirstlane(__hip_atomic_load(tmo, RLX_AGENT))) break;
  }
  __builtin_amdgcn_fence(__ATOMIC_ACQUIRE, "wavefront");
}
DI void arrive(char* ws, int line0, int stride, int n, int lane) {
  if (lane < n) __hip_atomic_fetch_add((gu32*)(ws + OFF_CTR) + (line0 + lane * stride) * 32, 1u, RLX_AGENT);
}

constexpr unsigned POISON = 0xffffffffu;
DI u32x4 clean(u32x4 v) { _Pragma("unroll") for (int j = 0; j < 4; ++j) v[j] = min(v[j], POISON - 1u); return v; }
DI unsigned umax4(const u32x4& v, unsigned m) { return max(max(max(v[0], v[1]), m), max(v[2], v[3])); }
#define VERIFY(MAXEXPR, RELOAD) do { _Pragma("clang loop unroll(disable)") for (unsigned sp_ = 0; !__all((MAXEXPR) != POISON) && sp_ < (1u << 16); ++sp_) { \
    __builtin_amdgcn_s_sleep(2); asm volatile("" ::: "memory"); RELOAD; } } while (0)

#define VERIFY_FAST(MAXEXPR, RELOAD) do { if (__builtin_expect(!__all((MAXEXPR) != POISON), 0)) { \
    _Pragma("clang loop unroll(disable)") for (unsigned sp_ = 0; sp_ < (1u << 16); ++sp_) { \
      __builtin_amdgcn_s_sleep(2); asm volatile("" ::: "memory"); RELOAD; __builtin_amdgcn_s_waitcnt(0x0F70); if (__all((MAXEXPR) != POISON)) break; } } } while (0)

struct RowStat { float mu, rs; };
DI RowStat finish_stats(float s, float q) {
  const float mean = s * (1.f / DM);
  const float var = fmaxf(q * (1.f / DM) - mean * mean, 0.f);
  const float sd = bfround(__builtin_amdgcn_sqrtf(bfround(bfround(var) + 1e-5f)));
  RowStat o; o.mu = bfround(mean); o.rs = __builtin_amdgcn_rcpf(sd); return o;
}
DI RowStat row_stats(const u32x4& p) {
  return finish_stats(xhalf_sum(__uint_as_float(p[0]) + __uint_as_float(p[2])), xhalf_sum(__uint_as_float(p[1]) + __uint_as_float(p[3])));
}
DI float dot2(unsigned a, unsigned b, float c) {
  return __builtin_amdgcn_fdot2_f32_bf16(__builtin_bit_cast(bf16x2_t, a), __builtin_bit_cast(bf16x2_t, b), c, false);
}
DI float sum8(const u32x4& v) { float c = 0.f; _Pragma("unroll") for (int j = 0; j < 4; ++j) c = dot2(v[j], 0x3f803f80u, c); return c; }
DI float sq8(const u32x4& v) { float c = 0.f; _Pragma("unroll") for (int j = 0; j < 4; ++j) c = dot2(v[j], v[j], c); return c; }
template <int NW> DI void publish_stats(const LAS char* part, rsrc_t rs, int off, int lane) {
  f32x2 t = {0.f, 0.f};
#pragma unroll
  for (int i = 0; i < NW / 2; ++i) t += *(const LAS f32x2*)(part + ((lane >> 5) * (NW / 2) + i) * 256 + (lane & 31) * 8);
  const float s = xhalf_sum(t[0]), q = xhalf_sum(t[1]);
  if (lane < 32) st64(rs, lane * 32, off, u32x2{min(__float_as_uint(s), POISON - 1u), min(__float_as_uint(q), POISON - 1u)});
}
DI void put_hi(LAS char* p, const f32x16& a) {
  *(LAS f32x4*)p = f32x4{a[8], a[9], a[10], a[11]}; *(LAS f32x4*)(p + 1024) = f32x4{a[12], a[13], a[14], a[15]};
}
DI void add_lo(const LAS char* p, f32x16& a) {
  const f32x4 u = *(const LAS f32x4*)p, v = *(const LAS f32x4*)(p + 1024);
#pragma unroll
  for (int j = 0; j < 4; ++j) { a[j] += u[j]; a[4 + j] += v[j]; }
}
DI float max16(const f32x16& s) {
  float a = fmaxf(fmaxf(s[0], s[1]), s[2]), b = fmaxf(fmaxf(s[3], s[4]), s[5]);
  a = fmaxf(fmaxf(a, s[6]), s[7]); b = fmaxf(fmaxf(b, s[8]), s[9]);
  a = fmaxf(fmaxf(a, s[10]), s[11]); b = fmaxf(fmaxf(b, s[12]), s[13]);
  a = fmaxf(fmaxf(a, s[14]), s[15]);
  return fmaxf(a, b);
}

constexpr int PREP_BLOCKS = 714;
DI u32x4 tr_frag(const LAS char* a, int second) {
  const u32x2 lo = __builtin_bit_cast(u32x2, __builtin_amdgcn_ds_read_tr16_b64_v4i16((LAS s16x4*)a));
  const u32x2 hi = __builtin_bit_cast(u32x2, __builtin_amdgcn_ds_read_tr16_b64_v4i16((LAS s16x4*)(a + second)));
  return u32x4{lo[0], lo[1], hi[0], hi[1]};
}
__global__ __launch_bounds__(256) void k_prep(const uint16_t* __restrict__ x0, const uint16_t* __restrict__ wqkv,
                                              const uint16_t* __restrict__ woa, const uint16_t* __restrict__ wq,
                                              const uint16_t* __restrict__ wk, const uint16_t* __restrict__ wv,
                                              const uint16_t* __restrict__ wob, char* __restrict__ ws) {
  __shared__ __attribute__((aligned(16))) char tile_[9728];
  LAS char* smem = (LAS char*)tile_;
  asm volatile("" :: "s"(x0), "s"(wqkv), "s"(woa), "s"(wq), "s"(wk), "s"(wv), "s"(wob), "s"(ws));
  const rsrc_t rs = __builtin_amdgcn_make_buffer_rsrc(ws, 0, WS_END, 0x00020000);
  const int b = blockIdx.x, tid = threadIdx.x, lane = tid & 63, r = lane & 31, h = lane >> 5;
  const int w = __builtin_amdgcn_readfirstlane(tid >> 6), voff = lane * 16, c8 = tid & 7;
  if (b < 458) {
    const int cu = b < 66 ? ((b >> 1) < 24 ? b >> 1 : ((b >> 1) < 32 ? (b >> 1) + 8 : 48)) : (b - 66) >> 3;
    const uint16_t* src; int ld = 512;
    if (cu < 24) { src = wqkv + cu * 64 + c8 * 8; ld = 1536; }
    else if (cu == 48) { src = ((c8 & 2) ? wv : wk) + (c8 & 1) * 8; ld = 16; }
    else src = (cu < 32 ? woa : (cu < 40 ? wq : wob)) + (cu & 7) * 64 + c8 * 8;
    const int wt = cu < 40 ? 2 * cu : (cu < 48 ? 2 * cu + 1 : T_WKV);
    if (b < 66) {
      const int kb = b & 1;
      u32x4 v[8];
#pragma unroll
      for (int i = 0; i < 8; ++i) v[i] = *(const u32x4*)(src + (size_t)(kb * 256 + i * 32 + (tid >> 3)) * ld);
      f32x4 s0 = {0.f, 0.f, 0.f, 0.f}, s1 = s0;
#pragma unroll
      for (int i = 0; i < 8; ++i) {
        s0 += f32x4{bflo(v[i][0]), bfhi(v[i][0]), bflo(v[i][1]), bfhi(v[i][1])};
        s1 += f32x4{bflo(v[i][2]), bfhi(v[i][2]), bflo(v[i][3]), bfhi(v[i][3])};
      }
      *(LAS f32x4*)(smem + (tid >> 3) * 272 + c8 * 32) = s0; *(LAS f32x4*)(smem + (tid >> 3) * 272 + c8 * 32 + 16) = s1;
      __syncthreads();
      if (w == 0) {
        float t = 0.f;
#pragma unroll
        for (int i = 0; i < 32; ++i) t += *(const LAS float*)(smem + i * 272 + lane * 4);
        float* cs1 = (float*)(ws + OFF_CS1); float* cs3 = (float*)(ws + OFF_CS3);
        if (cu < 24) cs1[((wt & 15) + h) * 256 + kb * 128 + (wt >> 4) * 32 + r] = t;
        else if (cu < 40) cs3[((wt - T_WQ) >> 2) * 512 + kb * 256 + ((wt - T_WQ) & 3) * 32 + lane] = t;
        else if (h == 0) {
#pragma unroll
          for (int i = 0; i < 4; ++i) cs3[i * 512 + kb * 256 + 128 + r] = t;
        }
      }
    } else {
      const int k8 = (b - 66) & 7;
      u32x4 v[2];
#pragma unroll
      for (int i = 0; i < 2; ++i) v[i] = *(const u32x4*)(src + (size_t)(k8 * 64 + i * 32 + (tid >> 3)) * ld);
#pragma unroll
      for (int i = 0; i < 2; ++i) *(LAS u32x4*)(smem + (i * 32 + (tid >> 3)) * 144 + c8 * 16) = v[i];
      __syncthreads();
#pragma unroll
      for (int t = 0; t < 2; ++t) {
        const u32x4 f = tr_frag(smem + (w * 16 + 4 * h + ((lane & 15) >> 2)) * 144 + t * 64 + ((lane >> 4) & 1) * 32 + (lane & 3) * 8, 8 * 144);
        if (t == 0 || cu < 48) st128(rs, voff, OFF_W + ((wt + t) * 32 + k8 * 4 + w) * 1024, f);
      }
    }
  } else {
    const int u = b - 458, tt = u >> 2, kq = u & 3;
    u32x4 v[2];
#pragma unroll
    for (int i = 0; i < 2; ++i) v[i] = *(const u32x4*)(x0 + (size_t)(tt * 32 + i * 16 + (tid >> 4)) * DM + kq * 128 + (tid & 15) * 8);
    if (u * 256 + tid < ZERO_BYTES / 16) st128(rs, (u * 256 + tid) * 16, OFF_CTR, u32x4{0u, 0u, 0u, 0u});
#pragma unroll
    for (int i = 0; i < 2; ++i) *(LAS u32x4*)(smem + (i * 16 + (tid >> 4)) * 272 + (tid & 15) * 16) = v[i];
    __syncthreads();
    float s = 0.f, q = 0.f;
#pragma unroll
    for (int i = 0; i < 2; ++i) {
      const LAS char* xs = smem + r * 272 + (2 * w + i) * 32 + h * 8;
      const u32x2 e0 = *(const LAS u32x2*)xs, e1 = *(const LAS u32x2*)(xs + 16);
      const u32x4 f = clean(u32x4{e0[0], e0[1], e1[0], e1[1]});
      st128(rs, voff, OFF_XF + (tt * 32 + kq * 8 + 2 * w + i) * 1024, f);
      s += sum8(f); q += sq8(f);
    }
    s = xhalf_sum(s); q = xhalf_sum(q);
    if (h == 0) *(LAS f32x2*)(smem + 8704 + w * 256 + r * 8) = f32x2{s, q};
    __syncthreads();
    if (w == 0) publish_stats<4>(smem + 8704, rs, OFF_ST + tt * 1024 + kq * 8, lane);
  }
}

#define ATTN_ASM \
  "s_nop 4\n\t" \
  "s_mov_b32 %[m0s], m0\n\t" \
  "s_mov_b32 s96, %[m0a]\n\t" \
  "s_mov_b32 m0, s96\n\t" \
  "s_add_u32 s96, s96, 0x2000\n\t" \
  "global_load_lds_dwordx4 v95, %[wsb] sc1\n\t" \
  "global_load_lds_dwordx4 v95, %[wsb] offset:1024 sc1\n\t" \
  "v_add_u32 v95, 0x2000, v95\n\t" \
  "s_mov_b32 m0, s96\n\t" \
  "s_add_u32 s96, s96, 0x2000\n\t" \
  "global_load_lds_dwordx4 v95, %[wsb] sc1\n\t" \
  "global_load_lds_dwordx4 v95, %[wsb] offset:1024 sc1\n\t" \
  "v_add_u32 v95, 0x2000, v95\n\t" \
  "s_mov_b32 m0, s96\n\t" \
  "s_add_u32 s96, s96, 0x2000\n\t" \
  "global_load_lds_dwordx4 v95, %[wsb] sc1\n\t" \
  "global_load_lds_dwordx4 v95, %[wsb] offset:1024 sc1\n\t" \
  "v_add_u32 v95, 0x2000, v95\n\t" \
  "s_waitcnt vmcnt(2)\n\t" \
  "s_mov_b32 s97, 0\n\t" \
  "Lca%=:\n\t" \
  "ds_read_b128 v[48:51], v98 offset:0\n\t" \
  "ds_read_b128 v[52:55], v98 offset:1024\n\t" \
  "ds_read_b128 v[56:59], v98 offset:8192\n\t" \
  "ds_read_b128 v[60:63], v98 offset:9216\n\t" \
  "s_waitcnt lgkmcnt(0)\n\t" \
  "v_max3_u32 v48, v48, v49, v50\n\t" \
  "v_max3_u32 v48, v51, v52, v48\n\t" \
  "v_max3_u32 v48, v53, v54, v48\n\t" \
  "v_max3_u32 v48, v55, v56, v48\n\t" \
  "v_max3_u32 v48, v57, v58, v48\n\t" \
  "v_max3_u32 v48, v59, v60, v48\n\t" \
  "v_max3_u32 v48, v61, v62, v48\n\t" \
  "v_max3_u32 v48, v63, v50, v48\n\t" \
  "v_cmp_eq_u32 vcc, -1, v48\n\t" \
  "s_nop 1\n\t" \
  "s_cbranch_vccz Loa%=\n\t" \
  "s_add_u32 s97, s97, 1\n\t" \
  "s_cmp_gt_u32 s97, 0xffff\n\t" \
  "s_cbranch_scc1 Loa%=\n\t" \
  "s_sleep 2\n\t" \
  "s_add_u32 m0, %[m0a], 0\n\t" \
  "v_subrev_u32 v48, 24576, v95\n\t" \
  "global_load_lds_dwordx4 v48, %[wsb] sc1\n\t" \
  "global_load_lds_dwordx4 v48, %[wsb] offset:1024 sc1\n\t" \
  "s_add_u32 m0, %[m0a], 8192\n\t" \
  "v_subrev_u32 v48, 16384, v95\n\t" \
  "global_load_lds_dwordx4 v48, %[wsb] sc1\n\t" \
  "global_load_lds_dwordx4 v48, %[wsb] offset:1024 sc1\n\t" \
  "s_waitcnt vmcnt(0)\n\t" \
  "s_branch Lca%=\n\t" \
  "Loa%=:\n\t" \
  "s_barrier\n\t" \
  "ds_read_b128 v[64:67], v92 offset:0\n\t" \
  "ds_read_b128 v[68:71], v93 offset:0\n\t" \
  "ds_read_b128 v[72:75], v93 offset:0\n\t" \
  "s_mov_b32 %[n], 0\n\t" \
  "s_waitcnt lgkmcnt(0)\n\t" \
  "v_mfma_f32_32x32x16_bf16 v[32:47], v[64:67], v[84:87], 0\n\t" \
  "s_getpc_b64 s[92:93]\n\t" \
  "Lpc%=:\n\t" \
  "s_add_u32 s90, s92, Lb0%=-Lpc%=\n\t" \
  "s_addc_u32 s91, s93, 0\n\t" \
  "s_cmp_eq_u32 %[kh], 0\n\t" \
  "s_cselect_b32 s94, 0, Lb1%=-Lb0%=\n\t" \
  "s_add_u32 s90, s90, s94\n\t" \
  "s_addc_u32 s91, s91, 0\n\t" \
  "s_add_u32 s92, s92, Ltab%=-Lpc%=\n\t" \
  "s_addc_u32 s93, s93, 0\n\t" \
  "s_setprio 1\n\t" \
  "s_nop 5\n\t" \
  "s_setpc_b64 s[90:91]\n\t" \
  "Lb0%=:\n\t" \
  "s_setprio 2\n\t" \
  "v_exp_f32 v32, v32\n\t" \
  "v_exp_f32 v33, v33\n\t" \
  "s_waitcnt lgkmcnt(2)\n\t" \
  "v_exp_f32 v34, v34\n\t" \
  "v_mfma_f32_32x32x16_bf16 v[16:31], v[72:75], v[80:83], v[16:31]\n\t" \
  "ds_read_b128 v[72:75], v93 offset:512\n\t" \
  "v_exp_f32 v35, v35\n\t" \
  "v_cvt_pk_bf16_f32 v76, v32, v33\n\t" \
  "v_exp_f32 v36, v36\n\t" \
  "v_exp_f32 v37, v37\n\t" \
  "v_mfma_f32_32x32x16_bf16 v[48:63], v[64:67], v[88:91], 0\n\t" \
  "ds_read_b128 v[64:67], v92 offset:2048\n\t" \
  "v_cvt_pk_bf16_f32 v77, v34, v35\n\t" \
  "v_exp_f32 v38, v38\n\t" \
  "v_exp_f32 v39, v39\n\t" \
  "v_cvt_pk_bf16_f32 v78, v36, v37\n\t" \
  "v_exp_f32 v40, v40\n\t" \
  "v_exp_f32 v41, v41\n\t" \
  "v_cvt_pk_bf16_f32 v79, v38, v39\n\t" \
  "v_exp_f32 v42, v42\n\t" \
  "v_exp_f32 v43, v43\n\t" \
  "s_waitcnt lgkmcnt(2)\n\t" \
  "v_exp_f32 v44, v44\n\t" \
  "v_mfma_f32_32x32x16_bf16 v[0:15], v[68:71], v[76:79], v[0:15]\n\t" \
  "v_cvt_pk_bf16_f32 v80, v40, v41\n\t" \
  "v_exp_f32 v45, v45\n\t" \
  "v_cvt_pk_bf16_f32 v81, v42, v43\n\t" \
  "v_exp_f32 v46, v46\n\t" \
  "v_exp_f32 v47, v47\n\t" \
  "v_cvt_pk_bf16_f32 v82, v44, v45\n\t" \
  "v_cvt_pk_bf16_f32 v83, v46, v47\n\t" \
  "v_exp_f32 v48, v48\n\t" \
  "v_exp_f32 v49, v49\n\t" \
  "s_waitcnt lgkmcnt(1)\n\t" \
  "v_exp_f32 v50, v50\n\t" \
  "v_mfma_f32_32x32x16_bf16 v[0:15], v[72:75], v[80:83], v[0:15]\n\t" \
  "v_exp_f32 v51, v51\n\t" \
  "v_cvt_pk_bf16_f32 v76, v48, v49\n\t" \
  "v_exp_f32 v52, v52\n\t" \
  "v_exp_f32 v53, v53\n\t" \
  "s_waitcnt lgkmcnt(0)\n\t" \
  "v_exp_f32 v54, v54\n\t" \
  "v_mfma_f32_32x32x16_bf16 v[32:47], v[64:67], v[84:87], 0\n\t" \
  "v_cvt_pk_bf16_f32 v77, v50, v51\n\t" \
  "v_exp_f32 v55, v55\n\t" \
  "v_cvt_pk_bf16_f32 v78, v52, v53\n\t" \
  "v_exp_f32 v56, v56\n\t" \
  "v_exp_f32 v57, v57\n\t" \
  "v_cvt_pk_bf16_f32 v79, v54, v55\n\t" \
  "v_exp_f32 v58, v58\n\t" \
  "v_exp_f32 v59, v59\n\t" \
  "v_mfma_f32_32x32x16_bf16 v[16:31], v[68:71], v[76:79], v[16:31]\n\t" \
  "ds_read_b128 v[68:71], v93 offset:2048\n\t" \
  "v_cvt_pk_bf16_f32 v80, v56, v57\n\t" \
  "v_exp_f32 v60, v60\n\t" \
  "v_exp_f32 v61, v61\n\t" \
  "v_cvt_pk_bf16_f32 v81, v58, v59\n\t" \
  "v_exp_f32 v62, v62\n\t" \
  "v_exp_f32 v63, v63\n\t" \
  "v_cvt_pk_bf16_f32 v82, v60, v61\n\t" \
  "v_cvt_pk_bf16_f32 v83, v62, v63\n\t" \
  "v_exp_f32 v32, v32\n\t" \
  "v_exp_f32 v33, v33\n\t" \
  "s_waitcnt lgkmcnt(2)\n\t" \
  "v_exp_f32 v34, v34\n\t" \
  "v_mfma_f32_32x32x16_bf16 v[16:31], v[72:75], v[80:83], v[16:31]\n\t" \
  "ds_read_b128 v[72:75], v93 offset:2560\n\t" \
  "v_exp_f32 v35, v35\n\t" \
  "v_cvt_pk_bf16_f32 v76, v32, v33\n\t" \
  "v_exp_f32 v36, v36\n\t" \
  "v_exp_f32 v37, v37\n\t" \
  "v_mfma_f32_32x32x16_bf16 v[48:63], v[64:67], v[88:91], 0\n\t" \
  "ds_read_b128 v[64:67], v92 offset:4096\n\t" \
  "v_cvt_pk_bf16_f32 v77, v34, v35\n\t" \
  "v_exp_f32 v38, v38\n\t" \
  "v_exp_f32 v39, v39\n\t" \
  "v_cvt_pk_bf16_f32 v78, v36, v37\n\t" \
  "v_exp_f32 v40, v40\n\t" \
  "v_exp_f32 v41, v41\n\t" \
  "v_cvt_pk_bf16_f32 v79, v38, v39\n\t" \
  "v_exp_f32 v42, v42\n\t" \
  "v_exp_f32 v43, v43\n\t" \
  "s_waitcnt lgkmcnt(2)\n\t" \
  "v_exp_f32 v44, v44\n\t" \
  "v_mfma_f32_32x32x16_bf16 v[0:15], v[68:71], v[76:79], v[0:15]\n\t" \
  "v_cvt_pk_bf16_f32 v80, v40, v41\n\t" \
  "v_exp_f32 v45, v45\n\t" \
  "v_cvt_pk_bf16_f32 v81, v42, v43\n\t" \
  "v_exp_f32 v46, v46\n\t" \
  "v_exp_f32 v47, v47\n\t" \
  "v_cvt_pk_bf16_f32 v82, v44, v45\n\t" \
  "v_cvt_pk_bf16_f32 v83, v46, v47\n\t" \
  "s_setprio 0\n\t" \
  "v_exp_f32 v48, v48\n\t" \
  "v_exp_f32 v49, v49\n\t" \
  "s_waitcnt lgkmcnt(1)\n\t" \
  "v_exp_f32 v50, v50\n\t" \
  "v_mfma_f32_32x32x16_bf16 v[0:15], v[72:75], v[80:83], v[0:15]\n\t" \
  "v_exp_f32 v51, v51\n\t" \
  "v_cvt_pk_bf16_f32 v76, v48, v49\n\t" \
  "v_exp_f32 v52, v52\n\t" \
  "v_exp_f32 v53, v53\n\t" \
  "s_waitcnt lgkmcnt(0)\n\t" \
  "v_exp_f32 v54, v54\n\t" \
  "v_mfma_f32_32x32x16_bf16 v[32:47], v[64:67], v[84:87], 0\n\t" \
  "v_cvt_pk_bf16_f32 v77, v50, v51\n\t" \
  "v_exp_f32 v55, v55\n\t" \
  "v_cvt_pk_bf16_f32 v78, v52, v53\n\t" \
  "v_exp_f32 v56, v56\n\t" \
  "v_exp_f32 v57, v57\n\t" \
  "v_cvt_pk_bf16_f32 v79, v54, v55\n\t" \
  "v_exp_f32 v58, v58\n\t" \
  "v_exp_f32 v59, v59\n\t" \
  "v_mfma_f32_32x32x16_bf16 v[16:31], v[68:71], v[76:79], v[16:31]\n\t" \
  "ds_read_b128 v[68:71], v93 offset:4096\n\t" \
  "v_cvt_pk_bf16_f32 v80, v56, v57\n\t" \
  "v_exp_f32 v60, v60\n\t" \
  "v_exp_f32 v61, v61\n\t" \
  "v_cvt_pk_bf16_f32 v81, v58, v59\n\t" \
  "v_exp_f32 v62, v62\n\t" \
  "v_exp_f32 v63, v63\n\t" \
  "v_cvt_pk_bf16_f32 v82, v60, v61\n\t" \
  "v_cvt_pk_bf16_f32 v83, v62, v63\n\t" \
  "s_add_u32 %[n], %[n], 1\n\t" \
  "v_add_u32 v92, 0x1000, v92\n\t" \
  "s_lshl_b32 s94, %[n], 6\n\t" \
  "v_add_u32 v93, v94, v93\n\t" \
  "s_add_u32 s94, s92, s94\n\t" \
  "s_addc_u32 s95, s93, 0\n\t" \
  "s_setpc_b64 s[94:95]\n\t" \
  "Lb1%=:\n\t" \
  "v_exp_f32 v32, v32\n\t" \
  "v_exp_f32 v33, v33\n\t" \
  "s_waitcnt lgkmcnt(2)\n\t" \
  "v_exp_f32 v34, v34\n\t" \
  "v_mfma_f32_32x32x16_bf16 v[16:31], v[72:75], v[80:83], v[16:31]\n\t" \
  "ds_read_b128 v[72:75], v93 offset:512\n\t" \
  "v_exp_f32 v35, v35\n\t" \
  "v_cvt_pk_bf16_f32 v76, v32, v33\n\t" \
  "v_exp_f32 v36, v36\n\t" \
  "v_exp_f32 v37, v37\n\t" \
  "v_mfma_f32_32x32x16_bf16 v[48:63], v[64:67], v[88:91], 0\n\t" \
  "ds_read_b128 v[64:67], v92 offset:2048\n\t" \
  "v_cvt_pk_bf16_f32 v77, v34, v35\n\t" \
  "v_exp_f32 v38, v38\n\t" \
  "v_exp_f32 v39, v39\n\t" \
  "v_cvt_pk_bf16_f32 v78, v36, v37\n\t" \
  "v_exp_f32 v40, v40\n\t" \
  "v_exp_f32 v41, v41\n\t" \
  "v_cvt_pk_bf16_f32 v79, v38, v39\n\t" \
  "v_exp_f32 v42, v42\n\t" \
  "v_exp_f32 v43, v43\n\t" \
  "s_waitcnt lgkmcnt(2)\n\t" \
  "v_exp_f32 v44, v44\n\t" \
  "v_mfma_f32_32x32x16_bf16 v[0:15], v[68:71], v[76:79], v[0:15]\n\t" \
  "v_cvt_pk_bf16_f32 v80, v40, v41\n\t" \
  "v_exp_f32 v45, v45\n\t" \
  "v_cvt_pk_bf16_f32 v81, v42, v43\n\t" \
  "v_exp_f32 v46, v46\n\t" \
  "v_exp_f32 v47, v47\n\t" \
  "v_cvt_pk_bf16_f32 v82, v44, v45\n\t" \
  "v_cvt_pk_bf16_f32 v83, v46, v47\n\t" \
  "v_exp_f32 v48, v48\n\t" \
  "v_exp_f32 v49, v49\n\t" \
  "s_waitcnt lgkmcnt(1)\n\t" \
  "v_exp_f32 v50, v50\n\t" \
  "v_mfma_f32_32x32x16_bf16 v[0:15], v[72:75], v[80:83], v[0:15]\n\t" \
  "v_exp_f32 v51, v51\n\t" \
  "v_cvt_pk_bf16_f32 v76, v48, v49\n\t" \
  "v_exp_f32 v52, v52\n\t" \
  "v_exp_f32 v53, v53\n\t" \
  "s_waitcnt lgkmcnt(0)\n\t" \
  "v_exp_f32 v54, v54\n\t" \
  "v_mfma_f32_32x32x16_bf16 v[32:47], v[64:67], v[84:87], 0\n\t" \
  "v_cvt_pk_bf16_f32 v77, v50, v51\n\t" \
  "v_exp_f32 v55, v55\n\t" \
  "v_cvt_pk_bf16_f32 v78, v52, v53\n\t" \
  "v_exp_f32 v56, v56\n\t" \
  "v_exp_f32 v57, v57\n\t" \
  "v_cvt_pk_bf16_f32 v79, v54, v55\n\t" \
  "v_exp_f32 v58, v58\n\t" \
  "v_exp_f32 v59, v59\n\t" \
  "v_mfma_f32_32x32x16_bf16 v[16:31], v[68:71], v[76:79], v[16:31]\n\t" \
  "ds_read_b128 v[68:71], v93 offset:2048\n\t" \
  "v_cvt_pk_bf16_f32 v80, v56, v57\n\t" \
  "v_exp_f32 v60, v60\n\t" \
  "v_exp_f32 v61, v61\n\t" \
  "v_cvt_pk_bf16_f32 v81, v58, v59\n\t" \
  "v_exp_f32 v62, v62\n\t" \
  "v_exp_f32 v63, v63\n\t" \
  "v_cvt_pk_bf16_f32 v82, v60, v61\n\t" \
  "v_cvt_pk_bf16_f32 v83, v62, v63\n\t" \
  "v_exp_f32 v32, v32\n\t" \
  "v_exp_f32 v33, v33\n\t" \
  "s_waitcnt lgkmcnt(2)\n\t" \
  "v_exp_f32 v34, v34\n\t" \
  "v_mfma_f32_32x32x16_bf16 v[16:31], v[72:75], v[80:83], v[16:31]\n\t" \
  "ds_read_b128 v[72:75], v93 offset:2560\n\t" \
  "v_exp_f32 v35, v35\n\t" \
  "v_cvt_pk_bf16_f32 v76, v32, v33\n\t" \
  "v_exp_f32 v36, v36\n\t" \
  "v_exp_f32 v37, v37\n\t" \
  "v_mfma_f32_32x32x16_bf16 v[48:63], v[64:67], v[88:91], 0\n\t" \
  "ds_read_b128 v[64:67], v92 offset:4096\n\t" \
  "v_cvt_pk_bf16_f32 v77, v34, v35\n\t" \
  "v_exp_f32 v38, v38\n\t" \
  "v_exp_f32 v39, v39\n\t" \
  "v_cvt_pk_bf16_f32 v78, v36, v37\n\t" \
  "v_exp_f32 v40, v40\n\t" \
  "v_exp_f32 v41, v41\n\t" \
  "v_cvt_pk_bf16_f32 v79, v38, v39\n\t" \
  "v_exp_f32 v42, v42\n\t" \
  "v_exp_f32 v43, v43\n\t" \
  "s_waitcnt lgkmcnt(2)\n\t" \
  "v_exp_f32 v44, v44\n\t" \
  "v_mfma_f32_32x32x16_bf16 v[0:15], v[68:71], v[76:79], v[0:15]\n\t" \
  "v_cvt_pk_bf16_f32 v80, v40, v41\n\t" \
  "v_exp_f32 v45, v45\n\t" \
  "v_cvt_pk_bf16_f32 v81, v42, v43\n\t" \
  "v_exp_f32 v46, v46\n\t" \
  "v_exp_f32 v47, v47\n\t" \
  "v_cvt_pk_bf16_f32 v82, v44, v45\n\t" \
  "v_cvt_pk_bf16_f32 v83, v46, v47\n\t" \
  "v_exp_f32 v48, v48\n\t" \
  "v_exp_f32 v49, v49\n\t" \
  "s_waitcnt lgkmcnt(1)\n\t" \
  "v_exp_f32 v50, v50\n\t" \
  "v_mfma_f32_32x32x16_bf16 v[0:15], v[72:75], v[80:83], v[0:15]\n\t" \
  "v_exp_f32 v51, v51\n\t" \
  "v_cvt_pk_bf16_f32 v76, v48, v49\n\t" \
  "v_exp_f32 v52, v52\n\t" \
  "v_exp_f32 v53, v53\n\t" \
  "s_waitcnt lgkmcnt(0)\n\t" \
  "v_exp_f32 v54, v54\n\t" \
  "v_mfma_f32_32x32x16_bf16 v[32:47], v[64:67], v[84:87], 0\n\t" \
  "v_cvt_pk_bf16_f32 v77, v50, v51\n\t" \
  "v_exp_f32 v55, v55\n\t" \
  "v_cvt_pk_bf16_f32 v78, v52, v53\n\t" \
  "v_exp_f32 v56, v56\n\t" \
  "v_exp_f32 v57, v57\n\t" \
  "v_cvt_pk_bf16_f32 v79, v54, v55\n\t" \
  "v_exp_f32 v58, v58\n\t" \
  "v_exp_f32 v59, v59\n\t" \
  "v_mfma_f32_32x32x16_bf16 v[16:31], v[68:71], v[76:79], v[16:31]\n\t" \
  "ds_read_b128 v[68:71], v93 offset:4096\n\t" \
  "v_cvt_pk_bf16_f32 v80, v56, v57\n\t" \
  "v_exp_f32 v60, v60\n\t" \
  "v_exp_f32 v61, v61\n\t" \
  "v_cvt_pk_bf16_f32 v81, v58, v59\n\t" \
  "v_exp_f32 v62, v62\n\t" \
  "v_exp_f32 v63, v63\n\t" \
  "v_cvt_pk_bf16_f32 v82, v60, v61\n\t" \
  "v_cvt_pk_bf16_f32 v83, v62, v63\n\t" \
  "s_add_u32 %[n], %[n], 1\n\t" \
  "v_add_u32 v92, 0x1000, v92\n\t" \
  "s_lshl_b32 s94, %[n], 6\n\t" \
  "v_add_u32 v93, v94, v93\n\t" \
  "s_add_u32 s94, s92, s94\n\t" \
  "s_addc_u32 s95, s93, 0\n\t" \
  "s_setpc_b64 s[94:95]\n\t" \
  ".p2align 6\n\t" \
  "Ltab%=:\n\t" \
  "s_nop 0\n\t" \
  ".p2align 6\n\t" \
  "s_branch Lh1%=\n\t" \
  ".p2align 6\n\t" \
  "s_branch Lh2%=\n\t" \
  ".p2align 6\n\t" \
  "s_barrier\n\t" \
  "s_mov_b32 m0, s96\n\t" \
  "s_add_u32 s96, s96, 0x2000\n\t" \
  "global_load_lds_dwordx4 v95, %[wsb] sc1\n\t" \
  "global_load_lds_dwordx4 v95, %[wsb] offset:1024 sc1\n\t" \
  "v_add_u32 v95, 0x2000, v95\n\t" \
  "s_setpc_b64 s[90:91]\n\t" \
  ".p2align 6\n\t" \
  "s_branch Lh4%=\n\t" \
  ".p2align 6\n\t" \
  "s_branch Lh5%=\n\t" \
  ".p2align 6\n\t" \
  "s_setpc_b64 s[90:91]\n\t" \
  ".p2align 6\n\t" \
  "s_barrier\n\t" \
  "s_mov_b32 m0, %[m0s]\n\t" \
  "global_load_dwordx4 v[128:131], v96, %[wsb]\n\t" \
  "global_load_dwordx4 v[132:135], v96, %[wsb] offset:1024\n\t" \
  "v_add_u32 v96, 0x800, v96\n\t" \
  "global_load_dwordx4 v[192:195], v97, %[wsb] offset:0\n\t" \
  "global_load_dwordx4 v[196:199], v97, %[wsb] offset:1024\n\t" \
  "s_setpc_b64 s[90:91]\n\t" \
  ".p2align 6\n\t" \
  "global_load_dwordx4 v[136:139], v96, %[wsb]\n\t" \
  "global_load_dwordx4 v[140:143], v96, %[wsb] offset:1024\n\t" \
  "v_add_u32 v96, 0x800, v96\n\t" \
  "global_load_dwordx4 v[200:203], v97, %[wsb] offset:2048\n\t" \
  "global_load_dwordx4 v[204:207], v97, %[wsb] offset:3072\n\t" \
  "v_add_u32 v97, 0x80000, v97\n\t" \
  "s_setpc_b64 s[90:91]\n\t" \
  ".p2align 6\n\t" \
  "global_load_dwordx4 v[144:147], v96, %[wsb]\n\t" \
  "global_load_dwordx4 v[148:151], v96, %[wsb] offset:1024\n\t" \
  "v_add_u32 v96, 0x800, v96\n\t" \
  "global_load_dwordx4 v[208:211], v97, %[wsb] offset:0\n\t" \
  "global_load_dwordx4 v[212:215], v97, %[wsb] offset:1024\n\t" \
  "s_setpc_b64 s[90:91]\n\t" \
  ".p2align 6\n\t" \
  "global_load_dwordx4 v[152:155], v96, %[wsb]\n\t" \
  "global_load_dwordx4 v[156:159], v96, %[wsb] offset:1024\n\t" \
  "v_add_u32 v96, 0x800, v96\n\t" \
  "global_load_dwordx4 v[216:219], v97, %[wsb] offset:2048\n\t" \
  "global_load_dwordx4 v[220:223], v97, %[wsb] offset:3072\n\t" \
  "v_add_u32 v97, 0x80000, v97\n\t" \
  "s_setpc_b64 s[90:91]\n\t" \
  ".p2align 6\n\t" \
  "global_load_dwordx4 v[160:163], v96, %[wsb]\n\t" \
  "global_load_dwordx4 v[164:167], v96, %[wsb] offset:1024\n\t" \
  "v_add_u32 v96, 0x800, v96\n\t" \
  "global_load_dwordx4 v[224:227], v97, %[wsb] offset:0\n\t" \
  "global_load_dwordx4 v[228:231], v97, %[wsb] offset:1024\n\t" \
  "s_setpc_b64 s[90:91]\n\t" \
  ".p2align 6\n\t" \
  "global_load_dwordx4 v[168:171], v96, %[wsb]\n\t" \
  "global_load_dwordx4 v[172:175], v96, %[wsb] offset:1024\n\t" \
  "v_add_u32 v96, 0x800, v96\n\t" \
  "global_load_dwordx4 v[232:235], v97, %[wsb] offset:2048\n\t" \
  "global_load_dwordx4 v[236:239], v97, %[wsb] offset:3072\n\t" \
  "v_add_u32 v97, 0x80000, v97\n\t" \
  "s_setpc_b64 s[90:91]\n\t" \
  ".p2align 6\n\t" \
  "global_load_dwordx4 v[176:179], v96, %[wsb]\n\t" \
  "global_load_dwordx4 v[180:183], v96, %[wsb] offset:1024\n\t" \
  "v_add_u32 v96, 0x800, v96\n\t" \
  "s_setpc_b64 s[90:91]\n\t" \
  ".p2align 6\n\t" \
  "global_load_dwordx4 v[184:187], v96, %[wsb]\n\t" \
  "global_load_dwordx4 v[188:191], v96, %[wsb] offset:1024\n\t" \
  "v_add_u32 v96, 0x800, v96\n\t" \
  "s_setpc_b64 s[90:91]\n\t" \
  ".p2align 6\n\t" \
  "s_setpc_b64 s[90:91]\n\t" \
  ".p2align 6\n\t" \
  "s_branch Lend%=\n\t" \
  "Lh1%=:\n\t" \
  "s_waitcnt vmcnt(0)\n\t" \
  "s_mov_b32 s97, 0\n\t" \
  "Lch1%=:\n\t" \
  "ds_read_b128 v[48:51], v98 offset:16384\n\t" \
  "ds_read_b128 v[52:55], v98 offset:17408\n\t" \
  "s_waitcnt lgkmcnt(0)\n\t" \
  "v_max3_u32 v48, v48, v49, v50\n\t" \
  "v_max3_u32 v48, v51, v52, v48\n\t" \
  "v_max3_u32 v48, v53, v54, v48\n\t" \
  "v_max3_u32 v48, v55, v50, v48\n\t" \
  "v_cmp_eq_u32 vcc, -1, v48\n\t" \
  "s_nop 1\n\t" \
  "s_cbranch_vccz Loh1%=\n\t" \
  "s_add_u32 s97, s97, 1\n\t" \
  "s_cmp_gt_u32 s97, 0xffff\n\t" \
  "s_cbranch_scc1 Loh1%=\n\t" \
  "s_sleep 2\n\t" \
  "s_add_u32 m0, %[m0a], 16384\n\t" \
  "v_subrev_u32 v48, 8192, v95\n\t" \
  "global_load_lds_dwordx4 v48, %[wsb] sc1\n\t" \
  "global_load_lds_dwordx4 v48, %[wsb] offset:1024 sc1\n\t" \
  "s_waitcnt vmcnt(0)\n\t" \
  "s_branch Lch1%=\n\t" \
  "Loh1%=:\n\t" \
  "s_mov_b32 m0, s96\n\t" \
  "s_add_u32 s96, s96, 0x2000\n\t" \
  "global_load_lds_dwordx4 v95, %[wsb] sc1\n\t" \
  "global_load_lds_dwordx4 v95, %[wsb] offset:1024 sc1\n\t" \
  "v_add_u32 v95, 0x2000, v95\n\t" \
  "s_mov_b32 m0, s96\n\t" \
  "s_add_u32 s96, s96, 0x2000\n\t" \
  "global_load_lds_dwordx4 v95, %[wsb] sc1\n\t" \
  "global_load_lds_dwordx4 v95, %[wsb] offset:1024 sc1\n\t" \
  "v_add_u32 v95, 0x2000, v95\n\t" \
  "s_setpc_b64 s[90:91]\n\t" \
  "Lh2%=:\n\t" \
  "s_waitcnt vmcnt(0)\n\t" \
  "s_mov_b32 s97, 0\n\t" \
  "Lch2%=:\n\t" \
  "ds_read_b128 v[48:51], v98 offset:24576\n\t" \
  "ds_read_b128 v[52:55], v98 offset:25600\n\t" \
  "ds_read_b128 v[56:59], v98 offset:32768\n\t" \
  "ds_read_b128 v[60:63], v98 offset:33792\n\t" \
  "s_waitcnt lgkmcnt(0)\n\t" \
  "v_max3_u32 v48, v48, v49, v50\n\t" \
  "v_max3_u32 v48, v51, v52, v48\n\t" \
  "v_max3_u32 v48, v53, v54, v48\n\t" \
  "v_max3_u32 v48, v55, v56, v48\n\t" \
  "v_max3_u32 v48, v57, v58, v48\n\t" \
  "v_max3_u32 v48, v59, v60, v48\n\t" \
  "v_max3_u32 v48, v61, v62, v48\n\t" \
  "v_max3_u32 v48, v63, v50, v48\n\t" \
  "v_cmp_eq_u32 vcc, -1, v48\n\t" \
  "s_nop 1\n\t" \
  "s_cbranch_vccz Loh2%=\n\t" \
  "s_add_u32 s97, s97, 1\n\t" \
  "s_cmp_gt_u32 s97, 0xffff\n\t" \
  "s_cbranch_scc1 Loh2%=\n\t" \
  "s_sleep 2\n\t" \
  "s_add_u32 m0, %[m0a], 24576\n\t" \
  "v_subrev_u32 v48, 16384, v95\n\t" \
  "global_load_lds_dwordx4 v48, %[wsb] sc1\n\t" \
  "global_load_lds_dwordx4 v48, %[wsb] offset:1024 sc1\n\t" \
  "s_add_u32 m0, %[m0a], 32768\n\t" \
  "v_subrev_u32 v48, 8192, v95\n\t" \
  "global_load_lds_dwordx4 v48, %[wsb] sc1\n\t" \
  "global_load_lds_dwordx4 v48, %[wsb] offset:1024 sc1\n\t" \
  "s_waitcnt vmcnt(0)\n\t" \
  "s_branch Lch2%=\n\t" \
  "Loh2%=:\n\t" \
  "s_mov_b32 m0, s96\n\t" \
  "s_add_u32 s96, s96, 0x2000\n\t" \
  "global_load_lds_dwordx4 v95, %[wsb] sc1\n\t" \
  "global_load_lds_dwordx4 v95, %[wsb] offset:1024 sc1\n\t" \
  "v_add_u32 v95, 0x2000, v95\n\t" \
  "s_mov_b32 m0, s96\n\t" \
  "s_add_u32 s96, s96, 0x2000\n\t" \
  "global_load_lds_dwordx4 v95, %[wsb] sc1\n\t" \
  "global_load_lds_dwordx4 v95, %[wsb] offset:1024 sc1\n\t" \
  "v_add_u32 v95, 0x2000, v95\n\t" \
  "s_setpc_b64 s[90:91]\n\t" \
  "Lh4%=:\n\t" \
  "s_waitcnt vmcnt(2)\n\t" \
  "s_mov_b32 s97, 0\n\t" \
  "Lch4%=:\n\t" \
  "ds_read_b128 v[48:51], v98 offset:40960\n\t" \
  "ds_read_b128 v[52:55], v98 offset:41984\n\t" \
  "ds_read_b128 v[56:59], v98 offset:49152\n\t" \
  "ds_read_b128 v[60:63], v98 offset:50176\n\t" \
  "s_waitcnt lgkmcnt(0)\n\t" \
  "v_max3_u32 v48, v48, v49, v50\n\t" \
  "v_max3_u32 v48, v51, v52, v48\n\t" \
  "v_max3_u32 v48, v53, v54, v48\n\t" \
  "v_max3_u32 v48, v55, v56, v48\n\t" \
  "v_max3_u32 v48, v57, v58, v48\n\t" \
  "v_max3_u32 v48, v59, v60, v48\n\t" \
  "v_max3_u32 v48, v61, v62, v48\n\t" \
  "v_max3_u32 v48, v63, v50, v48\n\t" \
  "v_cmp_eq_u32 vcc, -1, v48\n\t" \
  "s_nop 1\n\t" \
  "s_cbranch_vccz Loh4%=\n\t" \
  "s_add_u32 s97, s97, 1\n\t" \
  "s_cmp_gt_u32 s97, 0xffff\n\t" \
  "s_cbranch_scc1 Loh4%=\n\t" \
  "s_sleep 2\n\t" \
  "s_add_u32 m0, %[m0a], 40960\n\t" \
  "v_subrev_u32 v48, 24576, v95\n\t" \
  "global_load_lds_dwordx4 v48, %[wsb] sc1\n\t" \
  "global_load_lds_dwordx4 v48, %[wsb] offset:1024 sc1\n\t" \
  "s_add_u32 m0, %[m0a], 49152\n\t" \
  "v_subrev_u32 v48, 16384, v95\n\t" \
  "global_load_lds_dwordx4 v48, %[wsb] sc1\n\t" \
  "global_load_lds_dwordx4 v48, %[wsb] offset:1024 sc1\n\t" \
  "s_waitcnt vmcnt(0)\n\t" \
  "s_branch Lch4%=\n\t" \
  "Loh4%=:\n\t" \
  "s_setpc_b64 s[90:91]\n\t" \
  "Lh5%=:\n\t" \
  "s_waitcnt vmcnt(0)\n\t" \
  "s_mov_b32 s97, 0\n\t" \
  "Lch5%=:\n\t" \
  "ds_read_b128 v[48:51], v98 offset:57344\n\t" \
  "ds_read_b128 v[52:55], v98 offset:58368\n\t" \
  "s_waitcnt lgkmcnt(0)\n\t" \
  "v_max3_u32 v48, v48, v49, v50\n\t" \
  "v_max3_u32 v48, v51, v52, v48\n\t" \
  "v_max3_u32 v48, v53, v54, v48\n\t" \
  "v_max3_u32 v48, v55, v50, v48\n\t" \
  "v_cmp_eq_u32 vcc, -1, v48\n\t" \
  "s_nop 1\n\t" \
  "s_cbranch_vccz Loh5%=\n\t" \
  "s_add_u32 s97, s97, 1\n\t" \
  "s_cmp_gt_u32 s97, 0xffff\n\t" \
  "s_cbranch_scc1 Loh5%=\n\t" \
  "s_sleep 2\n\t" \
  "s_add_u32 m0, %[m0a], 57344\n\t" \
  "v_subrev_u32 v48, 8192, v95\n\t" \
  "global_load_lds_dwordx4 v48, %[wsb] sc1\n\t" \
  "global_load_lds_dwordx4 v48, %[wsb] offset:1024 sc1\n\t" \
  "s_waitcnt vmcnt(0)\n\t" \
  "s_branch Lch5%=\n\t" \
  "Loh5%=:\n\t" \
  "s_setpc_b64 s[90:91]\n\t" \
  "Lend%=:\n\t" \
  "s_waitcnt lgkmcnt(0)\n\t" \
  "v_mfma_f32_32x32x16_bf16 v[16:31], v[72:75], v[80:83], v[16:31]\n\t" \
  "s_setprio 0\n\t" \
  "s_waitcnt vmcnt(0)\n\t" \
  "s_nop 7\n\t" \
  "s_nop 7\n\t" \
  ""

template <class W, class H> DI f32x16 gemm_half(const LAS char* act, W wf, H hook) {
  f32x16 acc = zero16();
  u32x4 a[16];
#pragma unroll
  for (int i = 0; i < 4; ++i) a[i] = *(const LAS u32x4*)(act + i * 1024);
#pragma unroll
  for (int i = 0; i < 16; ++i) {
    SBAR();
    if (i + 4 < 16) a[i + 4] = *(const LAS u32x4*)(act + (i + 4) * 1024);
    acc = MFMA(wf(i), a[i], acc);
    SBAR();
    hook(i);
  }
  SBAR();
  return acc;
}
DI f32x4 lds2(const LAS char* p, int half) { return *(const LAS f32x4*)p + *(const LAS f32x4*)(p + half); }
DI void resid_epilogue(const f32x16& acc, u32x4& xr, rsrc_t rs, int xoff, LAS char* part, uint16_t* out, bool final_, int tt, int ks, int lane) {
  const int r = lane & 31, h = lane >> 5;
  const u32x4 y = pack8(acc, 0);
#pragma unroll
  for (int j = 0; j < 4; ++j) xr[j] = cvtpk(bflo(xr[j]) + bflo(y[j]), bfhi(xr[j]) + bfhi(y[j]));
  xr = clean(xr);
  if (final_) {
    auto a = __builtin_amdgcn_permlane32_swap(xr[0], xr[2], false, false);
    auto c = __builtin_amdgcn_permlane32_swap(xr[1], xr[3], false, false);
    *(u32x4*)(out + (size_t)(tt * 32 + r) * DM + ks * 16 + 8 * h) = u32x4{a[0], c[0], a[1], c[1]};
  } else {
    st128(rs, lane * 16, xoff + (tt * 32 + ks) * 1024, xr);
    const float s = xhalf_sum(sum8(xr)), q = xhalf_sum(sq8(xr));
    if (h == 0) *(LAS f32x2*)(part + r * 8) = f32x2{s, q};
  }
}

#define QPUB 5
constexpr int NEARLY = 12;
__global__ __launch_bounds__(512, 2) void k_mega(char* ws, uint16_t* out) {
  extern __shared__ __attribute__((aligned(16))) char smem_[];
  LAS char* smem = (LAS char*)smem_;
  const unsigned lds0 = (unsigned)(uintptr_t)smem;
  const rsrc_t rs = __builtin_amdgcn_make_buffer_rsrc(ws, 0, WS_END, 0x00020000);
  const int w_ = __builtin_amdgcn_readfirstlane(threadIdx.x >> 6);
#define IDS() int tid = threadIdx.x, w = w_, b = blockIdx.x; asm volatile("" : "+v"(tid)); asm volatile("" : "+s"(w), "+s"(b)); \
  const int lane = tid & 63, r = lane & 31, h = lane >> 5, w4 = w & 3, kh = w >> 2, xcd = b & 7, idx = b >> 3; \
  const int tt = xcd * 8 + (idx >> 2), cb = idx & 3, nt = cb * 4 + w4;                 \
  const int panel = (xcd & 3) * 4 + (idx & 3), hp = (xcd >> 2) * 8 + (idx >> 2);       \
  const unsigned voff = lane * 16, vrot = voff ^ (kh << 8); \
  (void)r; (void)h; (void)tt; (void)nt; (void)panel; (void)hp; (void)voff; (void)vrot
  u32x4 xr;
  STAMP_DECL;
  {
    IDS();
    STAMP(0);
#pragma unroll
    for (int t = 0; t < 3; ++t) glds4(ws, OFF_W + ((t * 16 + hp) * 32 + 4 * w) * 1024 + voff, lds0 + L_WL + (t * 32 + 4 * w) * 1024);
    xr = ld128(rs, voff, OFF_XF + (tt * 32 + 2 * nt + kh) * 1024);
  }

#pragma unroll 1
  for (int L = 0; L < 2; ++L) {
    const int xa = OFF_XF + (2 * L) * 2048 * KB, xb = xa + 2048 * KB, xc = xb + 2048 * KB;
    const int sa_ = OFF_ST + (2 * L) * 64 * KB, sb_ = sa_ + 64 * KB, sc_ = sb_ + 64 * KB;
    const int pqo = OFF_PQ + L * 2048 * KB, ofo = OFF_OF + L * 2048 * KB, kvo = OFF_KV + L * 128 * KB;
    const int acco = OFF_ACC + L * ACC_N * 4;
    u32x4 wp[16];
    {
      IDS();
      const int t1 = panel * 4 + w4;
      constexpr int WIN = 4;
      u32x4 xf[16];
#define XLD(i) xf[i] = ld128(rs, vrot + ((i) & 3) * 1024, xa + (t1 * 32 + kh * 16 + ((i) & ~3)) * 1024)
#define XMAX(g) umax4(xf[4 * (g)], umax4(xf[4 * (g) + 1], umax4(xf[4 * (g) + 2], umax4(xf[4 * (g) + 3], 0u))))
      if (w == 4) glds1(ws, OFF_CS1 + hp * 1024 + voff, lds0 + 128 * KB);
      u32x4 sg;
#define X1LD() do { _Pragma("unroll") for (int i = 0; i < WIN; ++i) XLD(i); } while (0)
#define S1LD() sg = ld128(rs, (r ^ (kh << 4)) * 32 + h * 16, sa_ + t1 * 1024)
      X1LD();
      SBAR();
      VERIFY(XMAX(0), X1LD());
      BAR();
      STAMP(1 + 16 * L);
      f32x16 aq = zero16(), ak = zero16(), av = zero16();
      const LAS char* wl = smem + L_WL + kh * 16384;
      u32x4 wf[2][2];
#define WLD(i) do { wf[(i) & 1][0] = *(const LAS u32x4*)(wl + voff + (32 + (i)) * 1024); wf[(i) & 1][1] = *(const LAS u32x4*)(wl + voff + (64 + (i)) * 1024); } while (0)
      WLD(0);
      RowStat st;
      LAS float* sst = (LAS float*)(smem + 129 * KB + w * 256);
#pragma unroll
      for (int i = 0; i < 16; ++i) {
        SBAR();
        if (i < 15) WLD(i + 1);
        ak = MFMA(xf[i], wf[i & 1][0], ak);
        av = MFMA(xf[i], wf[i & 1][1], av);
        SBAR();
        if (2 * i + WIN < 16) { XLD(2 * i + WIN); XLD(2 * i + WIN + 1); }
        if ((i & 3) == 3 && i < 15 && L == 1) VERIFY_FAST(XMAX((i >> 2) + 1), { XLD(i + 1); XLD(i + 2); XLD(i + 3); XLD(i + 4); });
        if (i == 6) S1LD();
        if (i == 12) {
          VERIFY_FAST(umax4(sg, 0u), S1LD());
          st = row_stats(sg);
          if (h == 0) { sst[r] = st.mu; sst[32 + r] = st.rs; }
        }
      }
      SBAR();
      STAMP(2 + 16 * L);
      put_hi(smem + L_ACT + w * 4096 + voff, ak); put_hi(smem + L_ACT + w * 4096 + 2048 + voff, av);
      if (kh == 0) {
        const u32x4 pz = {POISON, POISON, POISON, POISON};
#pragma unroll
        for (int k = 0; k < 2; ++k) {
          st128(rs, voff + k * 1024, pqo + (t1 * 32 + 2 * hp) * 1024, pz);
          st128(rs, voff + k * 1024, xb + (tt * 32 + 2 * nt) * 1024, pz);
          st128(rs, voff + k * 1024, ofo + (tt * 32 + 2 * nt) * 1024, pz);
          if (L == 0) st128(rs, voff + k * 1024, xc + (tt * 32 + 2 * nt) * 1024, pz);
        }
        if (cb == 0 && w < 2) st128(rs, voff, kvo + tt * 2048 + w * 1024, pz);
        if (w == 2 && lane < 32) st64(rs, lane * 32, sb_ + tt * 1024 + cb * 8, u32x2{POISON, POISON});
        if (L == 0 && w == 3 && lane < 32) st64(rs, lane * 32, sc_ + tt * 1024 + cb * 8, u32x2{POISON, POISON});
      }
      LAS char* exr = smem + L_ACT + (w ^ 4) * 4096;
      const LAS char* csl = smem + 128 * KB;
      const float csk = *(const LAS float*)(csl + 128 + r * 4) + *(const LAS float*)(csl + 640 + r * 4);
      const float csv = *(const LAS float*)(csl + 256 + r * 4) + *(const LAS float*)(csl + 768 + r * 4);
      f32x4 mu[2], rr[2];
#pragma unroll
      for (int g = 0; g < 2; ++g) { mu[g] = *(const LAS f32x4*)(sst + 8 * g + 4 * h); rr[g] = *(const LAS f32x4*)(sst + 32 + 8 * g + 4 * h); }
      BAR();
      STAMP(42);
      add_lo(exr + voff, ak); add_lo(exr + 2048 + voff, av);
#pragma unroll
      for (int g = 0; g < 2; ++g) {
#pragma unroll
        for (int j = 0; j < 4; ++j) {
          ak[4 * g + j] = phi(ak[4 * g + j] - mu[g][j] * csk, rr[g][j], rr[g][j] * LOG2E);
          av[4 * g + j] = rr[g][j] * (av[4 * g + j] - mu[g][j] * csv);
        }
      }
      const u32x4 ones = {0x3f803f80u, 0x3f803f80u, 0x3f803f80u, 0x3f803f80u};
      const u32x4 pk = pack8(ak, 0);
      const f32x16 z = MFMA(pk, pack8(av, 0), zero16());
      const f32x16 zs = MFMA(pk, ones, zero16());
#define WQLD(i) wf[(i) & 1][0] = *(const LAS u32x4*)(wl + vrot + (i) * 1024)
      WQLD(0);
      {
        LAS float* red = (LAS float*)exr + h * 8 + (r & 15) * 16;
        if (r < 16) {
          *(LAS f32x4*)red = f32x4{z[0], z[1], z[2], z[3]}; *(LAS f32x4*)(red + 4) = f32x4{z[4], z[5], z[6], z[7]};
          if (r == 0) { *(LAS f32x4*)(red + 256) = f32x4{zs[0], zs[1], zs[2], zs[3]}; *(LAS f32x4*)(red + 260) = f32x4{zs[4], zs[5], zs[6], zs[7]}; }
          asm volatile("" ::: "memory");
        } else {
          *(LAS f32x4*)(red + 272) = f32x4{z[8], z[9], z[10], z[11]}; *(LAS f32x4*)(red + 276) = f32x4{z[12], z[13], z[14], z[15]};
          if (r == 16) { *(LAS f32x4*)(red + 528) = f32x4{zs[8], zs[9], zs[10], zs[11]}; *(LAS f32x4*)(red + 532) = f32x4{zs[12], zs[13], zs[14], zs[15]}; }
        }
      }
      STAMP(43);
      BAR();
#pragma unroll
      for (int i = 0; i < 2; ++i) {
        const int j = i * 512 + tid;
        if (j < 544) {
          const LAS float* ra = (const LAS float*)(smem + L_ACT) + j;
          float t = 0.f;
#pragma unroll
          for (int k = 0; k < 8; k += 2) t += ra[k * 1024] + ra[(k + 1) * 1024];
          atomicAdd((float*)(ws + acco) + hp * 544 + j, t);
        }
      }
      STAMP(45);
#pragma unroll
      for (int i = 0; i < 16; ++i) {
        SBAR();
        if (i < 15) WQLD(i + 1);
        aq = MFMA(wf[i & 1][0], xf[i], aq);
        SBAR();
        if (i == QPUB) {
          DRAIN();
          STAMP(3 + 16 * L);
          asm volatile("s_barrier" ::: "memory");
          if (w == 0) arrive(ws, CTR_A + L * 128 + (idx & 15), 16, 8, lane);
        }
        if (i >= QPUB + 1 && i < QPUB + 9) {
#pragma unroll
          for (int k = 2 * (i - QPUB - 1); k < 2 * (i - QPUB); ++k) wp[k] = ldw(rs, vrot + (k & 3) * 1024, OFF_W + ((T_WOA + nt) * 32 + kh * 16 + (k & ~3)) * 1024);
        }
      }
      SBAR();
      put_hi(smem + 136 * KB + w * 2048 + vrot, aq);
      f32x4 cs[2];
#pragma unroll
      for (int g = 0; g < 2; ++g) cs[g] = lds2(csl + kh * 64 + g * 32 + h * 16, 512);
      BAR();
      add_lo(smem + 136 * KB + (w ^ 4) * 2048 + vrot, aq);
      const float rsl = st.rs * LOG2E;
#pragma unroll
      for (int j = 0; j < 8; ++j) aq[j] = phi(aq[j] - st.mu * cs[j >> 2][j & 3], st.rs, rsl);
      st128(rs, vrot, pqo + (t1 * 32 + 2 * hp + kh) * 1024, clean(pack8(aq, 0)));
      STAMP(44);
      if (w == 0) poll(ws, CTR_A + L * 128 + xcd * 16, 16, 16, lane);
      BAR();
      STAMP(4 + 16 * L);
    }
    u32x4 wkv[8];
    {
      IDS();
      u32x4 pq[4], kv[4][2];
      const int row = r < 16 ? r : 16;
#pragma unroll
      for (int i = 0; i < 4; ++i) {
        kv[i][0] = ld128(rs, row * 64 + h * 32 + i * 1088, acco + w * 4352);
        kv[i][1] = ld128(rs, row * 64 + h * 32 + i * 1088 + 16, acco + w * 4352);
        pq[i] = ld128(rs, voff + i * 1024, pqo + (tt * 32 + w * 4) * 1024);
      }
      SBAR();
      STAMP(46);
#pragma unroll
      for (int i = 0; i < 4; ++i) {
        VERIFY_FAST(umax4(pq[i], 0u), pq[i] = ld128(rs, voff + i * 1024, pqo + (tt * 32 + w * 4) * 1024));
        const f32x4 k0 = __builtin_bit_cast(f32x4, kv[i][0]), k1 = __builtin_bit_cast(f32x4, kv[i][1]);
        const u32x4 a = {cvtpk(k0[0], k0[1]), cvtpk(k0[2], k0[3]), cvtpk(k1[0], k1[1]), cvtpk(k1[2], k1[3])};
        f32x16 d = MFMA(a, pq[i], zero16());
        const float inv = __builtin_amdgcn_rcpf(d[8] + 1e-6f);
#pragma unroll
        for (int j = 0; j < 8; ++j) d[j] *= inv;
        *(LAS u32x4*)(smem + L_ACT + (w * 4 + i) * 1024 + voff) = pack8(d, 0);
      }
      const int wqo = OFF_W + ((T_WQ + nt) * 32 + kh * 16) * 1024;
#pragma unroll
      for (int i = 0; i < 8; ++i) asm volatile("" : "=v"(wkv[i]));
      if (cb == 0 && kh == 1) {
#pragma unroll
        for (int i = 0; i < 8; ++i) wkv[i] = ldw(rs, (voff ^ (w4 == 1 ? 256 : 0)) + (i & 3) * 1024, OFF_W + (T_WKV * 32 + w4 * 8 + (i & 4)) * 1024);
      }
      if (w == 1) glds2(ws, OFF_CS3 + cb * 2048 + voff, lds0 + 56 * KB);
      STAMP(50);
      BAR();
      STAMP(5 + 16 * L);
      f32x16 acc = gemm_half(smem + L_ACT + kh * 16384 + voff, [&](int i) { return wp[i]; },
                             [&](int i) { if (i < NEARLY) wp[i] = ldw(rs, vrot + (i & 3) * 1024, wqo + (i & ~3) * 1024); });
      STAMP(6 + 16 * L);
      put_hi(smem + L_WL + w * 2048 + voff, acc);
      BAR();
      STAMP(51);
      add_lo(smem + L_WL + (w ^ 4) * 2048 + voff, acc);
      resid_epilogue(acc, xr, rs, xb, smem + 48 * KB + w * 256, out, false, tt, 2 * nt + kh, lane);
      BAR();
      if (w == 0) publish_stats<8>(smem + 48 * KB, rs, sb_ + tt * 1024 + cb * 8, lane);
      STAMP(7 + 16 * L);
    }
    u32x4 q0;
    {
      IDS();
      u32x4 xa4[4];
#define X3LD() do { _Pragma("unroll") for (int k = 0; k < 4; ++k) xa4[k] = ld128(rs, voff + k * 1024, xb + (tt * 32 + 4 * w) * 1024); } while (0)
      DRAIN();
      X3LD();
      VERIFY(umax4(xa4[0], umax4(xa4[1], umax4(xa4[2], umax4(xa4[3], 0u)))), X3LD());
      u32x4 sg = ld128(rs, r * 32 + h * 16, sb_ + tt * 1024);
      STAMP(8 + 16 * L);
#pragma unroll
      for (int k = 0; k < 4; ++k) *(LAS u32x4*)(smem + L_ACT + (4 * w + k) * 1024 + voff) = xa4[k];
      BAR();
      STAMP(9 + 16 * L);
#pragma unroll
      for (int i = NEARLY; i < 16; ++i) wp[i] = ldw(rs, vrot + (i & 3) * 1024, OFF_W + ((T_WQ + nt) * 32 + kh * 16 + (i & ~3)) * 1024);
      SBAR();
      const LAS char* csl = smem + 56 * KB + h * 16;
      RowStat st;
#define S3GET() do { VERIFY_FAST(umax4(sg, 0u), sg = ld128(rs, r * 32 + h * 16, sb_ + tt * 1024)); st = row_stats(sg); } while (0)
      if (cb == 0 && kh == 1) {
        f32x16 kacc = zero16();
        __builtin_amdgcn_s_setprio(2);
        {
          u32x4 a[8];
#pragma unroll
          for (int i = 0; i < 8; ++i) a[i] = *(const LAS u32x4*)(smem + L_ACT + (w4 * 8 + i) * 1024 + voff);
          SBAR();
#pragma unroll
          for (int i = 0; i < 8; ++i) kacc = MFMA(wkv[i], a[i], kacc);
          S3GET();
          LAS char* kp = smem + 60 * KB + w4 * 4096 + voff;
          if (w4 != 0) { *(LAS f32x4*)(kp + (w4 == 1 ? 2048 : 0)) = f32x4{kacc[0], kacc[1], kacc[2], kacc[3]}; *(LAS f32x4*)(kp + (w4 == 1 ? 3072 : 1024)) = f32x4{kacc[4], kacc[5], kacc[6], kacc[7]}; }
          put_hi(kp + (w4 == 1 ? 0 : 2048), kacc);
        }
        BAR();
        if (w4 < 2) {
          const LAS char* kp = smem + 60 * KB + w4 * 2048 + voff;
          add_lo(kp + (w4 ^ 1) * 4096, kacc); add_lo(kp + 8192, kacc); add_lo(kp + 12288, kacc);
#pragma unroll
          for (int g = 0; g < 2; ++g) {
            const f32x4 c = lds2(csl + 512 + w4 * 64 + g * 32, 1024);
#pragma unroll
            for (int j = 0; j < 4; ++j) kacc[4 * g + j] = st.rs * (kacc[4 * g + j] - st.mu * c[j]);
          }
          u32x4 v = pack8(kacc, 0);
          if (w == 5) {
            const int t16 = r & 15, hh = (t16 >> 2) & 1, jj = ((t16 >> 3) << 2) | (t16 & 3);
            LAS uint16_t* vb = (LAS uint16_t*)(smem + 76 * KB + (r >> 4) * 512 + hh * 256) + jj;
#pragma unroll
            for (int j = 0; j < 8; ++j) vb[(8 * (j >> 2) + 4 * h + (j & 3)) * 8] = (uint16_t)((j & 1) ? (v[j >> 1] >> 16) : (v[j >> 1] & 0xffffu));
            v = *(const LAS u32x4*)(smem + 76 * KB + voff);
          }
          st128(rs, voff, kvo + tt * 2048 + w4 * 1024, clean(v));
        }
        __builtin_amdgcn_s_setprio(0);
      }
      f32x16 acc = gemm_half(smem + L_ACT + kh * 16384 + voff, [&](int i) { return wp[i]; },
                             [&](int i) {
        if (i == 6 && cb == 0 && kh == 0) asm volatile("s_barrier" ::: "memory");
        if (i == 10 && !(cb == 0 && kh == 1)) S3GET();
      });
      put_hi(smem + 80 * KB + w * 2048 + voff, acc);
      f32x4 c[2];
#pragma unroll
      for (int g = 0; g < 2; ++g) c[g] = lds2(csl + w4 * 128 + kh * 64 + g * 32, 1024);
      BAR();
      add_lo(smem + 80 * KB + (w ^ 4) * 2048 + voff, acc);
      const float sc = st.rs * (0.25f * LOG2E);
#pragma unroll
      for (int j = 0; j < 8; ++j) acc[j] = sc * (acc[j] - st.mu * c[j >> 2][j & 3]);
      q0 = pack8(acc, 0);
      *(LAS u32x4*)(smem + L_COMB + w * 3072 + voff) = q0;
      if (tid >= 64 && tid < 74) {
        const int s5 = (tid - 64) >> 1, o5 = s5 == 0 ? 0 : (s5 == 1 ? 512 : (s5 == 2 ? 2048 : (s5 == 3 ? 2560 : 4096)));
        const unsigned c = (tid & 1) ? 0u : 0x3f803f80u;
        *(LAS u32x4*)(smem + L_CONST + o5 + (tid & 1) * 16) = u32x4{c, c, c, c};
      }
      PIN(q0);
      STAMP(10 + 16 * L);
      BAR();
      STAMP(11 + 16 * L);
    }
    u32x16 wo0, wo1, wo2, wo3, wn0, wn1, wn2;
    {
      IDS();
      f32x16 oa = zero16(), ob = zero16(), sa, sb;
      u32x4 kf, v0, v1, p0, p1 = {0u, 0u, 0u, 0u};
      unsigned ka = lds0 + kh * 65536 + lane * 16;
      unsigned va = r < 16 ? lds0 + kh * 65536 + 1024 + (h * 16 + r) * 16 : lds0 + L_CONST + ((r == 16 || r == 20) ? 0 : 16);
      const unsigned vinc = r < 16 ? 4096u : 0u;
      const unsigned o0 = kh * 65536 + w4 * 2048;
      unsigned ga = kvo + o0 + voff, wo = OFF_W + ((T_WOB + nt) * 32 + kh * 16) * 1024 + vrot, n, m0s;
      unsigned wn = OFF_W + (hp * 32 + 4 * w) * 1024 + voff;
      const u32x4 q1 = *(const LAS u32x4*)(smem + L_COMB + (w ^ 4) * 3072 + voff);
      asm volatile(ATTN_ASM
                   : "+{v[0:15]}"(oa), "+{v[16:31]}"(ob), "=&{v[32:47]}"(sa), "=&{v[48:63]}"(sb), "=&{v[64:67]}"(kf), "=&{v[68:71]}"(v0),
                     "=&{v[72:75]}"(v1), "=&{v[76:79]}"(p0), "+{v[80:83]}"(p1), "+{v92}"(ka), "+{v93}"(va), "+{v95}"(ga), "+{v96}"(wo),
                     "=&{v[128:143]}"(wo0), "=&{v[144:159]}"(wo1), "=&{v[160:175]}"(wo2), "=&{v[176:191]}"(wo3),
                     "+{v97}"(wn), "=&{v[192:207]}"(wn0), "=&{v[208:223]}"(wn1), "=&{v[224:239]}"(wn2),
                     [n] "=&s"(n), [m0s] "=&s"(m0s)
                   : "{v[84:87]}"(q0), "{v[88:91]}"(q1), "{v94}"(vinc), "{v98}"(lds0 + o0 + voff), [wsb] "s"(ws), [kh] "s"(kh), [m0a] "s"(__builtin_amdgcn_readfirstlane(lds0 + o0))
                   : "memory", "scc", "vcc", "s90", "s91", "s92", "s93", "s94", "s95", "s96", "s97");
      STAMP(12 + 16 * L);
      LAS char* cw = smem + L_COMB + w * 3072 + voff;
      const LAS char* cr = smem + L_COMB + (w ^ 4) * 3072 + voff;
      *(LAS f32x4*)cw = f32x4{ob[0], ob[1], ob[2], ob[3]}; *(LAS f32x4*)(cw + 1024) = f32x4{ob[4], ob[5], ob[6], ob[7]};
      *(LAS float*)(cw + 2048) = ob[8];
      BAR();
      STAMP(54);
      add_lo(cr, oa);
      oa[8] += *(const LAS float*)(cr + 2048);
      float mx = fmaxf(fmaxf(fabsf(oa[0]), fabsf(oa[1])), fabsf(oa[2]));
      mx = fmaxf(fmaxf(mx, fabsf(oa[3])), fabsf(oa[4])); mx = fmaxf(fmaxf(mx, fabsf(oa[5])), fabsf(oa[6]));
      const float sum8 = ((oa[0] + oa[1]) + (oa[2] + oa[3])) + ((oa[4] + oa[5]) + (oa[6] + oa[7]));
      const bool ok = fmaxf(fmaxf(mx, fabsf(oa[7])), oa[8]) < 1.0e36f && oa[8] > 1.0e-27f && sum8 == sum8;
      if (__builtin_expect(!__all(ok), 0)) {
        const LAS char* kb0 = smem + lane * 16;
        const LAS char* vb0 = r < 16 ? smem + 1024 + (h * 16 + r) * 16 : smem + L_CONST + ((r == 16 || r == 20) ? 0 : 16);
        const int vstep = r < 16 ? 2048 : 0;
        float m = -INFINITY;
        oa = zero16();
#pragma unroll 1
        for (int t = 0; t < 64; ++t) {
          f32x16 s = MFMA(*(const LAS u32x4*)(kb0 + t * 2048), q0, zero16());
          const float mn = fmaxf(m, xhalf_max(max16(s)));
          const float al = __builtin_amdgcn_exp2f(m - mn);
          m = mn;
#pragma unroll
          for (int i = 0; i < 16; ++i) { oa[i] *= al; s[i] = __builtin_amdgcn_exp2f(s[i] - mn); }
          oa = MFMA(*(const LAS u32x4*)(vb0 + t * vstep), pack8(s, 0), oa);
          oa = MFMA(*(const LAS u32x4*)(vb0 + t * vstep + 512), pack8(s, 1), oa);
        }
      }
      const float inv = __builtin_amdgcn_rcpf(oa[8]);
#pragma unroll
      for (int i = 0; i < 8; ++i) oa[i] *= inv;
      st128(rs, voff, ofo + (tt * 32 + 2 * nt + kh) * 1024, clean(pack8(oa, 0)));
      STAMP(55);
    }
    {
      IDS();
      u32x4 o4[4];
#define O5LD() do { _Pragma("unroll") for (int k = 0; k < 4; ++k) o4[k] = ld128(rs, voff + k * 1024, ofo + (tt * 32 + 4 * w) * 1024); } while (0)
      DRAIN();
      BAR();
      O5LD();
      SBAR();
      if (L == 0) {
#pragma unroll
        for (int i = 0; i < 4; ++i) {
          LAS char* d = smem + L_WL + (4 * w + i) * 1024 + voff;
          *(LAS u32x4*)d = u32x4{wn0[4 * i], wn0[4 * i + 1], wn0[4 * i + 2], wn0[4 * i + 3]};
          *(LAS u32x4*)(d + 32768) = u32x4{wn1[4 * i], wn1[4 * i + 1], wn1[4 * i + 2], wn1[4 * i + 3]};
          *(LAS u32x4*)(d + 65536) = u32x4{wn2[4 * i], wn2[4 * i + 1], wn2[4 * i + 2], wn2[4 * i + 3]};
        }
      }
      SBAR();
      VERIFY(umax4(o4[0], umax4(o4[1], umax4(o4[2], umax4(o4[3], 0u)))), O5LD());
      STAMP(13 + 16 * L);
#pragma unroll
      for (int k = 0; k < 4; ++k) *(LAS u32x4*)(smem + L_ACT + (4 * w + k) * 1024 + voff) = o4[k];
      BAR();
      STAMP(14 + 16 * L);
      f32x16 acc = gemm_half(smem + L_ACT + kh * 16384 + voff, [&](int i) {
        const u32x16& v = i < 4 ? wo0 : (i < 8 ? wo1 : (i < 12 ? wo2 : wo3));
        return u32x4{v[4 * (i & 3)], v[4 * (i & 3) + 1], v[4 * (i & 3) + 2], v[4 * (i & 3) + 3]};
      }, [](int) {});
      put_hi(smem + 136 * KB + w * 2048 + voff, acc);
      BAR();
      add_lo(smem + 136 * KB + (w ^ 4) * 2048 + voff, acc);
      resid_epilogue(acc, xr, rs, xc, smem + 152 * KB + w * 256, out, L == 1, tt, 2 * nt + kh, lane);
      STAMP(15 + 16 * L);
      if (L == 0) {
        BAR();
        if (w == 0) publish_stats<8>(smem + 152 * KB, rs, sc_ + tt * 1024 + cb * 8, lane);
        DRAIN();
        __builtin_amdgcn_s_sleep(10);
        STAMP(16);
      }
    }
  }
  STAMP(60);
  STAMP_FLUSH();
}

extern "C" void kernel_launch(void* const* d_in, const int* in_sizes, int n_in, void* d_out, int out_size, void* d_ws,
                              size_t ws_size, hipStream_t stream) {
  char* ws = (char*)d_ws;
  (void)hipFuncSetAttribute((const void*)k_mega, hipFuncAttributeMaxDynamicSharedMemorySize, 160 * KB);
  k_prep<<<PREP_BLOCKS, 256, 0, stream>>>((const uint16_t*)d_in[0], (const uint16_t*)d_in[1], (const uint16_t*)d_in[2], (const uint16_t*)d_in[4],
                                          (const uint16_t*)d_in[5], (const uint16_t*)d_in[6], (const uint16_t*)d_in[7], ws);
  k_mega<<<256, 512, 160 * KB, stream>>>(ws, (uint16_t*)d_out);
}
```

```cpp
#include <hip/hip_runtime.h>
#include <hip/hip_bf16.h>
#include <stdint.h>

typedef __attribute__((ext_vector_type(8))) short bf16x8;
typedef __attribute__((ext_vector_type(4))) short s16x4;
typedef __attribute__((ext_vector_type(16))) float f32x16;
typedef __attribute__((ext_vector_type(4))) float f32x4;
typedef __attribute__((ext_vector_type(2))) float f32x2;
typedef __attribute__((ext_vector_type(16))) unsigned u32x16;
typedef __attribute__((ext_vector_type(4))) unsigned u32x4;
typedef __attribute__((ext_vector_type(2))) unsigned u32x2;
typedef __attribute__((ext_vector_type(2))) __bf16 bf16x2_t;
#define LAS __attribute__((address_space(3)))
#define GAS __attribute__((address_space(1)))
typedef GAS unsigned gu32;
#define DI __device__ __forceinline__
#define MFMA(a, b, c) __builtin_amdgcn_mfma_f32_32x32x16_bf16(__builtin_bit_cast(bf16x8, (a)), __builtin_bit_cast(bf16x8, (b)), (c), 0, 0, 0)
#define RLX_AGENT __ATOMIC_RELAXED, __HIP_MEMORY_SCOPE_AGENT

#define STAMP(slot) do {} while (0)
#define STAMP_DECL do {} while (0)
#define STAMP_FLUSH() do {} while (0)

constexpr int DM = 512, NH = 32;
constexpr float LOG2E = 1.4426950408889634f;

constexpr int KB = 1024;
constexpr int OFF_CTR = 0;
constexpr int CTR_A = 0, CTR_TMO = 256, CTR_LINES = 264;
constexpr int OFF_ACC = OFF_CTR + CTR_LINES * 128;
constexpr int ACC_N = NH * 17 * 16;
constexpr int ZERO_BYTES = CTR_LINES * 128 + 2 * ACC_N * 4;
constexpr int OFF_W = 256 * KB;
constexpr int T_WOA = 48, T_WQ = 64, T_WKV = 80, T_WOB = 81;
constexpr int OFF_CS1 = OFF_W + 97 * 32 * KB;
constexpr int OFF_CS3 = OFF_CS1 + 16 * KB;
constexpr int OFF_XF = OFF_CS3 + 8 * KB;
constexpr int OFF_ST = OFF_XF + 4 * 2048 * KB;
constexpr int OFF_PQ = OFF_ST + 4 * 64 * KB;
constexpr int OFF_OF = OFF_PQ + 2 * 2048 * KB;
constexpr int OFF_KV = OFF_OF + 2 * 2048 * KB;
constexpr int WS_END = OFF_KV + 2 * 128 * KB;

constexpr int L_ACT = 0, L_WL = 32 * KB;
constexpr int L_CONST = 128 * KB, L_COMB = 136 * KB;

DI unsigned cvtpk(float lo, float hi) {
  f32x2 v = {lo, hi};
  return __builtin_bit_cast(unsigned, __builtin_convertvector(v, bf16x2_t));
}
DI float bflo(unsigned u) { return __uint_as_float(u << 16); }
DI float bfhi(unsigned u) { return __uint_as_float(u & 0xffff0000u); }
DI float bfround(float x) { return bflo(cvtpk(x, x)); }
DI u32x4 pack8(const f32x16& x, int s) {
  u32x4 p;
  p[0] = cvtpk(x[8 * s + 0], x[8 * s + 1]); p[1] = cvtpk(x[8 * s + 2], x[8 * s + 3]);
  p[2] = cvtpk(x[8 * s + 4], x[8 * s + 5]); p[3] = cvtpk(x[8 * s + 6], x[8 * s + 7]);
  return p;
}
DI float xhalf_sum(float v) {
  auto r = __builtin_amdgcn_permlane32_swap(__float_as_uint(v), __float_as_uint(v), false, false);
  return __uint_as_float(r[0]) + __uint_as_float(r[1]);
}
DI float xhalf_max(float v) {
  auto r = __builtin_amdgcn_permlane32_swap(__float_as_uint(v), __float_as_uint(v), false, false);
  return fmaxf(__uint_as_float(r[0]), __uint_as_float(r[1]));
}
DI f32x16 zero16() { f32x16 z; _Pragma("unroll") for (int i = 0; i < 16; ++i) z[i] = 0.f; return z; }
DI float phi(float y, float rs, float rsl) {
  return fmaxf(__builtin_fmaf(y, rs, 1e-6f), 1e-6f) + __builtin_amdgcn_fmed3f(__builtin_amdgcn_exp2f(y * rsl), 0.f, 1.f);
}

#define GLDS_HEAD "s_nop 4\n\ts_mov_b32 %0, m0\n\ts_mov_b32 m0, %3\n\ts_nop 0\n\t"
#define GLDS_OPS : "=&s"(sv) : "v"(voff), "s"(sbase), "s"(lds_base) : "memory"
DI void glds4(const char* sbase, unsigned voff, unsigned lds_base) {
  unsigned sv;
  lds_base = __builtin_amdgcn_readfirstlane(lds_base);
  asm volatile(GLDS_HEAD "global_load_lds_dwordx4 %1, %2\n\tglobal_load_lds_dwordx4 %1, %2 offset:1024\n\t"
               "global_load_lds_dwordx4 %1, %2 offset:2048\n\tglobal_load_lds_dwordx4 %1, %2 offset:3072\n\ts_mov_b32 m0, %0" GLDS_OPS);
}
DI void glds2(const char* sbase, unsigned voff, unsigned lds_base) {
  unsigned sv;
  lds_base = __builtin_amdgcn_readfirstlane(lds_base);
  asm volatile(GLDS_HEAD "global_load_lds_dwordx4 %1, %2\n\tglobal_load_lds_dwordx4 %1, %2 offset:1024\n\ts_mov_b32 m0, %0" GLDS_OPS);
}
DI void glds1(const char* sbase, unsigned voff, unsigned lds_base) {
  unsigned sv;
  lds_base = __builtin_amdgcn_readfirstlane(lds_base);
  asm volatile(GLDS_HEAD "global_load_lds_dwordx4 %1, %2\n\ts_mov_b32 m0, %0" GLDS_OPS);
}
#define BAR() asm volatile("s_waitcnt lgkmcnt(0)\n\ts_barrier" ::: "memory")
#define DRAIN() asm volatile("s_waitcnt vmcnt(0)" ::: "memory")
#define PIN(x) asm volatile("" : "+v"(x))
#define SBAR() __builtin_amdgcn_sched_barrier(0)

typedef __amdgpu_buffer_rsrc_t rsrc_t;
DI u32x4 ld128(rsrc_t rs, int voff, int soff) { return __builtin_amdgcn_raw_buffer_load_b128(rs, voff, soff, 16); }
DI u32x4 ldw(rsrc_t rs, int voff, int soff) { return __builtin_amdgcn_raw_buffer_load_b128(rs, voff, soff, 0); }
DI void st128(rsrc_t rs, int voff, int soff, u32x4 v) { __builtin_amdgcn_raw_buffer_store_b128(v, rs, voff + soff, 0, 16); }
DI void st64(rsrc_t rs, int voff, int soff, u32x2 v) { __builtin_amdgcn_raw_buffer_store_b64(v, rs, voff, soff, 16); }

DI void poll(char* ws, int line0, int n, unsigned want, int lane) {
  gu32* c = (gu32*)(ws + OFF_CTR) + (line0 + (lane < n ? lane : 0)) * 32;
  gu32* tmo = (gu32*)(ws + OFF_CTR) + CTR_TMO * 32;
#pragma clang loop unroll(disable)
  for (unsigned spins = 0;;) {
    if (__all(__hip_atomic_load(c, RLX_AGENT) >= want)) break;
    __builtin_amdgcn_s_sleep(1);
    if (++spins > (1u << 17)) { if (lane == 0) __hip_atomic_store(tmo, 1u, RLX_AGENT); break; }
    if ((spins & 255u) == 0u && __builtin_amdgcn_readfirstlane(__hip_atomic_load(tmo, RLX_AGENT))) break;
  }
  __builtin_amdgcn_fence(__ATOMIC_ACQUIRE, "wavefront");
}
DI void arrive(char* ws, int line0, int stride, int n, int lane) {
  if (lane < n) __hip_atomic_fetch_add((gu32*)(ws + OFF_CTR) + (line0 + lane * stride) * 32, 1u, RLX_AGENT);
}

constexpr unsigned POISON = 0xffffffffu;
DI u32x4 clean(u32x4 v) { _Pragma("unroll") for (int j = 0; j < 4; ++j) v[j] = min(v[j], POISON - 1u); return v; }
DI unsigned umax4(const u32x4& v, unsigned m) { return max(max(max(v[0], v[1]), m), max(v[2], v[3])); }
#define VERIFY(MAXEXPR, RELOAD) do { _Pragma("clang loop unroll(disable)") for (unsigned sp_ = 0; !__all((MAXEXPR) != POISON) && sp_ < (1u << 16); ++sp_) { \
    __builtin_amdgcn_s_sleep(2); asm volatile("" ::: "memory"); RELOAD; } } while (0)

#define VERIFY_FAST(MAXEXPR, RELOAD) do { if (__builtin_expect(!__all((MAXEXPR) != POISON), 0)) { \
    _Pragma("clang loop unroll(disable)") for (unsigned sp_ = 0; sp_ < (1u << 16); ++sp_) { \
      __builtin_amdgcn_s_sleep(2); asm volatile("" ::: "memory"); RELOAD; __builtin_amdgcn_s_waitcnt(0x0F70); if (__all((MAXEXPR) != POISON)) break; } } } while (0)

struct RowStat { float mu, rs; };
DI RowStat finish_stats(float s, float q) {
  const float mean = s * (1.f / DM);
  const float var = fmaxf(q * (1.f / DM) - mean * mean, 0.f);
  const float sd = bfround(__builtin_amdgcn_sqrtf(bfround(bfround(var) + 1e-5f)));
  RowStat o; o.mu = bfround(mean); o.rs = __builtin_amdgcn_rcpf(sd); return o;
}
DI RowStat row_stats(const u32x4& p) {
  return finish_stats(xhalf_sum(__uint_as_float(p[0]) + __uint_as_float(p[2])), xhalf_sum(__uint_as_float(p[1]) + __uint_as_float(p[3])));
}
DI float dot2(unsigned a, unsigned b, float c) {
  return __builtin_amdgcn_fdot2_f32_bf16(__builtin_bit_cast(bf16x2_t, a), __builtin_bit_cast(bf16x2_t, b), c, false);
}
DI float sum8(const u32x4& v) { float c = 0.f; _Pragma("unroll") for (int j = 0; j < 4; ++j) c = dot2(v[j], 0x3f803f80u, c); return c; }
DI float sq8(const u32x4& v) { float c = 0.f; _Pragma("unroll") for (int j = 0; j < 4; ++j) c = dot2(v[j], v[j], c); return c; }
template <int NW> DI void publish_stats(const LAS char* part, rsrc_t rs, int off, int lane) {
  f32x2 t = {0.f, 0.f};
#pragma unroll
  for (int i = 0; i < NW / 2; ++i) t += *(const LAS f32x2*)(part + ((lane >> 5) * (NW / 2) + i) * 256 + (lane & 31) * 8);
  const float s = xhalf_sum(t[0]), q = xhalf_sum(t[1]);
  if (lane < 32) st64(rs, lane * 32, off, u32x2{min(__float_as_uint(s), POISON - 1u), min(__float_as_uint(q), POISON - 1u)});
}
DI void put_hi(LAS char* p, const f32x16& a) {
  *(LAS f32x4*)p = f32x4{a[8], a[9], a[10], a[11]}; *(LAS f32x4*)(p + 1024) = f32x4{a[12], a[13], a[14], a[15]};
}
DI void add_lo(const LAS char* p, f32x16& a) {
  const f32x4 u = *(const LAS f32x4*)p, v = *(const LAS f32x4*)(p + 1024);
#pragma unroll
  for (int j = 0; j < 4; ++j) { a[j] += u[j]; a[4 + j] += v[j]; }
}
DI float max16(const f32x16& s) {
  float a = fmaxf(fmaxf(s[0], s[1]), s[2]), b = fmaxf(fmaxf(s[3], s[4]), s[5]);
  a = fmaxf(fmaxf(a, s[6]), s[7]); b = fmaxf(fmaxf(b, s[8]), s[9]);
  a = fmaxf(fmaxf(a, s[10]), s[11]); b = fmaxf(fmaxf(b, s[12]), s[13]);
  a = fmaxf(fmaxf(a, s[14]), s[15]);
  return fmaxf(a, b);
}

constexpr int PREP_BLOCKS = 714;
DI u32x4 tr_frag(const LAS char* a, int second) {
  const u32x2 lo = __builtin_bit_cast(u32x2, __builtin_amdgcn_ds_read_tr16_b64_v4i16((LAS s16x4*)a));
  const u32x2 hi = __builtin_bit_cast(u32x2, __builtin_amdgcn_ds_read_tr16_b64_v4i16((LAS s16x4*)(a + second)));
  return u32x4{lo[0], lo[1], hi[0], hi[1]};
}
__global__ __launch_bounds__(256) void k_prep(const uint16_t* __restrict__ x0, const uint16_t* __restrict__ wqkv,
                                              const uint16_t* __restrict__ woa, const uint16_t* __restrict__ wq,
                                              const uint16_t* __restrict__ wk, const uint16_t* __restrict__ wv,
                                              const uint16_t* __restrict__ wob, char* __restrict__ ws) {
  __shared__ __attribute__((aligned(16))) char tile_[9728];
  LAS char* smem = (LAS char*)tile_;
  asm volatile("" :: "s"(x0), "s"(wqkv), "s"(woa), "s"(wq), "s"(wk), "s"(wv), "s"(wob), "s"(ws));
  const rsrc_t rs = __builtin_amdgcn_make_buffer_rsrc(ws, 0, WS_END, 0x00020000);
  const int b = blockIdx.x, tid = threadIdx.x, lane = tid & 63, r = lane & 31, h = lane >> 5;
  const int w = __builtin_amdgcn_readfirstlane(tid >> 6), voff = lane * 16, c8 = tid & 7;
  if (b < 458) {
    const int cu = b < 66 ? ((b >> 1) < 24 ? b >> 1 : ((b >> 1) < 32 ? (b >> 1) + 8 : 48)) : (b - 66) >> 3;
    const uint16_t* src; int ld = 512;
    if (cu < 24) { src = wqkv + cu * 64 + c8 * 8; ld = 1536; }
    else if (cu == 48) { src = ((c8 & 2) ? wv : wk) + (c8 & 1) * 8; ld = 16; }
    else src = (cu < 32 ? woa : (cu < 40 ? wq : wob)) + (cu & 7) * 64 + c8 * 8;
    const int wt = cu < 40 ? 2 * cu : (cu < 48 ? 2 * cu + 1 : T_WKV);
    if (b < 66) {
      const int kb = b & 1;
      u32x4 v[8];
#pragma unroll
      for (int i = 0; i < 8; ++i) v[i] = *(const u32x4*)(src + (size_t)(kb * 256 + i * 32 + (tid >> 3)) * ld);
      f32x4 s0 = {0.f, 0.f, 0.f, 0.f}, s1 = s0;
#pragma unroll
      for (int i = 0; i < 8; ++i) {
        s0 += f32x4{bflo(v[i][0]), bfhi(v[i][0]), bflo(v[i][1]), bfhi(v[i][1])};
        s1 += f32x4{bflo(v[i][2]), bfhi(v[i][2]), bflo(v[i][3]), bfhi(v[i][3])};
      }
      *(LAS f32x4*)(smem + (tid >> 3) * 272 + c8 * 32) = s0; *(LAS f32x4*)(smem + (tid >> 3) * 272 + c8 * 32 + 16) = s1;
      __syncthreads();
      if (w == 0) {
        float t = 0.f;
#pragma unroll
        for (int i = 0; i < 32; ++i) t += *(const LAS float*)(smem + i * 272 + lane * 4);
        float* cs1 = (float*)(ws + OFF_CS1); float* cs3 = (float*)(ws + OFF_CS3);
        if (cu < 24) cs1[((wt & 15) + h) * 256 + kb * 128 + (wt >> 4) * 32 + r] = t;
        else if (cu < 40) cs3[((wt - T_WQ) >> 2) * 512 + kb * 256 + ((wt - T_WQ) & 3) * 32 + lane] = t;
        else if (h == 0) {
#pragma unroll
          for (int i = 0; i < 4; ++i) cs3[i * 512 + kb * 256 + 128 + r] = t;
        }
      }
    } else {
      const int k8 = (b - 66) & 7;
      u32x4 v[2];
#pragma unroll
      for (int i = 0; i < 2; ++i) v[i] = *(const u32x4*)(src + (size_t)(k8 * 64 + i * 32 + (tid >> 3)) * ld);
#pragma unroll
      for (int i = 0; i < 2; ++i) *(LAS u32x4*)(smem + (i * 32 + (tid >> 3)) * 144 + c8 * 16) = v[i];
      __syncthreads();
#pragma unroll
      for (int t = 0; t < 2; ++t) {
        const u32x4 f = tr_frag(smem + (w * 16 + 4 * h + ((lane & 15) >> 2)) * 144 + t * 64 + ((lane >> 4) & 1) * 32 + (lane & 3) * 8, 8 * 144);
        if (t == 0 || cu < 48) st128(rs, voff, OFF_W + ((wt + t) * 32 + k8 * 4 + w) * 1024, f);
      }
    }
  } else {
    const int u = b - 458, tt = u >> 2, kq = u & 3;
    u32x4 v[2];
#pragma unroll
    for (int i = 0; i < 2; ++i) v[i] = *(const u32x4*)(x0 + (size_t)(tt * 32 + i * 16 + (tid >> 4)) * DM + kq * 128 + (tid & 15) * 8);
    if (u * 256 + tid < ZERO_BYTES / 16) st128(rs, (u * 256 + tid) * 16, OFF_CTR, u32x4{0u, 0u, 0u, 0u});
#pragma unroll
    for (int i = 0; i < 2; ++i) *(LAS u32x4*)(smem + (i * 16 + (tid >> 4)) * 272 + (tid & 15) * 16) = v[i];
    __syncthreads();
    float s = 0.f, q = 0.f;
#pragma unroll
    for (int i = 0; i < 2; ++i) {
      const LAS char* xs = smem + r * 272 + (2 * w + i) * 32 + h * 8;
      const u32x2 e0 = *(const LAS u32x2*)xs, e1 = *(const LAS u32x2*)(xs + 16);
      const u32x4 f = clean(u32x4{e0[0], e0[1], e1[0], e1[1]});
      st128(rs, voff, OFF_XF + (tt * 32 + kq * 8 + 2 * w + i) * 1024, f);
      s += sum8(f); q += sq8(f);
    }
    s = xhalf_sum(s); q = xhalf_sum(q);
    if (h == 0) *(LAS f32x2*)(smem + 8704 + w * 256 + r * 8) = f32x2{s, q};
    __syncthreads();
    if (w == 0) publish_stats<4>(smem + 8704, rs, OFF_ST + tt * 1024 + kq * 8, lane);
  }
}

#define ATTN_ASM \
  "s_nop 4\n\t" \
  "s_mov_b32 %[m0s], m0\n\t" \
  "s_mov_b32 s96, %[m0a]\n\t" \
  "s_mov_b32 m0, s96\n\t" \
  "s_add_u32 s96, s96, 0x2000\n\t" \
  "global_load_lds_dwordx4 v95, %[wsb] sc1\n\t" \
  "global_load_lds_dwordx4 v95, %[wsb] offset:1024 sc1\n\t" \
  "v_add_u32 v95, 0x2000, v95\n\t" \
  "s_mov_b32 m0, s96\n\t" \
  "s_add_u32 s96, s96, 0x2000\n\t" \
  "global_load_lds_dwordx4 v95, %[wsb] sc1\n\t" \
  "global_load_lds_dwordx4 v95, %[wsb] offset:1024 sc1\n\t" \
  "v_add_u32 v95, 0x2000, v95\n\t" \
  "s_mov_b32 m0, s96\n\t" \
  "s_add_u32 s96, s96, 0x2000\n\t" \
  "global_load_lds_dwordx4 v95, %[wsb] sc1\n\t" \
  "global_load_lds_dwordx4 v95, %[wsb] offset:1024 sc1\n\t" \
  "v_add_u32 v95, 0x2000, v95\n\t" \
  "s_waitcnt vmcnt(2)\n\t" \
  "s_mov_b32 s97, 0\n\t" \
  "Lca%=:\n\t" \
  "ds_read_b128 v[48:51], v98 offset:0\n\t" \
  "ds_read_b128 v[52:55], v98 offset:1024\n\t" \
  "ds_read_b128 v[56:59], v98 offset:8192\n\t" \
  "ds_read_b128 v[60:63], v98 offset:9216\n\t" \
  "s_waitcnt lgkmcnt(0)\n\t" \
  "v_max3_u32 v48, v48, v49, v50\n\t" \
  "v_max3_u32 v48, v51, v52, v48\n\t" \
  "v_max3_u32 v48, v53, v54, v48\n\t" \
  "v_max3_u32 v48, v55, v56, v48\n\t" \
  "v_max3_u32 v48, v57, v58, v48\n\t" \
  "v_max3_u32 v48, v59, v60, v48\n\t" \
  "v_max3_u32 v48, v61, v62, v48\n\t" \
  "v_max3_u32 v48, v63, v50, v48\n\t" \
  "v_cmp_eq_u32 vcc, -1, v48\n\t" \
  "s_nop 1\n\t" \
  "s_cbranch_vccz Loa%=\n\t" \
  "s_add_u32 s97, s97, 1\n\t" \
  "s_cmp_gt_u32 s97, 0xffff\n\t" \
  "s_cbranch_scc1 Loa%=\n\t" \
  "s_sleep 2\n\t" \
  "s_add_u32 m0, %[m0a], 0\n\t" \
  "v_subrev_u32 v48, 24576, v95\n\t" \
  "global_load_lds_dwordx4 v48, %[wsb] sc1\n\t" \
  "global_load_lds_dwordx4 v48, %[wsb] offset:1024 sc1\n\t" \
  "s_add_u32 m0, %[m0a], 8192\n\t" \
  "v_subrev_u32 v48, 16384, v95\n\t" \
  "global_load_lds_dwordx4 v48, %[wsb] sc1\n\t" \
  "global_load_lds_dwordx4 v48, %[wsb] offset:1024 sc1\n\t" \
  "s_waitcnt vmcnt(0)\n\t" \
  "s_branch Lca%=\n\t" \
  "Loa%=:\n\t" \
  "s_barrier\n\t" \
  "ds_read_b128 v[64:67], v92 offset:0\n\t" \
  "ds_read_b128 v[68:71], v93 offset:0\n\t" \
  "ds_read_b128 v[72:75], v93 offset:0\n\t" \
  "s_mov_b32 %[n], 0\n\t" \
  "s_waitcnt lgkmcnt(0)\n\t" \
  "v_mfma_f32_32x32x16_bf16 v[32:47], v[64:67], v[84:87], 0\n\t" \
  "s_getpc_b64 s[92:93]\n\t" \
  "Lpc%=:\n\t" \
  "s_add_u32 s90, s92, Lb0%=-Lpc%=\n\t" \
  "s_addc_u32 s91, s93, 0\n\t" \
  "s_cmp_eq_u32 %[kh], 0\n\t" \
  "s_cselect_b32 s94, 0, Lb1%=-Lb0%=\n\t" \
  "s_add_u32 s90, s90, s94\n\t" \
  "s_addc_u32 s91, s91, 0\n\t" \
  "s_add_u32 s92, s92, Ltab%=-Lpc%=\n\t" \
  "s_addc_u32 s93, s93, 0\n\t" \
  "s_setprio 1\n\t" \
  "s_nop 5\n\t" \
  "s_setpc_b64 s[90:91]\n\t" \
  "Lb0%=:\n\t" \
  "s_setprio 2\n\t" \
  "v_exp_f32 v32, v32\n\t" \
  "v_exp_f32 v33, v33\n\t" \
  "s_waitcnt lgkmcnt(2)\n\t" \
  "v_exp_f32 v34, v34\n\t" \
  "v_mfma_f32_32x32x16_bf16 v[16:31], v[72:75], v[80:83], v[16:31]\n\t" \
  "ds_read_b128 v[72:75], v93 offset:512\n\t" \
  "v_exp_f32 v35, v35\n\t" \
  "v_cvt_pk_bf16_f32 v76, v32, v33\n\t" \
  "v_exp_f32 v36, v36\n\t" \
  "v_exp_f32 v37, v37\n\t" \
  "v_mfma_f32_32x32x16_bf16 v[48:63], v[64:67], v[88:91], 0\n\t" \
  "ds_read_b128 v[64:67], v92 offset:2048\n\t" \
  "v_cvt_pk_bf16_f32 v77, v34, v35\n\t" \
  "v_exp_f32 v38, v38\n\t" \
  "v_exp_f32 v39, v39\n\t" \
  "v_cvt_pk_bf16_f32 v78, v36, v37\n\t" \
  "v_exp_f32 v40, v40\n\t" \
  "v_exp_f32 v41, v41\n\t" \
  "v_cvt_pk_bf16_f32 v79, v38, v39\n\t" \
  "v_exp_f32 v42, v42\n\t" \
  "v_exp_f32 v43, v43\n\t" \
  "s_waitcnt lgkmcnt(2)\n\t" \
  "v_exp_f32 v44, v44\n\t" \
  "v_mfma_f32_32x32x16_bf16 v[0:15], v[68:71], v[76:79], v[0:15]\n\t" \
  "v_cvt_pk_bf16_f32 v80, v40, v41\n\t" \
  "v_exp_f32 v45, v45\n\t" \
  "v_cvt_pk_bf16_f32 v81, v42, v43\n\t" \
  "v_exp_f32 v46, v46\n\t" \
  "v_exp_f32 v47, v47\n\t" \
  "v_cvt_pk_bf16_f32 v82, v44, v45\n\t" \
  "v_cvt_pk_bf16_f32 v83, v46, v47\n\t" \
  "v_exp_f32 v48, v48\n\t" \
  "v_exp_f32 v49, v49\n\t" \
  "s_waitcnt lgkmcnt(1)\n\t" \
  "v_exp_f32 v50, v50\n\t" \
  "v_mfma_f32_32x32x16_bf16 v[0:15], v[72:75], v[80:83], v[0:15]\n\t" \
  "v_exp_f32 v51, v51\n\t" \
  "v_cvt_pk_bf16_f32 v76, v48, v49\n\t" \
  "v_exp_f32 v52, v52\n\t" \
  "v_exp_f32 v53, v53\n\t" \
  "s_waitcnt lgkmcnt(0)\n\t" \
  "v_exp_f32 v54, v54\n\t" \
  "v_mfma_f32_32x32x16_bf16 v[32:47], v[64:67], v[84:87], 0\n\t" \
  "v_cvt_pk_bf16_f32 v77, v50, v51\n\t" \
  "v_exp_f32 v55, v55\n\t" \
  "v_cvt_pk_bf16_f32 v78, v52, v53\n\t" \
  "v_exp_f32 v56, v56\n\t" \
  "v_exp_f32 v57, v57\n\t" \
  "v_cvt_pk_bf16_f32 v79, v54, v55\n\t" \
  "v_exp_f32 v58, v58\n\t" \
  "v_exp_f32 v59, v59\n\t" \
  "v_mfma_f32_32x32x16_bf16 v[16:31], v[68:71], v[76:79], v[16:31]\n\t" \
  "ds_read_b128 v[68:71], v93 offset:2048\n\t" \
  "v_cvt_pk_bf16_f32 v80, v56, v57\n\t" \
  "v_exp_f32 v60, v60\n\t" \
  "v_exp_f32 v61, v61\n\t" \
  "v_cvt_pk_bf16_f32 v81, v58, v59\n\t" \
  "v_exp_f32 v62, v62\n\t" \
  "v_exp_f32 v63, v63\n\t" \
  "v_cvt_pk_bf16_f32 v82, v60, v61\n\t" \
  "v_cvt_pk_bf16_f32 v83, v62, v63\n\t" \
  "v_exp_f32 v32, v32\n\t" \
  "v_exp_f32 v33, v33\n\t" \
  "s_waitcnt lgkmcnt(2)\n\t" \
  "v_exp_f32 v34, v34\n\t" \
  "v_mfma_f32_32x32x16_bf16 v[16:31], v[72:75], v[80:83], v[16:31]\n\t" \
  "ds_read_b128 v[72:75], v93 offset:2560\n\t" \
  "v_exp_f32 v35, v35\n\t" \
  "v_cvt_pk_bf16_f32 v76, v32, v33\n\t" \
  "v_exp_f32 v36, v36\n\t" \
  "v_exp_f32 v37, v37\n\t" \
  "v_mfma_f32_32x32x16_bf16 v[48:63], v[64:67], v[88:91], 0\n\t" \
  "ds_read_b128 v[64:67], v92 offset:4096\n\t" \
  "v_cvt_pk_bf16_f32 v77, v34, v35\n\t" \
  "v_exp_f32 v38, v38\n\t" \
  "v_exp_f32 v39, v39\n\t" \
  "v_cvt_pk_bf16_f32 v78, v36, v37\n\t" \
  "v_exp_f32 v40, v40\n\t" \
  "v_exp_f32 v41, v41\n\t" \
  "v_cvt_pk_bf16_f32 v79, v38, v39\n\t" \
  "v_exp_f32 v42, v42\n\t" \
  "v_exp_f32 v43, v43\n\t" \
  "s_waitcnt lgkmcnt(2)\n\t" \
  "v_exp_f32 v44, v44\n\t" \
  "v_mfma_f32_32x32x16_bf16 v[0:15], v[68:71], v[76:79], v[0:15]\n\t" \
  "v_cvt_pk_bf16_f32 v80, v40, v41\n\t" \
  "v_exp_f32 v45, v45\n\t" \
  "v_cvt_pk_bf16_f32 v81, v42, v43\n\t" \
  "v_exp_f32 v46, v46\n\t" \
  "v_exp_f32 v47, v47\n\t" \
  "v_cvt_pk_bf16_f32 v82, v44, v45\n\t" \
  "v_cvt_pk_bf16_f32 v83, v46, v47\n\t" \
  "s_setprio 0\n\t" \
  "v_exp_f32 v48, v48\n\t" \
  "v_exp_f32 v49, v49\n\t" \
  "s_waitcnt lgkmcnt(1)\n\t" \
  "v_exp_f32 v50, v50\n\t" \
  "v_mfma_f32_32x32x16_bf16 v[0:15], v[72:75], v[80:83], v[0:15]\n\t" \
  "v_exp_f32 v51, v51\n\t" \
  "v_cvt_pk_bf16_f32 v76, v48, v49\n\t" \
  "v_exp_f32 v52, v52\n\t" \
  "v_exp_f32 v53, v53\n\t" \
  "s_waitcnt lgkmcnt(0)\n\t" \
  "v_exp_f32 v54, v54\n\t" \
  "v_mfma_f32_32x32x16_bf16 v[32:47], v[64:67], v[84:87], 0\n\t" \
  "v_cvt_pk_bf16_f32 v77, v50, v51\n\t" \
  "v_exp_f32 v55, v55\n\t" \
  "v_cvt_pk_bf16_f32 v78, v52, v53\n\t" \
  "v_exp_f32 v56, v56\n\t" \
  "v_exp_f32 v57, v57\n\t" \
  "v_cvt_pk_bf16_f32 v79, v54, v55\n\t" \
  "v_exp_f32 v58, v58\n\t" \
  "v_exp_f32 v59, v59\n\t" \
  "v_mfma_f32_32x32x16_bf16 v[16:31], v[68:71], v[76:79], v[16:31]\n\t" \
  "ds_read_b128 v[68:71], v93 offset:4096\n\t" \
  "v_cvt_pk_bf16_f32 v80, v56, v57\n\t" \
  "v_exp_f32 v60, v60\n\t" \
  "v_exp_f32 v61, v61\n\t" \
  "v_cvt_pk_bf16_f32 v81, v58, v59\n\t" \
  "v_exp_f32 v62, v62\n\t" \
  "v_exp_f32 v63, v63\n\t" \
  "v_cvt_pk_bf16_f32 v82, v60, v61\n\t" \
  "v_cvt_pk_bf16_f32 v83, v62, v63\n\t" \
  "s_add_u32 %[n], %[n], 1\n\t" \
  "v_add_u32 v92, 0x1000, v92\n\t" \
  "s_lshl_b32 s94, %[n], 6\n\t" \
  "v_add_u32 v93, v94, v93\n\t" \
  "s_add_u32 s94, s92, s94\n\t" \
  "s_addc_u32 s95, s93, 0\n\t" \
  "s_setpc_b64 s[94:95]\n\t" \
  "Lb1%=:\n\t" \
  "v_exp_f32 v32, v32\n\t" \
  "v_exp_f32 v33, v33\n\t" \
  "s_waitcnt lgkmcnt(2)\n\t" \
  "v_exp_f32 v34, v34\n\t" \
  "v_mfma_f32_32x32x16_bf16 v[16:31], v[72:75], v[80:83], v[16:31]\n\t" \
  "ds_read_b128 v[72:75], v93 offset:512\n\t" \
  "v_exp_f32 v35, v35\n\t" \
  "v_cvt_pk_bf16_f32 v76, v32, v33\n\t" \
  "v_exp_f32 v36, v36\n\t" \
  "v_exp_f32 v37, v37\n\t" \
  "v_mfma_f32_32x32x16_bf16 v[48:63], v[64:67], v[88:91], 0\n\t" \
  "ds_read_b128 v[64:67], v92 offset:2048\n\t" \
  "v_cvt_pk_bf16_f32 v77, v34, v35\n\t" \
  "v_exp_f32 v38, v38\n\t" \
  "v_exp_f32 v39, v39\n\t" \
  "v_cvt_pk_bf16_f32 v78, v36, v37\n\t" \
  "v_exp_f32 v40, v40\n\t" \
  "v_exp_f32 v41, v41\n\t" \
  "v_cvt_pk_bf16_f32 v79, v38, v39\n\t" \
  "v_exp_f32 v42, v42\n\t" \
  "v_exp_f32 v43, v43\n\t" \
  "s_waitcnt lgkmcnt(2)\n\t" \
  "v_exp_f32 v44, v44\n\t" \
  "v_mfma_f32_32x32x16_bf16 v[0:15], v[68:71], v[76:79], v[0:15]\n\t" \
  "v_cvt_pk_bf16_f32 v80, v40, v41\n\t" \
  "v_exp_f32 v45, v45\n\t" \
  "v_cvt_pk_bf16_f32 v81, v42, v43\n\t" \
  "v_exp_f32 v46, v46\n\t" \
  "v_exp_f32 v47, v47\n\t" \
  "v_cvt_pk_bf16_f32 v82, v44, v45\n\t" \
  "v_cvt_pk_bf16_f32 v83, v46, v47\n\t" \
  "v_exp_f32 v48, v48\n\t" \
  "v_exp_f32 v49, v49\n\t" \
  "s_waitcnt lgkmcnt(1)\n\t" \
  "v_exp_f32 v50, v50\n\t" \
  "v_mfma_f32_32x32x16_bf16 v[0:15], v[72:75], v[80:83], v[0:15]\n\t" \
  "v_exp_f32 v51, v51\n\t" \
  "v_cvt_pk_bf16_f32 v76, v48, v49\n\t" \
  "v_exp_f32 v52, v52\n\t" \
  "v_exp_f32 v53, v53\n\t" \
  "s_waitcnt lgkmcnt(0)\n\t" \
  "v_exp_f32 v54, v54\n\t" \
  "v_mfma_f32_32x32x16_bf16 v[32:47], v[64:67], v[84:87], 0\n\t" \
  "v_cvt_pk_bf16_f32 v77, v50, v51\n\t" \
  "v_exp_f32 v55, v55\n\t" \
  "v_cvt_pk_bf16_f32 v78, v52, v53\n\t" \
  "v_exp_f32 v56, v56\n\t" \
  "v_exp_f32 v57, v57\n\t" \
  "v_cvt_pk_bf16_f32 v79, v54, v55\n\t" \
  "v_exp_f32 v58, v58\n\t" \
  "v_exp_f32 v59, v59\n\t" \
  "v_mfma_f32_32x32x16_bf16 v[16:31], v[68:71], v[76:79], v[16:31]\n\t" \
  "ds_read_b128 v[68:71], v93 offset:2048\n\t" \
  "v_cvt_pk_bf16_f32 v80, v56, v57\n\t" \
  "v_exp_f32 v60, v60\n\t" \
  "v_exp_f32 v61, v61\n\t" \
  "v_cvt_pk_bf16_f32 v81, v58, v59\n\t" \
  "v_exp_f32 v62, v62\n\t" \
  "v_exp_f32 v63, v63\n\t" \
  "v_cvt_pk_bf16_f32 v82, v60, v61\n\t" \
  "v_cvt_pk_bf16_f32 v83, v62, v63\n\t" \
  "v_exp_f32 v32, v32\n\t" \
  "v_exp_f32 v33, v33\n\t" \
  "s_waitcnt lgkmcnt(2)\n\t" \
  "v_exp_f32 v34, v34\n\t" \
  "v_mfma_f32_32x32x16_bf16 v[16:31], v[72:75], v[80:83], v[16:31]\n\t" \
  "ds_read_b128 v[72:75], v93 offset:2560\n\t" \
  "v_exp_f32 v35, v35\n\t" \
  "v_cvt_pk_bf16_f32 v76, v32, v33\n\t" \
  "v_exp_f32 v36, v36\n\t" \
  "v_exp_f32 v37, v37\n\t" \
  "v_mfma_f32_32x32x16_bf16 v[48:63], v[64:67], v[88:91], 0\n\t" \
  "ds_read_b128 v[64:67], v92 offset:4096\n\t" \
  "v_cvt_pk_bf16_f32 v77, v34, v35\n\t" \
  "v_exp_f32 v38, v38\n\t" \
  "v_exp_f32 v39, v39\n\t" \
  "v_cvt_pk_bf16_f32 v78, v36, v37\n\t" \
  "v_exp_f32 v40, v40\n\t" \
  "v_exp_f32 v41, v41\n\t" \
  "v_cvt_pk_bf16_f32 v79, v38, v39\n\t" \
  "v_exp_f32 v42, v42\n\t" \
  "v_exp_f32 v43, v43\n\t" \
  "s_waitcnt lgkmcnt(2)\n\t" \
  "v_exp_f32 v44, v44\n\t" \
  "v_mfma_f32_32x32x16_bf16 v[0:15], v[68:71], v[76:79], v[0:15]\n\t" \
  "v_cvt_pk_bf16_f32 v80, v40, v41\n\t" \
  "v_exp_f32 v45, v45\n\t" \
  "v_cvt_pk_bf16_f32 v81, v42, v43\n\t" \
  "v_exp_f32 v46, v46\n\t" \
  "v_exp_f32 v47, v47\n\t" \
  "v_cvt_pk_bf16_f32 v82, v44, v45\n\t" \
  "v_cvt_pk_bf16_f32 v83, v46, v47\n\t" \
  "v_exp_f32 v48, v48\n\t" \
  "v_exp_f32 v49, v49\n\t" \
  "s_waitcnt lgkmcnt(1)\n\t" \
  "v_exp_f32 v50, v50\n\t" \
  "v_mfma_f32_32x32x16_bf16 v[0:15], v[72:75], v[80:83], v[0:15]\n\t" \
  "v_exp_f32 v51, v51\n\t" \
  "v_cvt_pk_bf16_f32 v76, v48, v49\n\t" \
  "v_exp_f32 v52, v52\n\t" \
  "v_exp_f32 v53, v53\n\t" \
  "s_waitcnt lgkmcnt(0)\n\t" \
  "v_exp_f32 v54, v54\n\t" \
  "v_mfma_f32_32x32x16_bf16 v[32:47], v[64:67], v[84:87], 0\n\t" \
  "v_cvt_pk_bf16_f32 v77, v50, v51\n\t" \
  "v_exp_f32 v55, v55\n\t" \
  "v_cvt_pk_bf16_f32 v78, v52, v53\n\t" \
  "v_exp_f32 v56, v56\n\t" \
  "v_exp_f32 v57, v57\n\t" \
  "v_cvt_pk_bf16_f32 v79, v54, v55\n\t" \
  "v_exp_f32 v58, v58\n\t" \
  "v_exp_f32 v59, v59\n\t" \
  "v_mfma_f32_32x32x16_bf16 v[16:31], v[68:71], v[76:79], v[16:31]\n\t" \
  "ds_read_b128 v[68:71], v93 offset:4096\n\t" \
  "v_cvt_pk_bf16_f32 v80, v56, v57\n\t" \
  "v_exp_f32 v60, v60\n\t" \
  "v_exp_f32 v61, v61\n\t" \
  "v_cvt_pk_bf16_f32 v81, v58, v59\n\t" \
  "v_exp_f32 v62, v62\n\t" \
  "v_exp_f32 v63, v63\n\t" \
  "v_cvt_pk_bf16_f32 v82, v60, v61\n\t" \
  "v_cvt_pk_bf16_f32 v83, v62, v63\n\t" \
  "s_add_u32 %[n], %[n], 1\n\t" \
  "v_add_u32 v92, 0x1000, v92\n\t" \
  "s_lshl_b32 s94, %[n], 6\n\t" \
  "v_add_u32 v93, v94, v93\n\t" \
  "s_add_u32 s94, s92, s94\n\t" \
  "s_addc_u32 s95, s93, 0\n\t" \
  "s_setpc_b64 s[94:95]\n\t" \
  ".p2align 6\n\t" \
  "Ltab%=:\n\t" \
  "s_nop 0\n\t" \
  ".p2align 6\n\t" \
  "s_branch Lh1%=\n\t" \
  ".p2align 6\n\t" \
  "s_branch Lh2%=\n\t" \
  ".p2align 6\n\t" \
  "s_barrier\n\t" \
  "s_mov_b32 m0, s96\n\t" \
  "s_add_u32 s96, s96, 0x2000\n\t" \
  "global_load_lds_dwordx4 v95, %[wsb] sc1\n\t" \
  "global_load_lds_dwordx4 v95, %[wsb] offset:1024 sc1\n\t" \
  "v_add_u32 v95, 0x2000, v95\n\t" \
  "s_setpc_b64 s[90:91]\n\t" \
  ".p2align 6\n\t" \
  "s_branch Lh4%=\n\t" \
  ".p2align 6\n\t" \
  "s_branch Lh5%=\n\t" \
  ".p2align 6\n\t" \
  "s_setpc_b64 s[90:91]\n\t" \
  ".p2align 6\n\t" \
  "s_barrier\n\t" \
  "s_mov_b32 m0, %[m0s]\n\t" \
  "global_load_dwordx4 v[128:131], v96, %[wsb]\n\t" \
  "global_load_dwordx4 v[132:135], v96, %[wsb] offset:1024\n\t" \
  "v_add_u32 v96, 0x800, v96\n\t" \
  "global_load_dwordx4 v[192:195], v97, %[wsb] offset:0\n\t" \
  "global_load_dwordx4 v[196:199], v97, %[wsb] offset:1024\n\t" \
  "s_setpc_b64 s[90:91]\n\t" \
  ".p2align 6\n\t" \
  "global_load_dwordx4 v[136:139], v96, %[wsb]\n\t" \
  "global_load_dwordx4 v[140:143], v96, %[wsb] offset:1024\n\t" \
  "v_add_u32 v96, 0x800, v96\n\t" \
  "global_load_dwordx4 v[200:203], v97, %[wsb] offset:2048\n\t" \
  "global_load_dwordx4 v[204:207], v97, %[wsb] offset:3072\n\t" \
  "v_add_u32 v97, 0x80000, v97\n\t" \
  "s_setpc_b64 s[90:91]\n\t" \
  ".p2align 6\n\t" \
  "global_load_dwordx4 v[144:147], v96, %[wsb]\n\t" \
  "global_load_dwordx4 v[148:151], v96, %[wsb] offset:1024\n\t" \
  "v_add_u32 v96, 0x800, v96\n\t" \
  "global_load_dwordx4 v[208:211], v97, %[wsb] offset:0\n\t" \
  "global_load_dwordx4 v[212:215], v97, %[wsb] offset:1024\n\t" \
  "s_setpc_b64 s[90:91]\n\t" \
  ".p2align 6\n\t" \
  "global_load_dwordx4 v[152:155], v96, %[wsb]\n\t" \
  "global_load_dwordx4 v[156:159], v96, %[wsb] offset:1024\n\t" \
  "v_add_u32 v96, 0x800, v96\n\t" \
  "global_load_dwordx4 v[216:219], v97, %[wsb] offset:2048\n\t" \
  "global_load_dwordx4 v[220:223], v97, %[wsb] offset:3072\n\t" \
  "v_add_u32 v97, 0x80000, v97\n\t" \
  "s_setpc_b64 s[90:91]\n\t" \
  ".p2align 6\n\t" \
  "global_load_dwordx4 v[160:163], v96, %[wsb]\n\t" \
  "global_load_dwordx4 v[164:167], v96, %[wsb] offset:1024\n\t" \
  "v_add_u32 v96, 0x800, v96\n\t" \
  "global_load_dwordx4 v[224:227], v97, %[wsb] offset:0\n\t" \
  "global_load_dwordx4 v[228:231], v97, %[wsb] offset:1024\n\t" \
  "s_setpc_b64 s[90:91]\n\t" \
  ".p2align 6\n\t" \
  "global_load_dwordx4 v[168:171], v96, %[wsb]\n\t" \
  "global_load_dwordx4 v[172:175], v96, %[wsb] offset:1024\n\t" \
  "v_add_u32 v96, 0x800, v96\n\t" \
  "global_load_dwordx4 v[232:235], v97, %[wsb] offset:2048\n\t" \
  "global_load_dwordx4 v[236:239], v97, %[wsb] offset:3072\n\t" \
  "v_add_u32 v97, 0x80000, v97\n\t" \
  "s_setpc_b64 s[90:91]\n\t" \
  ".p2align 6\n\t" \
  "global_load_dwordx4 v[176:179], v96, %[wsb]\n\t" \
  "global_load_dwordx4 v[180:183], v96, %[wsb] offset:1024\n\t" \
  "v_add_u32 v96, 0x800, v96\n\t" \
  "s_setpc_b64 s[90:91]\n\t" \
  ".p2align 6\n\t" \
  "global_load_dwordx4 v[184:187], v96, %[wsb]\n\t" \
  "global_load_dwordx4 v[188:191], v96, %[wsb] offset:1024\n\t" \
  "v_add_u32 v96, 0x800, v96\n\t" \
  "s_setpc_b64 s[90:91]\n\t" \
  ".p2align 6\n\t" \
  "s_setpc_b64 s[90:91]\n\t" \
  ".p2align 6\n\t" \
  "s_branch Lend%=\n\t" \
  "Lh1%=:\n\t" \
  "s_waitcnt vmcnt(0)\n\t" \
  "s_mov_b32 s97, 0\n\t" \
  "Lch1%=:\n\t" \
  "ds_read_b128 v[48:51], v98 offset:16384\n\t" \
  "ds_read_b128 v[52:55], v98 offset:17408\n\t" \
  "s_waitcnt lgkmcnt(0)\n\t" \
  "v_max3_u32 v48, v48, v49, v50\n\t" \
  "v_max3_u32 v48, v51, v52, v48\n\t" \
  "v_max3_u32 v48, v53, v54, v48\n\t" \
  "v_max3_u32 v48, v55, v50, v48\n\t" \
  "v_cmp_eq_u32 vcc, -1, v48\n\t" \
  "s_nop 1\n\t" \
  "s_cbranch_vccz Loh1%=\n\t" \
  "s_add_u32 s97, s97, 1\n\t" \
  "s_cmp_gt_u32 s97, 0xffff\n\t" \
  "s_cbranch_scc1 Loh1%=\n\t" \
  "s_sleep 2\n\t" \
  "s_add_u32 m0, %[m0a], 16384\n\t" \
  "v_subrev_u32 v48, 8192, v95\n\t" \
  "global_load_lds_dwordx4 v48, %[wsb] sc1\n\t" \
  "global_load_lds_dwordx4 v48, %[wsb] offset:1024 sc1\n\t" \
  "s_waitcnt vmcnt(0)\n\t" \
  "s_branch Lch1%=\n\t" \
  "Loh1%=:\n\t" \
  "s_mov_b32 m0, s96\n\t" \
  "s_add_u32 s96, s96, 0x2000\n\t" \
  "global_load_lds_dwordx4 v95, %[wsb] sc1\n\t" \
  "global_load_lds_dwordx4 v95, %[wsb] offset:1024 sc1\n\t" \
  "v_add_u32 v95, 0x2000, v95\n\t" \
  "s_mov_b32 m0, s96\n\t" \
  "s_add_u32 s96, s96, 0x2000\n\t" \
  "global_load_lds_dwordx4 v95, %[wsb] sc1\n\t" \
  "global_load_lds_dwordx4 v95, %[wsb] offset:1024 sc1\n\t" \
  "v_add_u32 v95, 0x2000, v95\n\t" \
  "s_setpc_b64 s[90:91]\n\t" \
  "Lh2%=:\n\t" \
  "s_waitcnt vmcnt(0)\n\t" \
  "s_mov_b32 s97, 0\n\t" \
  "Lch2%=:\n\t" \
  "ds_read_b128 v[48:51], v98 offset:24576\n\t" \
  "ds_read_b128 v[52:55], v98 offset:25600\n\t" \
  "ds_read_b128 v[56:59], v98 offset:32768\n\t" \
  "ds_read_b128 v[60:63], v98 offset:33792\n\t" \
  "s_waitcnt lgkmcnt(0)\n\t" \
  "v_max3_u32 v48, v48, v49, v50\n\t" \
  "v_max3_u32 v48, v51, v52, v48\n\t" \
  "v_max3_u32 v48, v53, v54, v48\n\t" \
  "v_max3_u32 v48, v55, v56, v48\n\t" \
  "v_max3_u32 v48, v57, v58, v48\n\t" \
  "v_max3_u32 v48, v59, v60, v48\n\t" \
  "v_max3_u32 v48, v61, v62, v48\n\t" \
  "v_max3_u32 v48, v63, v50, v48\n\t" \
  "v_cmp_eq_u32 vcc, -1, v48\n\t" \
  "s_nop 1\n\t" \
  "s_cbranch_vccz Loh2%=\n\t" \
  "s_add_u32 s97, s97, 1\n\t" \
  "s_cmp_gt_u32 s97, 0xffff\n\t" \
  "s_cbranch_scc1 Loh2%=\n\t" \
  "s_sleep 2\n\t" \
  "s_add_u32 m0, %[m0a], 24576\n\t" \
  "v_subrev_u32 v48, 16384, v95\n\t" \
  "global_load_lds_dwordx4 v48, %[wsb] sc1\n\t" \
  "global_load_lds_dwordx4 v48, %[wsb] offset:1024 sc1\n\t" \
  "s_add_u32 m0, %[m0a], 32768\n\t" \
  "v_subrev_u32 v48, 8192, v95\n\t" \
  "global_load_lds_dwordx4 v48, %[wsb] sc1\n\t" \
  "global_load_lds_dwordx4 v48, %[wsb] offset:1024 sc1\n\t" \
  "s_waitcnt vmcnt(0)\n\t" \
  "s_branch Lch2%=\n\t" \
  "Loh2%=:\n\t" \
  "s_mov_b32 m0, s96\n\t" \
  "s_add_u32 s96, s96, 0x2000\n\t" \
  "global_load_lds_dwordx4 v95, %[wsb] sc1\n\t" \
  "global_load_lds_dwordx4 v95, %[wsb] offset:1024 sc1\n\t" \
  "v_add_u32 v95, 0x2000, v95\n\t" \
  "s_mov_b32 m0, s96\n\t" \
  "s_add_u32 s96, s96, 0x2000\n\t" \
  "global_load_lds_dwordx4 v95, %[wsb] sc1\n\t" \
  "global_load_lds_dwordx4 v95, %[wsb] offset:1024 sc1\n\t" \
  "v_add_u32 v95, 0x2000, v95\n\t" \
  "s_setpc_b64 s[90:91]\n\t" \
  "Lh4%=:\n\t" \
  "s_waitcnt vmcnt(2)\n\t" \
  "s_mov_b32 s97, 0\n\t" \
  "Lch4%=:\n\t" \
  "ds_read_b128 v[48:51], v98 offset:40960\n\t" \
  "ds_read_b128 v[52:55], v98 offset:41984\n\t" \
  "ds_read_b128 v[56:59], v98 offset:49152\n\t" \
  "ds_read_b128 v[60:63], v98 offset:50176\n\t" \
  "s_waitcnt lgkmcnt(0)\n\t" \
  "v_max3_u32 v48, v48, v49, v50\n\t" \
  "v_max3_u32 v48, v51, v52, v48\n\t" \
  "v_max3_u32 v48, v53, v54, v48\n\t" \
  "v_max3_u32 v48, v55, v56, v48\n\t" \
  "v_max3_u32 v48, v57, v58, v48\n\t" \
  "v_max3_u32 v48, v59, v60, v48\n\t" \
  "v_max3_u32 v48, v61, v62, v48\n\t" \
  "v_max3_u32 v48, v63, v50, v48\n\t" \
  "v_cmp_eq_u32 vcc, -1, v48\n\t" \
  "s_nop 1\n\t" \
  "s_cbranch_vccz Loh4%=\n\t" \
  "s_add_u32 s97, s97, 1\n\t" \
  "s_cmp_gt_u32 s97, 0xffff\n\t" \
  "s_cbranch_scc1 Loh4%=\n\t" \
  "s_sleep 2\n\t" \
  "s_add_u32 m0, %[m0a], 40960\n\t" \
  "v_subrev_u32 v48, 24576, v95\n\t" \
  "global_load_lds_dwordx4 v48, %[wsb] sc1\n\t" \
  "global_load_lds_dwordx4 v48, %[wsb] offset:1024 sc1\n\t" \
  "s_add_u32 m0, %[m0a], 49152\n\t" \
  "v_subrev_u32 v48, 16384, v95\n\t" \
  "global_load_lds_dwordx4 v48, %[wsb] sc1\n\t" \
  "global_load_lds_dwordx4 v48, %[wsb] offset:1024 sc1\n\t" \
  "s_waitcnt vmcnt(0)\n\t" \
  "s_branch Lch4%=\n\t" \
  "Loh4%=:\n\t" \
  "s_setpc_b64 s[90:91]\n\t" \
  "Lh5%=:\n\t" \
  "s_waitcnt vmcnt(0)\n\t" \
  "s_mov_b32 s97, 0\n\t" \
  "Lch5%=:\n\t" \
  "ds_read_b128 v[48:51], v98 offset:57344\n\t" \
  "ds_read_b128 v[52:55], v98 offset:58368\n\t" \
  "s_waitcnt lgkmcnt(0)\n\t" \
  "v_max3_u32 v48, v48, v49, v50\n\t" \
  "v_max3_u32 v48, v51, v52, v48\n\t" \
  "v_max3_u32 v48, v53, v54, v48\n\t" \
  "v_max3_u32 v48, v55, v50, v48\n\t" \
  "v_cmp_eq_u32 vcc, -1, v48\n\t" \
  "s_nop 1\n\t" \
  "s_cbranch_vccz Loh5%=\n\t" \
  "s_add_u32 s97, s97, 1\n\t" \
  "s_cmp_gt_u32 s97, 0xffff\n\t" \
  "s_cbranch_scc1 Loh5%=\n\t" \
  "s_sleep 2\n\t" \
  "s_add_u32 m0, %[m0a], 57344\n\t" \
  "v_subrev_u32 v48, 8192, v95\n\t" \
  "global_load_lds_dwordx4 v48, %[wsb] sc1\n\t" \
  "global_load_lds_dwordx4 v48, %[wsb] offset:1024 sc1\n\t" \
  "s_waitcnt vmcnt(0)\n\t" \
  "s_branch Lch5%=\n\t" \
  "Loh5%=:\n\t" \
  "s_setpc_b64 s[90:91]\n\t" \
  "Lend%=:\n\t" \
  "s_waitcnt lgkmcnt(0)\n\t" \
  "v_mfma_f32_32x32x16_bf16 v[16:31], v[72:75], v[80:83], v[16:31]\n\t" \
  "s_setprio 0\n\t" \
  "s_waitcnt vmcnt(0)\n\t" \
  "s_nop 7\n\t" \
  "s_nop 7\n\t" \
  ""

template <class W, class H> DI f32x16 gemm_half(const LAS char* act, W wf, H hook) {
  f32x16 acc = zero16();
  u32x4 a[16];
#pragma unroll
  for (int i = 0; i < 4; ++i) a[i] = *(const LAS u32x4*)(act + i * 1024);
#pragma unroll
  for (int i = 0; i < 16; ++i) {
    SBAR();
    if (i + 4 < 16) a[i + 4] = *(const LAS u32x4*)(act + (i + 4) * 1024);
    acc = MFMA(wf(i), a[i], acc);
    SBAR();
    hook(i);
  }
  SBAR();
  return acc;
}
DI f32x4 lds2(const LAS char* p, int half) { return *(const LAS f32x4*)p + *(const LAS f32x4*)(p + half); }
DI void resid_epilogue(const f32x16& acc, u32x4& xr, rsrc_t rs, int xoff, LAS char* part, uint16_t* out, bool final_, int tt, int ks, int lane) {
  const int r = lane & 31, h = lane >> 5;
  const u32x4 y = pack8(acc, 0);
#pragma unroll
  for (int j = 0; j < 4; ++j) xr[j] = cvtpk(bflo(xr[j]) + bflo(y[j]), bfhi(xr[j]) + bfhi(y[j]));
  xr = clean(xr);
  if (final_) {
    auto a = __builtin_amdgcn_permlane32_swap(xr[0], xr[2], false, false);
    auto c = __builtin_amdgcn_permlane32_swap(xr[1], xr[3], false, false);
    *(u32x4*)(out + (size_t)(tt * 32 + r) * DM + ks * 16 + 8 * h) = u32x4{a[0], c[0], a[1], c[1]};
  } else {
    st128(rs, lane * 16, xoff + (tt * 32 + ks) * 1024, xr);
    const float s = xhalf_sum(sum8(xr)), q = xhalf_sum(sq8(xr));
    if (h == 0) *(LAS f32x2*)(part + r * 8) = f32x2{s, q};
  }
}

#define W2LO 4
#define QPUB 5
constexpr int NEARLY = 12;
__global__ __launch_bounds__(512, 2) void k_mega(char* ws, uint16_t* out) {
  extern __shared__ __attribute__((aligned(16))) char smem_[];
  LAS char* smem = (LAS char*)smem_;
  const unsigned lds0 = (unsigned)(uintptr_t)smem;
  const rsrc_t rs = __builtin_amdgcn_make_buffer_rsrc(ws, 0, WS_END, 0x00020000);
  const int w_ = __builtin_amdgcn_readfirstlane(threadIdx.x >> 6);
#define IDS() int tid = threadIdx.x, w = w_, b = blockIdx.x; asm volatile("" : "+v"(tid)); asm volatile("" : "+s"(w), "+s"(b)); \
  const int lane = tid & 63, r = lane & 31, h = lane >> 5, w4 = w & 3, kh = w >> 2, xcd = b & 7, idx = b >> 3; \
  const int tt = xcd * 8 + (idx >> 2), cb = idx & 3, nt = cb * 4 + w4;                 \
  const int panel = (xcd & 3) * 4 + (idx & 3), hp = (xcd >> 2) * 8 + (idx >> 2);       \
  const unsigned voff = lane * 16, vrot = voff ^ (kh << 8); \
  (void)r; (void)h; (void)tt; (void)nt; (void)panel; (void)hp; (void)voff; (void)vrot
  u32x4 xr;
  STAMP_DECL;
  {
    IDS();
    STAMP(0);
#pragma unroll
    for (int t = 0; t < 3; ++t) glds4(ws, OFF_W + ((t * 16 + hp) * 32 + 4 * w) * 1024 + voff, lds0 + L_WL + (t * 32 + 4 * w) * 1024);
    xr = ld128(rs, voff, OFF_XF + (tt * 32 + 2 * nt + kh) * 1024);
  }

#pragma unroll 1
  for (int L = 0; L < 2; ++L) {
    const int xa = OFF_XF + (2 * L) * 2048 * KB, xb = xa + 2048 * KB, xc = xb + 2048 * KB;
    const int sa_ = OFF_ST + (2 * L) * 64 * KB, sb_ = sa_ + 64 * KB, sc_ = sb_ + 64 * KB;
    const int pqo = OFF_PQ + L * 2048 * KB, ofo = OFF_OF + L * 2048 * KB, kvo = OFF_KV + L * 128 * KB;
    const int acco = OFF_ACC + L * ACC_N * 4;
    u32x4 wp[16];
    {
      IDS();
      const int t1 = panel * 4 + w4;
      constexpr int WIN = 4;
      u32x4 xf[16];
#define XLD(i) xf[i] = ld128(rs, vrot + ((i) & 3) * 1024, xa + (t1 * 32 + kh * 16 + ((i) & ~3)) * 1024)
#define XMAX(g) umax4(xf[4 * (g)], umax4(xf[4 * (g) + 1], umax4(xf[4 * (g) + 2], umax4(xf[4 * (g) + 3], 0u))))
      if (w == 4) glds1(ws, OFF_CS1 + hp * 1024 + voff, lds0 + 128 * KB);
      u32x4 sg;
#define X1LD() do { _Pragma("unroll") for (int i = 0; i < WIN; ++i) XLD(i); } while (0)
#define S1LD() sg = ld128(rs, (r ^ (kh << 4)) * 32 + h * 16, sa_ + t1 * 1024)
      X1LD();
      SBAR();
      VERIFY(XMAX(0), X1LD());
      BAR();
      STAMP(1 + 16 * L);
      f32x16 aq = zero16(), ak = zero16(), av = zero16();
      const LAS char* wl = smem + L_WL + kh * 16384;
      u32x4 wf[2][2];
#define WLD(i) do { wf[(i) & 1][0] = *(const LAS u32x4*)(wl + voff + (32 + (i)) * 1024); wf[(i) & 1][1] = *(const LAS u32x4*)(wl + voff + (64 + (i)) * 1024); } while (0)
      WLD(0);
      RowStat st;
      LAS float* sst = (LAS float*)(smem + 129 * KB + w * 256);
#pragma unroll
      for (int i = 0; i < 16; ++i) {
        SBAR();
        if (i < 15) WLD(i + 1);
        ak = MFMA(xf[i], wf[i & 1][0], ak);
        av = MFMA(xf[i], wf[i & 1][1], av);
        SBAR();
        if (2 * i + WIN < 16) { XLD(2 * i + WIN); XLD(2 * i + WIN + 1); }
        if ((i & 3) == 3 && i < 15 && L == 1) VERIFY_FAST(XMAX((i >> 2) + 1), { XLD(i + 1); XLD(i + 2); XLD(i + 3); XLD(i + 4); });
        if (i == 6) S1LD();
        if (i == 12) {
          VERIFY_FAST(umax4(sg, 0u), S1LD());
          st = row_stats(sg);
          if (h == 0) { sst[r] = st.mu; sst[32 + r] = st.rs; }
        }
      }
      SBAR();
      STAMP(2 + 16 * L);
      put_hi(smem + L_ACT + w * 4096 + voff, ak); put_hi(smem + L_ACT + w * 4096 + 2048 + voff, av);
      if (kh == 0) {
        const u32x4 pz = {POISON, POISON, POISON, POISON};
#pragma unroll
        for (int k = 0; k < 2; ++k) {
          st128(rs, voff + k * 1024, pqo + (t1 * 32 + 2 * hp) * 1024, pz);
          st128(rs, voff + k * 1024, xb + (tt * 32 + 2 * nt) * 1024, pz);
          st128(rs, voff + k * 1024, ofo + (tt * 32 + 2 * nt) * 1024, pz);
          if (L == 0) st128(rs, voff + k * 1024, xc + (tt * 32 + 2 * nt) * 1024, pz);
        }
        if (cb == 0 && w < 2) st128(rs, voff, kvo + tt * 2048 + w * 1024, pz);
        if (w == 2 && lane < 32) st64(rs, lane * 32, sb_ + tt * 1024 + cb * 8, u32x2{POISON, POISON});
        if (L == 0 && w == 3 && lane < 32) st64(rs, lane * 32, sc_ + tt * 1024 + cb * 8, u32x2{POISON, POISON});
      }
      LAS char* exr = smem + L_ACT + (w ^ 4) * 4096;
      const LAS char* csl = smem + 128 * KB;
      const float csk = *(const LAS float*)(csl + 128 + r * 4) + *(const LAS float*)(csl + 640 + r * 4);
      const float csv = *(const LAS float*)(csl + 256 + r * 4) + *(const LAS float*)(csl + 768 + r * 4);
      f32x4 mu[2], rr[2];
#pragma unroll
      for (int g = 0; g < 2; ++g) { mu[g] = *(const LAS f32x4*)(sst + 8 * g + 4 * h); rr[g] = *(const LAS f32x4*)(sst + 32 + 8 * g + 4 * h); }
      BAR();
      STAMP(42);
      add_lo(exr + voff, ak); add_lo(exr + 2048 + voff, av);
#pragma unroll
      for (int g = 0; g < 2; ++g) {
#pragma unroll
        for (int j = 0; j < 4; ++j) {
          ak[4 * g + j] = phi(ak[4 * g + j] - mu[g][j] * csk, rr[g][j], rr[g][j] * LOG2E);
          av[4 * g + j] = rr[g][j] * (av[4 * g + j] - mu[g][j] * csv);
        }
      }
      const u32x4 ones = {0x3f803f80u, 0x3f803f80u, 0x3f803f80u, 0x3f803f80u};
      const u32x4 pk = pack8(ak, 0);
      const f32x16 z = MFMA(pk, pack8(av, 0), zero16());
      const f32x16 zs = MFMA(pk, ones, zero16());
#define WQLD(i) wf[(i) & 1][0] = *(const LAS u32x4*)(wl + vrot + (i) * 1024)
      WQLD(0);
      {
        LAS float* red = (LAS float*)exr + h * 8 + (r & 15) * 16;
        if (r < 16) {
          *(LAS f32x4*)red = f32x4{z[0], z[1], z[2], z[3]}; *(LAS f32x4*)(red + 4) = f32x4{z[4], z[5], z[6], z[7]};
          if (r == 0) { *(LAS f32x4*)(red + 256) = f32x4{zs[0], zs[1], zs[2], zs[3]}; *(LAS f32x4*)(red + 260) = f32x4{zs[4], zs[5], zs[6], zs[7]}; }
          asm volatile("" ::: "memory");
        } else {
          *(LAS f32x4*)(red + 272) = f32x4{z[8], z[9], z[10], z[11]}; *(LAS f32x4*)(red + 276) = f32x4{z[12], z[13], z[14], z[15]};
          if (r == 16) { *(LAS f32x4*)(red + 528) = f32x4{zs[8], zs[9], zs[10], zs[11]}; *(LAS f32x4*)(red + 532) = f32x4{zs[12], zs[13], zs[14], zs[15]}; }
        }
      }
      STAMP(43);
      BAR();
#pragma unroll
      for (int i = 0; i < 2; ++i) {
        const int j = i * 512 + tid;
        if (j < 544) {
          const LAS float* ra = (const LAS float*)(smem + L_ACT) + j;
          float t = 0.f;
#pragma unroll
          for (int k = 0; k < 8; k += 2) t += ra[k * 1024] + ra[(k + 1) * 1024];
          atomicAdd((float*)(ws + acco) + hp * 544 + j, t);
        }
      }
      STAMP(45);
#pragma unroll
      for (int k = W2LO; k < 16; ++k) asm volatile("" : "=v"(wp[k]));
#pragma unroll
      for (int i = 0; i < 16; ++i) {
        SBAR();
        if (i < 15) WQLD(i + 1);
        aq = MFMA(wf[i & 1][0], xf[i], aq);
        SBAR();
        if (i == QPUB) {
          DRAIN();
          STAMP(3 + 16 * L);
          asm volatile("s_barrier" ::: "memory");
          if (w == 0) arrive(ws, CTR_A + L * 128 + (idx & 15), 16, 8, lane);
        }
        if (i >= QPUB + 1 && i < QPUB + 9 && (kh == 1 || i < QPUB + 1 + W2LO / 2)) {
#pragma unroll
          for (int k = 2 * (i - QPUB - 1); k < 2 * (i - QPUB); ++k) wp[k] = ldw(rs, vrot + (k & 3) * 1024, OFF_W + ((T_WOA + nt) * 32 + kh * 16 + (k & ~3)) * 1024);
        }
      }
      SBAR();
      put_hi(smem + 136 * KB + w * 2048 + vrot, aq);
      f32x4 cs[2];
#pragma unroll
      for (int g = 0; g < 2; ++g) cs[g] = lds2(csl + kh * 64 + g * 32 + h * 16, 512);
      BAR();
      add_lo(smem + 136 * KB + (w ^ 4) * 2048 + vrot, aq);
      const float rsl = st.rs * LOG2E;
#pragma unroll
      for (int j = 0; j < 8; ++j) aq[j] = phi(aq[j] - st.mu * cs[j >> 2][j & 3], st.rs, rsl);
      st128(rs, vrot, pqo + (t1 * 32 + 2 * hp + kh) * 1024, clean(pack8(aq, 0)));
      STAMP(44);
      if (w == 0) poll(ws, CTR_A + L * 128 + xcd * 16, 16, 16, lane);
      BAR();
      STAMP(4 + 16 * L);
    }
    u32x4 wkv[8];
    {
      IDS();
      u32x4 pq[4], kv[4][2];
      const int row = r < 16 ? r : 16;
#pragma unroll
      for (int i = 0; i < 4; ++i) {
        kv[i][0] = ld128(rs, row * 64 + h * 32 + i * 1088, acco + w * 4352);
        kv[i][1] = ld128(rs, row * 64 + h * 32 + i * 1088 + 16, acco + w * 4352);
        pq[i] = ld128(rs, voff + i * 1024, pqo + (tt * 32 + w * 4) * 1024);
      }
      SBAR();
      STAMP(46);
#pragma unroll
      for (int i = 0; i < 4; ++i) {
        VERIFY_FAST(umax4(pq[i], 0u), pq[i] = ld128(rs, voff + i * 1024, pqo + (tt * 32 + w * 4) * 1024));
        const f32x4 k0 = __builtin_bit_cast(f32x4, kv[i][0]), k1 = __builtin_bit_cast(f32x4, kv[i][1]);
        const u32x4 a = {cvtpk(k0[0], k0[1]), cvtpk(k0[2], k0[3]), cvtpk(k1[0], k1[1]), cvtpk(k1[2], k1[3])};
        f32x16 d = MFMA(a, pq[i], zero16());
        const float inv = __builtin_amdgcn_rcpf(d[8] + 1e-6f);
#pragma unroll
        for (int j = 0; j < 8; ++j) d[j] *= inv;
        *(LAS u32x4*)(smem + L_ACT + (w * 4 + i) * 1024 + voff) = pack8(d, 0);
      }
      if (kh == 0) {
#pragma unroll
        for (int k = W2LO; k < 16; ++k) wp[k] = ldw(rs, vrot + (k & 3) * 1024, OFF_W + ((T_WOA + nt) * 32 + (k & ~3)) * 1024);
      }
      const int wqo = OFF_W + ((T_WQ + nt) * 32 + kh * 16) * 1024;
#pragma unroll
      for (int i = 0; i < 8; ++i) asm volatile("" : "=v"(wkv[i]));
      if (cb == 0 && kh == 1) {
#pragma unroll
        for (int i = 0; i < 8; ++i) wkv[i] = ldw(rs, (voff ^ (w4 == 1 ? 256 : 0)) + (i & 3) * 1024, OFF_W + (T_WKV * 32 + w4 * 8 + (i & 4)) * 1024);
      }
      if (w == 1) glds2(ws, OFF_CS3 + cb * 2048 + voff, lds0 + 56 * KB);
      STAMP(50);
      BAR();
      STAMP(5 + 16 * L);
      f32x16 acc = gemm_half(smem + L_ACT + kh * 16384 + voff, [&](int i) { return wp[i]; },
                             [&](int i) { if (i < NEARLY) wp[i] = ldw(rs, vrot + (i & 3) * 1024, wqo + (i & ~3) * 1024); });
      STAMP(6 + 16 * L);
      put_hi(smem + L_WL + w * 2048 + voff, acc);
      BAR();
      STAMP(51);
      add_lo(smem + L_WL + (w ^ 4) * 2048 + voff, acc);
      resid_epilogue(acc, xr, rs, xb, smem + 48 * KB + w * 256, out, false, tt, 2 * nt + kh, lane);
      BAR();
      if (w == 0) publish_stats<8>(smem + 48 * KB, rs, sb_ + tt * 1024 + cb * 8, lane);
      STAMP(7 + 16 * L);
    }
    u32x4 q0;
    {
      IDS();
      u32x4 xa4[4];
#define X3LD() do { _Pragma("unroll") for (int k = 0; k < 4; ++k) xa4[k] = ld128(rs, voff + k * 1024, xb + (tt * 32 + 4 * w) * 1024); } while (0)
      DRAIN();
      X3LD();
      VERIFY(umax4(xa4[0], umax4(xa4[1], umax4(xa4[2], umax4(xa4[3], 0u)))), X3LD());
      u32x4 sg = ld128(rs, r * 32 + h * 16, sb_ + tt * 1024);
      STAMP(8 + 16 * L);
#pragma unroll
      for (int k = 0; k < 4; ++k) *(LAS u32x4*)(smem + L_ACT + (4 * w + k) * 1024 + voff) = xa4[k];
      BAR();
      STAMP(9 + 16 * L);
#pragma unroll
      for (int i = NEARLY; i < 16; ++i) wp[i] = ldw(rs, vrot + (i & 3) * 1024, OFF_W + ((T_WQ + nt) * 32 + kh * 16 + (i & ~3)) * 1024);
      SBAR();
      const LAS char* csl = smem + 56 * KB + h * 16;
      RowStat st;
#define S3GET() do { VERIFY_FAST(umax4(sg, 0u), sg = ld128(rs, r * 32 + h * 16, sb_ + tt * 1024)); st = row_stats(sg); } while (0)
      if (cb == 0 && kh == 1) {
        f32x16 kacc = zero16();
        __builtin_amdgcn_s_setprio(2);
        {
          u32x4 a[8];
#pragma unroll
          for (int i = 0; i < 8; ++i) a[i] = *(const LAS u32x4*)(smem + L_ACT + (w4 * 8 + i) * 1024 + voff);
          SBAR();
#pragma unroll
          for (int i = 0; i < 8; ++i) kacc = MFMA(wkv[i], a[i], kacc);
          S3GET();
          LAS char* kp = smem + 60 * KB + w4 * 4096 + voff;
          if (w4 != 0) { *(LAS f32x4*)(kp + (w4 == 1 ? 2048 : 0)) = f32x4{kacc[0], kacc[1], kacc[2], kacc[3]}; *(LAS f32x4*)(kp + (w4 == 1 ? 3072 : 1024)) = f32x4{kacc[4], kacc[5], kacc[6], kacc[7]}; }
          put_hi(kp + (w4 == 1 ? 0 : 2048), kacc);
        }
        BAR();
        if (w4 < 2) {
          const LAS char* kp = smem + 60 * KB + w4 * 2048 + voff;
          add_lo(kp + (w4 ^ 1) * 4096, kacc); add_lo(kp + 8192, kacc); add_lo(kp + 12288, kacc);
#pragma unroll
          for (int g = 0; g < 2; ++g) {
            const f32x4 c = lds2(csl + 512 + w4 * 64 + g * 32, 1024);
#pragma unroll
            for (int j = 0; j < 4; ++j) kacc[4 * g + j] = st.rs * (kacc[4 * g + j] - st.mu * c[j]);
          }
          u32x4 v = pack8(kacc, 0);
          if (w == 5) {
            const int t16 = r & 15, hh = (t16 >> 2) & 1, jj = ((t16 >> 3) << 2) | (t16 & 3);
            LAS uint16_t* vb = (LAS uint16_t*)(smem + 76 * KB + (r >> 4) * 512 + hh * 256) + jj;
#pragma unroll
            for (int j = 0; j < 8; ++j) vb[(8 * (j >> 2) + 4 * h + (j & 3)) * 8] = (uint16_t)((j & 1) ? (v[j >> 1] >> 16) : (v[j >> 1] & 0xffffu));
            v = *(const LAS u32x4*)(smem + 76 * KB + voff);
          }
          st128(rs, voff, kvo + tt * 2048 + w4 * 1024, clean(v));
        }
        __builtin_amdgcn_s_setprio(0);
      }
      f32x16 acc = gemm_half(smem + L_ACT + kh * 16384 + voff, [&](int i) { return wp[i]; },
                             [&](int i) {
        if (i == 6 && cb == 0 && kh == 0) asm volatile("s_barrier" ::: "memory");
        if (i == 10 && !(cb == 0 && kh == 1)) S3GET();
      });
      put_hi(smem + 80 * KB + w * 2048 + voff, acc);
      f32x4 c[2];
#pragma unroll
      for (int g = 0; g < 2; ++g) c[g] = lds2(csl + w4 * 128 + kh * 64 + g * 32, 1024);
      BAR();
      add_lo(smem + 80 * KB + (w ^ 4) * 2048 + voff, acc);
      const float sc = st.rs * (0.25f * LOG2E);
#pragma unroll
      for (int j = 0; j < 8; ++j) acc[j] = sc * (acc[j] - st.mu * c[j >> 2][j & 3]);
      q0 = pack8(acc, 0);
      *(LAS u32x4*)(smem + L_COMB + w * 3072 + voff) = q0;
      if (tid >= 64 && tid < 74) {
        const int s5 = (tid - 64) >> 1, o5 = s5 == 0 ? 0 : (s5 == 1 ? 512 : (s5 == 2 ? 2048 : (s5 == 3 ? 2560 : 4096)));
        const unsigned c = (tid & 1) ? 0u : 0x3f803f80u;
        *(LAS u32x4*)(smem + L_CONST + o5 + (tid & 1) * 16) = u32x4{c, c, c, c};
      }
      PIN(q0);
      STAMP(10 + 16 * L);
      BAR();
      STAMP(11 + 16 * L);
    }
    u32x16 wo0, wo1, wo2, wo3, wn0, wn1, wn2;
    {
      IDS();
      f32x16 oa = zero16(), ob = zero16(), sa, sb;
      u32x4 kf, v0, v1, p0, p1 = {0u, 0u, 0u, 0u};
      unsigned ka = lds0 + kh * 65536 + lane * 16;
      unsigned va = r < 16 ? lds0 + kh * 65536 + 1024 + (h * 16 + r) * 16 : lds0 + L_CONST + ((r == 16 || r == 20) ? 0 : 16);
      const unsigned vinc = r < 16 ? 4096u : 0u;
      const unsigned o0 = kh * 65536 + w4 * 2048;
      unsigned ga = kvo + o0 + voff, wo = OFF_W + ((T_WOB + nt) * 32 + kh * 16) * 1024 + vrot, n, m0s;
      unsigned wn = OFF_W + (hp * 32 + 4 * w) * 1024 + voff;
      const u32x4 q1 = *(const LAS u32x4*)(smem + L_COMB + (w ^ 4) * 3072 + voff);
      asm volatile(ATTN_ASM
                   : "+{v[0:15]}"(oa), "+{v[16:31]}"(ob), "=&{v[32:47]}"(sa), "=&{v[48:63]}"(sb), "=&{v[64:67]}"(kf), "=&{v[68:71]}"(v0),
                     "=&{v[72:75]}"(v1), "=&{v[76:79]}"(p0), "+{v[80:83]}"(p1), "+{v92}"(ka), "+{v93}"(va), "+{v95}"(ga), "+{v96}"(wo),
                     "=&{v[128:143]}"(wo0), "=&{v[144:159]}"(wo1), "=&{v[160:175]}"(wo2), "=&{v[176:191]}"(wo3),
                     "+{v97}"(wn), "=&{v[192:207]}"(wn0), "=&{v[208:223]}"(wn1), "=&{v[224:239]}"(wn2),
                     [n] "=&s"(n), [m0s] "=&s"(m0s)
                   : "{v[84:87]}"(q0), "{v[88:91]}"(q1), "{v94}"(vinc), "{v98}"(lds0 + o0 + voff), [wsb] "s"(ws), [kh] "s"(kh), [m0a] "s"(__builtin_amdgcn_readfirstlane(lds0 + o0))
                   : "memory", "scc", "vcc", "s90", "s91", "s92", "s93", "s94", "s95", "s96", "s97");
      STAMP(12 + 16 * L);
      LAS char* cw = smem + L_COMB + w * 3072 + voff;
      const LAS char* cr = smem + L_COMB + (w ^ 4) * 3072 + voff;
      *(LAS f32x4*)cw = f32x4{ob[0], ob[1], ob[2], ob[3]}; *(LAS f32x4*)(cw + 1024) = f32x4{ob[4], ob[5], ob[6], ob[7]};
      *(LAS float*)(cw + 2048) = ob[8];
      BAR();
      STAMP(54);
      add_lo(cr, oa);
      oa[8] += *(const LAS float*)(cr + 2048);
      float mx = fmaxf(fmaxf(fabsf(oa[0]), fabsf(oa[1])), fabsf(oa[2]));
      mx = fmaxf(fmaxf(mx, fabsf(oa[3])), fabsf(oa[4])); mx = fmaxf(fmaxf(mx, fabsf(oa[5])), fabsf(oa[6]));
      const float sum8 = ((oa[0] + oa[1]) + (oa[2] + oa[3])) + ((oa[4] + oa[5]) + (oa[6] + oa[7]));
      const bool ok = fmaxf(fmaxf(mx, fabsf(oa[7])), oa[8]) < 1.0e36f && oa[8] > 1.0e-27f && sum8 == sum8;
      if (__builtin_expect(!__all(ok), 0)) {
        const LAS char* kb0 = smem + lane * 16;
        const LAS char* vb0 = r < 16 ? smem + 1024 + (h * 16 + r) * 16 : smem + L_CONST + ((r == 16 || r == 20) ? 0 : 16);
        const int vstep = r < 16 ? 2048 : 0;
        float m = -INFINITY;
        oa = zero16();
#pragma unroll 1
        for (int t = 0; t < 64; ++t) {
          f32x16 s = MFMA(*(const LAS u32x4*)(kb0 + t * 2048), q0, zero16());
          const float mn = fmaxf(m, xhalf_max(max16(s)));
          const float al = __builtin_amdgcn_exp2f(m - mn);
          m = mn;
#pragma unroll
          for (int i = 0; i < 16; ++i) { oa[i] *= al; s[i] = __builtin_amdgcn_exp2f(s[i] - mn); }
          oa = MFMA(*(const LAS u32x4*)(vb0 + t * vstep), pack8(s, 0), oa);
          oa = MFMA(*(const LAS u32x4*)(vb0 + t * vstep + 512), pack8(s, 1), oa);
        }
      }
      const float inv = __builtin_amdgcn_rcpf(oa[8]);
#pragma unroll
      for (int i = 0; i < 8; ++i) oa[i] *= inv;
      st128(rs, voff, ofo + (tt * 32 + 2 * nt + kh) * 1024, clean(pack8(oa, 0)));
      STAMP(55);
    }
    {
      IDS();
      u32x4 o4[4];
#define O5LD() do { _Pragma("unroll") for (int k = 0; k < 4; ++k) o4[k] = ld128(rs, voff + k * 1024, ofo + (tt * 32 + 4 * w) * 1024); } while (0)
      DRAIN();
      BAR();
      O5LD();
      SBAR();
      if (L == 0) {
#pragma unroll
        for (int i = 0; i < 4; ++i) {
          LAS char* d = smem + L_WL + (4 * w + i) * 1024 + voff;
          *(LAS u32x4*)d = u32x4{wn0[4 * i], wn0[4 * i + 1], wn0[4 * i + 2], wn0[4 * i + 3]};
          *(LAS u32x4*)(d + 32768) = u32x4{wn1[4 * i], wn1[4 * i + 1], wn1[4 * i + 2], wn1[4 * i + 3]};
          *(LAS u32x4*)(d + 65536) = u32x4{wn2[4 * i], wn2[4 * i + 1], wn2[4 * i + 2], wn2[4 * i + 3]};
        }
      }
      SBAR();
      VERIFY(umax4(o4[0], umax4(o4[1], umax4(o4[2], umax4(o4[3], 0u)))), O5LD());
      STAMP(13 + 16 * L);
#pragma unroll
      for (int k = 0; k < 4; ++k) *(LAS u32x4*)(smem + L_ACT + (4 * w + k) * 1024 + voff) = o4[k];
      BAR();
      STAMP(14 + 16 * L);
      f32x16 acc = gemm_half(smem + L_ACT + kh * 16384 + voff, [&](int i) {
        const u32x16& v = i < 4 ? wo0 : (i < 8 ? wo1 : (i < 12 ? wo2 : wo3));
        return u32x4{v[4 * (i & 3)], v[4 * (i & 3) + 1], v[4 * (i & 3) + 2], v[4 * (i & 3) + 3]};
      }, [](int) {});
      put_hi(smem + 136 * KB + w * 2048 + voff, acc);
      BAR();
      add_lo(smem + 136 * KB + (w ^ 4) * 2048 + voff, acc);
      resid_epilogue(acc, xr, rs, xc, smem + 152 * KB + w * 256, out, L == 1, tt, 2 * nt + kh, lane);
      STAMP(15 + 16 * L);
      if (L == 0) {
        BAR();
        if (w == 0) publish_stats<8>(smem + 152 * KB, rs, sc_ + tt * 1024 + cb * 8, lane);
        DRAIN();
        __builtin_amdgcn_s_sleep(10);
        STAMP(16);
      }
    }
  }
  STAMP(60);
  STAMP_FLUSH();
}

extern "C" void kernel_launch(void* const* d_in, const int* in_sizes, int n_in, void* d_out, int out_size, void* d_ws,
                              size_t ws_size, hipStream_t stream) {
  char* ws = (char*)d_ws;
  (void)hipFuncSetAttribute((const void*)k_mega, hipFuncAttributeMaxDynamicSharedMemorySize, 160 * KB);
  k_prep<<<PREP_BLOCKS, 256, 0, stream>>>((const uint16_t*)d_in[0], (const uint16_t*)d_in[1], (const uint16_t*)d_in[2], (const uint16_t*)d_in[4],
                                          (const uint16_t*)d_in[5], (const uint16_t*)d_in[6], (const uint16_t*)d_in[7], ws);
  k_mega<<<256, 512, 160 * KB, stream>>>(ws, (uint16_t*)d_out);
}
```

```cpp
#include <hip/hip_runtime.h>
#include <hip/hip_bf16.h>
#include <stdint.h>

typedef __attribute__((ext_vector_type(8))) short bf16x8;
typedef __attribute__((ext_vector_type(4))) short s16x4;
typedef __attribute__((ext_vector_type(16))) float f32x16;
typedef __attribute__((ext_vector_type(4))) float f32x4;
typedef __attribute__((ext_vector_type(2))) float f32x2;
typedef __attribute__((ext_vector_type(16))) unsigned u32x16;
typedef __attribute__((ext_vector_type(4))) unsigned u32x4;
typedef __attribute__((ext_vector_type(2))) unsigned u32x2;
typedef __attribute__((ext_vector_type(2))) __bf16 bf16x2_t;
#define LAS __attribute__((address_space(3)))
#define GAS __attribute__((address_space(1)))
typedef GAS unsigned gu32;
#define DI __device__ __forceinline__
#define MFMA(a, b, c) __builtin_amdgcn_mfma_f32_32x32x16_bf16(__builtin_bit_cast(bf16x8, (a)), __builtin_bit_cast(bf16x8, (b)), (c), 0, 0, 0)
#define RLX_AGENT __ATOMIC_RELAXED, __HIP_MEMORY_SCOPE_AGENT


constexpr int DM = 512, NH = 32;
constexpr float LOG2E = 1.4426950408889634f;

constexpr int KB = 1024;
constexpr int OFF_CTR = 0;
constexpr int CTR_A = 0, CTR_TMO = 256, CTR_LINES = 264;
constexpr int ZERO_BYTES = CTR_LINES * 128;
constexpr int OFF_KVF = 64 * KB;
constexpr int KVF_B = NH * 17 * 32;
constexpr int OFF_W = 256 * KB;
constexpr int T_WOA = 48, T_WQ = 64, T_WKV = 80, T_WOB = 81;
constexpr int OFF_CS1 = OFF_W + 97 * 32 * KB;
constexpr int OFF_CS3 = OFF_CS1 + 16 * KB;
constexpr int OFF_XF = OFF_CS3 + 8 * KB;
constexpr int OFF_ST = OFF_XF + 4 * 2048 * KB;
constexpr int OFF_PQ = OFF_ST + 4 * 64 * KB;
constexpr int OFF_OF = OFF_PQ + 2 * 2048 * KB;
constexpr int OFF_KV = OFF_OF + 2 * 2048 * KB;
constexpr int OFF_KVP = OFF_KV + 2 * 128 * KB;
constexpr int KVP_B = 256 * 2176;
constexpr int WS_END = OFF_KVP + 2 * KVP_B;

constexpr int L_ACT = 0, L_WL = 32 * KB;
constexpr int L_CONST = 128 * KB, L_COMB = 136 * KB;

DI unsigned cvtpk(float lo, float hi) {
  f32x2 v = {lo, hi};
  return __builtin_bit_cast(unsigned, __builtin_convertvector(v, bf16x2_t));
}
DI float bflo(unsigned u) { return __uint_as_float(u << 16); }
DI float bfhi(unsigned u) { return __uint_as_float(u & 0xffff0000u); }
DI float bfround(float x) { return bflo(cvtpk(x, x)); }
DI u32x4 pack8(const f32x16& x, int s) {
  u32x4 p;
  p[0] = cvtpk(x[8 * s + 0], x[8 * s + 1]); p[1] = cvtpk(x[8 * s + 2], x[8 * s + 3]);
  p[2] = cvtpk(x[8 * s + 4], x[8 * s + 5]); p[3] = cvtpk(x[8 * s + 6], x[8 * s + 7]);
  return p;
}
DI float xhalf_sum(float v) {
  auto r = __builtin_amdgcn_permlane32_swap(__float_as_uint(v), __float_as_uint(v), false, false);
  return __uint_as_float(r[0]) + __uint_as_float(r[1]);
}
DI float xhalf_max(float v) {
  auto r = __builtin_amdgcn_permlane32_swap(__float_as_uint(v), __float_as_uint(v), false, false);
  return fmaxf(__uint_as_float(r[0]), __uint_as_float(r[1]));
}
DI f32x16 zero16() { f32x16 z; _Pragma("unroll") for (int i = 0; i < 16; ++i) z[i] = 0.f; return z; }
DI float phi(float y, float rs, float rsl) {
  return fmaxf(__builtin_fmaf(y, rs, 1e-6f), 1e-6f) + __builtin_amdgcn_fmed3f(__builtin_amdgcn_exp2f(y * rsl), 0.f, 1.f);
}

#define GLDS_HEAD "s_nop 4\n\ts_mov_b32 %0, m0\n\ts_mov_b32 m0, %3\n\ts_nop 0\n\t"
#define GLDS_OPS : "=&s"(sv) : "v"(voff), "s"(sbase), "s"(lds_base) : "memory"
DI void glds4(const char* sbase, unsigned voff, unsigned lds_base) {
  unsigned sv;
  lds_base = __builtin_amdgcn_readfirstlane(lds_base);
  asm volatile(GLDS_HEAD "global_load_lds_dwordx4 %1, %2\n\tglobal_load_lds_dwordx4 %1, %2 offset:1024\n\t"
               "global_load_lds_dwordx4 %1, %2 offset:2048\n\tglobal_load_lds_dwordx4 %1, %2 offset:3072\n\ts_mov_b32 m0, %0" GLDS_OPS);
}
DI void glds2(const char* sbase, unsigned voff, unsigned lds_base) {
  unsigned sv;
  lds_base = __builtin_amdgcn_readfirstlane(lds_base);
  asm volatile(GLDS_HEAD "global_load_lds_dwordx4 %1, %2\n\tglobal_load_lds_dwordx4 %1, %2 offset:1024\n\ts_mov_b32 m0, %0" GLDS_OPS);
}
DI void glds1(const char* sbase, unsigned voff, unsigned lds_base) {
  unsigned sv;
  lds_base = __builtin_amdgcn_readfirstlane(lds_base);
  asm volatile(GLDS_HEAD "global_load_lds_dwordx4 %1, %2\n\ts_mov_b32 m0, %0" GLDS_OPS);
}
#define BAR() asm volatile("s_waitcnt lgkmcnt(0)\n\ts_barrier" ::: "memory")
#define DRAIN() asm volatile("s_waitcnt vmcnt(0)" ::: "memory")
#define PIN(x) asm volatile("" : "+v"(x))
#define SBAR() __builtin_amdgcn_sched_barrier(0)

typedef __amdgpu_buffer_rsrc_t rsrc_t;
DI u32x4 ld128(rsrc_t rs, int voff, int soff) { return __builtin_amdgcn_raw_buffer_load_b128(rs, voff, soff, 16); }
DI u32x4 ldw(rsrc_t rs, int voff, int soff) { return __builtin_amdgcn_raw_buffer_load_b128(rs, voff, soff, 0); }
DI void st128(rsrc_t rs, int voff, int soff, u32x4 v) { __builtin_amdgcn_raw_buffer_store_b128(v, rs, voff + soff, 0, 16); }
DI void st64(rsrc_t rs, int voff, int soff, u32x2 v) { __builtin_amdgcn_raw_buffer_store_b64(v, rs, voff, soff, 16); }

DI void poll(char* ws, int line0, int n, unsigned want, int lane) {
  gu32* c = (gu32*)(ws + OFF_CTR) + (line0 + (lane < n ? lane : 0)) * 32;
  gu32* tmo = (gu32*)(ws + OFF_CTR) + CTR_TMO * 32;
#pragma clang loop unroll(disable)
  for (unsigned spins = 0;;) {
    if (__all(__hip_atomic_load(c, RLX_AGENT) >= want)) break;
    __builtin_amdgcn_s_sleep(1);
    if (++spins > (1u << 17)) { if (lane == 0) __hip_atomic_store(tmo, 1u, RLX_AGENT); break; }
    if ((spins & 255u) == 0u && __builtin_amdgcn_readfirstlane(__hip_atomic_load(tmo, RLX_AGENT))) break;
  }
  __builtin_amdgcn_fence(__ATOMIC_ACQUIRE, "wavefront");
}
DI void arrive(char* ws, int line0, int stride, int n, int lane) {
  if (lane < n) __hip_atomic_fetch_add((gu32*)(ws + OFF_CTR) + (line0 + lane * stride) * 32, 1u, RLX_AGENT);
}

constexpr unsigned POISON = 0xffffffffu;
DI u32x4 clean(u32x4 v) { _Pragma("unroll") for (int j = 0; j < 4; ++j) v[j] = min(v[j], POISON - 1u); return v; }
DI unsigned umax4(const u32x4& v, unsigned m) { return max(max(max(v[0], v[1]), m), max(v[2], v[3])); }
#define VERIFY(MAXEXPR, RELOAD) do { _Pragma("clang loop unroll(disable)") for (unsigned sp_ = 0; !__all((MAXEXPR) != POISON) && sp_ < (1u << 16); ++sp_) { \
    __builtin_amdgcn_s_sleep(2); asm volatile("" ::: "memory"); RELOAD; } } while (0)

#define VERIFY_FAST(MAXEXPR, RELOAD) do { if (__builtin_expect(!__all((MAXEXPR) != POISON), 0)) { \
    _Pragma("clang loop unroll(disable)") for (unsigned sp_ = 0; sp_ < (1u << 16); ++sp_) { \
      __builtin_amdgcn_s_sleep(2); asm volatile("" ::: "memory"); RELOAD; __builtin_amdgcn_s_waitcnt(0x0F70); if (__all((MAXEXPR) != POISON)) break; } } } while (0)

struct RowStat { float mu, rs; };
DI RowStat finish_stats(float s, float q) {
  const float mean = s * (1.f / DM);
  const float var = fmaxf(q * (1.f / DM) - mean * mean, 0.f);
  const float sd = bfround(__builtin_amdgcn_sqrtf(bfround(bfround(var) + 1e-5f)));
  RowStat o; o.mu = bfround(mean); o.rs = __builtin_amdgcn_rcpf(sd); return o;
}
DI RowStat row_stats(const u32x4& p) {
  return finish_stats(xhalf_sum(__uint_as_float(p[0]) + __uint_as_float(p[2])), xhalf_sum(__uint_as_float(p[1]) + __uint_as_float(p[3])));
}
DI float dot2(unsigned a, unsigned b, float c) {
  return __builtin_amdgcn_fdot2_f32_bf16(__builtin_bit_cast(bf16x2_t, a), __builtin_bit_cast(bf16x2_t, b), c, false);
}
DI float sum8(const u32x4& v) { float c = 0.f; _Pragma("unroll") for (int j = 0; j < 4; ++j) c = dot2(v[j], 0x3f803f80u, c); return c; }
DI float sq8(const u32x4& v) { float c = 0.f; _Pragma("unroll") for (int j = 0; j < 4; ++j) c = dot2(v[j], v[j], c); return c; }
template <int NW> DI void publish_stats(const LAS char* part, rsrc_t rs, int off, int lane) {
  f32x2 t = {0.f, 0.f};
#pragma unroll
  for (int i = 0; i < NW / 2; ++i) t += *(const LAS f32x2*)(part + ((lane >> 5) * (NW / 2) + i) * 256 + (lane & 31) * 8);
  const float s = xhalf_sum(t[0]), q = xhalf_sum(t[1]);
  if (lane < 32) st64(rs, lane * 32, off, u32x2{min(__float_as_uint(s), POISON - 1u), min(__float_as_uint(q), POISON - 1u)});
}
DI void put_hi(LAS char* p, const f32x16& a) {
  *(LAS f32x4*)p = f32x4{a[8], a[9], a[10], a[11]}; *(LAS f32x4*)(p + 1024) = f32x4{a[12], a[13], a[14], a[15]};
}
DI void add_lo(const LAS char* p, f32x16& a) {
  const f32x4 u = *(const LAS f32x4*)p, v = *(const LAS f32x4*)(p + 1024);
#pragma unroll
  for (int j = 0; j < 4; ++j) { a[j] += u[j]; a[4 + j] += v[j]; }
}
DI float max16(const f32x16& s) {
  float a = fmaxf(fmaxf(s[0], s[1]), s[2]), b = fmaxf(fmaxf(s[3], s[4]), s[5]);
  a = fmaxf(fmaxf(a, s[6]), s[7]); b = fmaxf(fmaxf(b, s[8]), s[9]);
  a = fmaxf(fmaxf(a, s[10]), s[11]); b = fmaxf(fmaxf(b, s[12]), s[13]);
  a = fmaxf(fmaxf(a, s[14]), s[15]);
  return fmaxf(a, b);
}

constexpr int PREP_BLOCKS = 714;
DI u32x4 tr_frag(const LAS char* a, int second) {
  const u32x2 lo = __builtin_bit_cast(u32x2, __builtin_amdgcn_ds_read_tr16_b64_v4i16((LAS s16x4*)a));
  const u32x2 hi = __builtin_bit_cast(u32x2, __builtin_amdgcn_ds_read_tr16_b64_v4i16((LAS s16x4*)(a + second)));
  return u32x4{lo[0], lo[1], hi[0], hi[1]};
}
__global__ __launch_bounds__(256) void k_prep(const uint16_t* __restrict__ x0, const uint16_t* __restrict__ wqkv,
                                              const uint16_t* __restrict__ woa, const uint16_t* __restrict__ wq,
                                              const uint16_t* __restrict__ wk, const uint16_t* __restrict__ wv,
                                              const uint16_t* __restrict__ wob, char* __restrict__ ws) {
  __shared__ __attribute__((aligned(16))) char tile_[9728];
  LAS char* smem = (LAS char*)tile_;
  asm volatile("" :: "s"(x0), "s"(wqkv), "s"(woa), "s"(wq), "s"(wk), "s"(wv), "s"(wob), "s"(ws));
  const rsrc_t rs = __builtin_amdgcn_make_buffer_rsrc(ws, 0, WS_END, 0x00020000);
  const int b = blockIdx.x, tid = threadIdx.x, lane = tid & 63, r = lane & 31, h = lane >> 5;
  const int w = __builtin_amdgcn_readfirstlane(tid >> 6), voff = lane * 16, c8 = tid & 7;
  if (b < 458) {
    const int cu = b < 66 ? ((b >> 1) < 24 ? b >> 1 : ((b >> 1) < 32 ? (b >> 1) + 8 : 48)) : (b - 66) >> 3;
    const uint16_t* src; int ld = 512;
    if (cu < 24) { src = wqkv + cu * 64 + c8 * 8; ld = 1536; }
    else if (cu == 48) { src = ((c8 & 2) ? wv : wk) + (c8 & 1) * 8; ld = 16; }
    else src = (cu < 32 ? woa : (cu < 40 ? wq : wob)) + (cu & 7) * 64 + c8 * 8;
    const int wt = cu < 40 ? 2 * cu : (cu < 48 ? 2 * cu + 1 : T_WKV);
    if (b < 66) {
      const int kb = b & 1;
      u32x4 v[8];
#pragma unroll
      for (int i = 0; i < 8; ++i) v[i] = *(const u32x4*)(src + (size_t)(kb * 256 + i * 32 + (tid >> 3)) * ld);
      f32x4 s0 = {0.f, 0.f, 0.f, 0.f}, s1 = s0;
#pragma unroll
      for (int i = 0; i < 8; ++i) {
        s0 += f32x4{bflo(v[i][0]), bfhi(v[i][0]), bflo(v[i][1]), bfhi(v[i][1])};
        s1 += f32x4{bflo(v[i][2]), bfhi(v[i][2]), bflo(v[i][3]), bfhi(v[i][3])};
      }
      *(LAS f32x4*)(smem + (tid >> 3) * 272 + c8 * 32) = s0; *(LAS f32x4*)(smem + (tid >> 3) * 272 + c8 * 32 + 16) = s1;
      __syncthreads();
      if (w == 0) {
        float t = 0.f;
#pragma unroll
        for (int i = 0; i < 32; ++i) t += *(const LAS float*)(smem + i * 272 + lane * 4);
        float* cs1 = (float*)(ws + OFF_CS1); float* cs3 = (float*)(ws + OFF_CS3);
        if (cu < 24) cs1[((wt & 15) + h) * 256 + kb * 128 + (wt >> 4) * 32 + r] = t;
        else if (cu < 40) cs3[((wt - T_WQ) >> 2) * 512 + kb * 256 + ((wt - T_WQ) & 3) * 32 + lane] = t;
        else if (h == 0) {
#pragma unroll
          for (int i = 0; i < 4; ++i) cs3[i * 512 + kb * 256 + 128 + r] = t;
        }
      }
    } else {
      const int k8 = (b - 66) & 7;
      u32x4 v[2];
#pragma unroll
      for (int i = 0; i < 2; ++i) v[i] = *(const u32x4*)(src + (size_t)(k8 * 64 + i * 32 + (tid >> 3)) * ld);
#pragma unroll
      for (int i = 0; i < 2; ++i) *(LAS u32x4*)(smem + (i * 32 + (tid >> 3)) * 144 + c8 * 16) = v[i];
      __syncthreads();
#pragma unroll
      for (int t = 0; t < 2; ++t) {
        const u32x4 f = tr_frag(smem + (w * 16 + 4 * h + ((lane & 15) >> 2)) * 144 + t * 64 + ((lane >> 4) & 1) * 32 + (lane & 3) * 8, 8 * 144);
        if (t == 0 || cu < 48) st128(rs, voff, OFF_W + ((wt + t) * 32 + k8 * 4 + w) * 1024, f);
      }
    }
  } else {
    const int u = b - 458, tt = u >> 2, kq = u & 3;
    u32x4 v[2];
#pragma unroll
    for (int i = 0; i < 2; ++i) v[i] = *(const u32x4*)(x0 + (size_t)(tt * 32 + i * 16 + (tid >> 4)) * DM + kq * 128 + (tid & 15) * 8);
    {
      const int z = u * 256 + tid;
      const u32x4 pz = {POISON, POISON, POISON, POISON};
      st128(rs, z * 16, OFF_KVP, pz);
      if (z < 2 * KVP_B / 16 - 65536) st128(rs, (65536 + z) * 16, OFF_KVP, pz);
      else if (z >= 8192 && z < 8192 + 2 * KVF_B / 16) st128(rs, (z - 8192) * 16, OFF_KVF, pz);
      else if (z >= 16384 && z < 16384 + ZERO_BYTES / 16) st128(rs, (z - 16384) * 16, OFF_CTR, u32x4{0u, 0u, 0u, 0u});
    }
#pragma unroll
    for (int i = 0; i < 2; ++i) *(LAS u32x4*)(smem + (i * 16 + (tid >> 4)) * 272 + (tid & 15) * 16) = v[i];
    __syncthreads();
    float s = 0.f, q = 0.f;
#pragma unroll
    for (int i = 0; i < 2; ++i) {
      const LAS char* xs = smem + r * 272 + (2 * w + i) * 32 + h * 8;
      const u32x2 e0 = *(const LAS u32x2*)xs, e1 = *(const LAS u32x2*)(xs + 16);
      const u32x4 f = clean(u32x4{e0[0], e0[1], e1[0], e1[1]});
      st128(rs, voff, OFF_XF + (tt * 32 + kq * 8 + 2 * w + i) * 1024, f);
      s += sum8(f); q += sq8(f);
    }
    s = xhalf_sum(s); q = xhalf_sum(q);
    if (h == 0) *(LAS f32x2*)(smem + 8704 + w * 256 + r * 8) = f32x2{s, q};
    __syncthreads();
    if (w == 0) publish_stats<4>(smem + 8704, rs, OFF_ST + tt * 1024 + kq * 8, lane);
  }
}

#define ATTN_ASM \
  "s_nop 4\n\t" \
  "s_mov_b32 %[m0s], m0\n\t" \
  "s_mov_b32 s96, %[m0a]\n\t" \
  "s_mov_b32 m0, s96\n\t" \
  "s_add_u32 s96, s96, 0x2000\n\t" \
  "global_load_lds_dwordx4 v95, %[wsb] sc1\n\t" \
  "global_load_lds_dwordx4 v95, %[wsb] offset:1024 sc1\n\t" \
  "v_add_u32 v95, 0x2000, v95\n\t" \
  "s_mov_b32 m0, s96\n\t" \
  "s_add_u32 s96, s96, 0x2000\n\t" \
  "global_load_lds_dwordx4 v95, %[wsb] sc1\n\t" \
  "global_load_lds_dwordx4 v95, %[wsb] offset:1024 sc1\n\t" \
  "v_add_u32 v95, 0x2000, v95\n\t" \
  "s_mov_b32 m0, s96\n\t" \
  "s_add_u32 s96, s96, 0x2000\n\t" \
  "global_load_lds_dwordx4 v95, %[wsb] sc1\n\t" \
  "global_load_lds_dwordx4 v95, %[wsb] offset:1024 sc1\n\t" \
  "v_add_u32 v95, 0x2000, v95\n\t" \
  "s_waitcnt vmcnt(2)\n\t" \
  "s_mov_b32 s97, 0\n\t" \
  "Lca%=:\n\t" \
  "ds_read_b128 v[48:51], v98 offset:0\n\t" \
  "ds_read_b128 v[52:55], v98 offset:1024\n\t" \
  "ds_read_b128 v[56:59], v98 offset:8192\n\t" \
  "ds_read_b128 v[60:63], v98 offset:9216\n\t" \
  "s_waitcnt lgkmcnt(0)\n\t" \
  "v_max3_u32 v48, v48, v49, v50\n\t" \
  "v_max3_u32 v48, v51, v52, v48\n\t" \
  "v_max3_u32 v48, v53, v54, v48\n\t" \
  "v_max3_u32 v48, v55, v56, v48\n\t" \
  "v_max3_u32 v48, v57, v58, v48\n\t" \
  "v_max3_u32 v48, v59, v60, v48\n\t" \
  "v_max3_u32 v48, v61, v62, v48\n\t" \
  "v_max3_u32 v48, v63, v50, v48\n\t" \
  "v_cmp_eq_u32 vcc, -1, v48\n\t" \
  "s_nop 1\n\t" \
  "s_cbranch_vccz Loa%=\n\t" \
  "s_add_u32 s97, s97, 1\n\t" \
  "s_cmp_gt_u32 s97, 0xffff\n\t" \
  "s_cbranch_scc1 Loa%=\n\t" \
  "s_sleep 2\n\t" \
  "s_add_u32 m0, %[m0a], 0\n\t" \
  "v_subrev_u32 v48, 24576, v95\n\t" \
  "global_load_lds_dwordx4 v48, %[wsb] sc1\n\t" \
  "global_load_lds_dwordx4 v48, %[wsb] offset:1024 sc1\n\t" \
  "s_add_u32 m0, %[m0a], 8192\n\t" \
  "v_subrev_u32 v48, 16384, v95\n\t" \
  "global_load_lds_dwordx4 v48, %[wsb] sc1\n\t" \
  "global_load_lds_dwordx4 v48, %[wsb] offset:1024 sc1\n\t" \
  "s_waitcnt vmcnt(0)\n\t" \
  "s_branch Lca%=\n\t" \
  "Loa%=:\n\t" \
  "s_barrier\n\t" \
  "ds_read_b128 v[64:67], v92 offset:0\n\t" \
  "ds_read_b128 v[68:71], v93 offset:0\n\t" \
  "ds_read_b128 v[72:75], v93 offset:0\n\t" \
  "s_mov_b32 %[n], 0\n\t" \
  "s_waitcnt lgkmcnt(0)\n\t" \
  "v_mfma_f32_32x32x16_bf16 v[32:47], v[64:67], v[84:87], 0\n\t" \
  "s_getpc_b64 s[92:93]\n\t" \
  "Lpc%=:\n\t" \
  "s_add_u32 s90, s92, Lb0%=-Lpc%=\n\t" \
  "s_addc_u32 s91, s93, 0\n\t" \
  "s_cmp_eq_u32 %[kh], 0\n\t" \
  "s_cselect_b32 s94, 0, Lb1%=-Lb0%=\n\t" \
  "s_add_u32 s90, s90, s94\n\t" \
  "s_addc_u32 s91, s91, 0\n\t" \
  "s_add_u32 s92, s92, Ltab%=-Lpc%=\n\t" \
  "s_addc_u32 s93, s93, 0\n\t" \
  "s_setprio 1\n\t" \
  "s_nop 5\n\t" \
  "s_setpc_b64 s[90:91]\n\t" \
  "Lb0%=:\n\t" \
  "s_setprio 2\n\t" \
  "v_exp_f32 v32, v32\n\t" \
  "v_exp_f32 v33, v33\n\t" \
  "s_waitcnt lgkmcnt(2)\n\t" \
  "v_exp_f32 v34, v34\n\t" \
  "v_mfma_f32_32x32x16_bf16 v[16:31], v[72:75], v[80:83], v[16:31]\n\t" \
  "ds_read_b128 v[72:75], v93 offset:512\n\t" \
  "v_exp_f32 v35, v35\n\t" \
  "v_cvt_pk_bf16_f32 v76, v32, v33\n\t" \
  "v_exp_f32 v36, v36\n\t" \
  "v_exp_f32 v37, v37\n\t" \
  "v_mfma_f32_32x32x16_bf16 v[48:63], v[64:67], v[88:91], 0\n\t" \
  "ds_read_b128 v[64:67], v92 offset:2048\n\t" \
  "v_cvt_pk_bf16_f32 v77, v34, v35\n\t" \
  "v_exp_f32 v38, v38\n\t" \
  "v_exp_f32 v39, v39\n\t" \
  "v_cvt_pk_bf16_f32 v78, v36, v37\n\t" \
  "v_exp_f32 v40, v40\n\t" \
  "v_exp_f32 v41, v41\n\t" \
  "v_cvt_pk_bf16_f32 v79, v38, v39\n\t" \
  "v_exp_f32 v42, v42\n\t" \
  "v_exp_f32 v43, v43\n\t" \
  "s_waitcnt lgkmcnt(2)\n\t" \
  "v_exp_f32 v44, v44\n\t" \
  "v_mfma_f32_32x32x16_bf16 v[0:15], v[68:71], v[76:79], v[0:15]\n\t" \
  "v_cvt_pk_bf16_f32 v80, v40, v41\n\t" \
  "v_exp_f32 v45, v45\n\t" \
  "v_cvt_pk_bf16_f32 v81, v42, v43\n\t" \
  "v_exp_f32 v46, v46\n\t" \
  "v_exp_f32 v47, v47\n\t" \
  "v_cvt_pk_bf16_f32 v82, v44, v45\n\t" \
  "v_cvt_pk_bf16_f32 v83, v46, v47\n\t" \
  "v_exp_f32 v48, v48\n\t" \
  "v_exp_f32 v49, v49\n\t" \
  "s_waitcnt lgkmcnt(1)\n\t" \
  "v_exp_f32 v50, v50\n\t" \
  "v_mfma_f32_32x32x16_bf16 v[0:15], v[72:75], v[80:83], v[0:15]\n\t" \
  "v_exp_f32 v51, v51\n\t" \
  "v_cvt_pk_bf16_f32 v76, v48, v49\n\t" \
  "v_exp_f32 v52, v52\n\t" \
  "v_exp_f32 v53, v53\n\t" \
  "s_waitcnt lgkmcnt(0)\n\t" \
  "v_exp_f32 v54, v54\n\t" \
  "v_mfma_f32_32x32x16_bf16 v[32:47], v[64:67], v[84:87], 0\n\t" \
  "v_cvt_pk_bf16_f32 v77, v50, v51\n\t" \
  "v_exp_f32 v55, v55\n\t" \
  "v_cvt_pk_bf16_f32 v78, v52, v53\n\t" \
  "v_exp_f32 v56, v56\n\t" \
  "v_exp_f32 v57, v57\n\t" \
  "v_cvt_pk_bf16_f32 v79, v54, v55\n\t" \
  "v_exp_f32 v58, v58\n\t" \
  "v_exp_f32 v59, v59\n\t" \
  "v_mfma_f32_32x32x16_bf16 v[16:31], v[68:71], v[76:79], v[16:31]\n\t" \
  "ds_read_b128 v[68:71], v93 offset:2048\n\t" \
  "v_cvt_pk_bf16_f32 v80, v56, v57\n\t" \
  "v_exp_f32 v60, v60\n\t" \
  "v_exp_f32 v61, v61\n\t" \
  "v_cvt_pk_bf16_f32 v81, v58, v59\n\t" \
  "v_exp_f32 v62, v62\n\t" \
  "v_exp_f32 v63, v63\n\t" \
  "v_cvt_pk_bf16_f32 v82, v60, v61\n\t" \
  "v_cvt_pk_bf16_f32 v83, v62, v63\n\t" \
  "v_exp_f32 v32, v32\n\t" \
  "v_exp_f32 v33, v33\n\t" \
  "s_waitcnt lgkmcnt(2)\n\t" \
  "v_exp_f32 v34, v34\n\t" \
  "v_mfma_f32_32x32x16_bf16 v[16:31], v[72:75], v[80:83], v[16:31]\n\t" \
  "ds_read_b128 v[72:75], v93 offset:2560\n\t" \
  "v_exp_f32 v35, v35\n\t" \
  "v_cvt_pk_bf16_f32 v76, v32, v33\n\t" \
  "v_exp_f32 v36, v36\n\t" \
  "v_exp_f32 v37, v37\n\t" \
  "v_mfma_f32_32x32x16_bf16 v[48:63], v[64:67], v[88:91], 0\n\t" \
  "ds_read_b128 v[64:67], v92 offset:4096\n\t" \
  "v_cvt_pk_bf16_f32 v77, v34, v35\n\t" \
  "v_exp_f32 v38, v38\n\t" \
  "v_exp_f32 v39, v39\n\t" \
  "v_cvt_pk_bf16_f32 v78, v36, v37\n\t" \
  "v_exp_f32 v40, v40\n\t" \
  "v_exp_f32 v41, v41\n\t" \
  "v_cvt_pk_bf16_f32 v79, v38, v39\n\t" \
  "v_exp_f32 v42, v42\n\t" \
  "v_exp_f32 v43, v43\n\t" \
  "s_waitcnt lgkmcnt(2)\n\t" \
  "v_exp_f32 v44, v44\n\t" \
  "v_mfma_f32_32x32x16_bf16 v[0:15], v[68:71], v[76:79], v[0:15]\n\t" \
  "v_cvt_pk_bf16_f32 v80, v40, v41\n\t" \
  "v_exp_f32 v45, v45\n\t" \
  "v_cvt_pk_bf16_f32 v81, v42, v43\n\t" \
  "v_exp_f32 v46, v46\n\t" \
  "v_exp_f32 v47, v47\n\t" \
  "v_cvt_pk_bf16_f32 v82, v44, v45\n\t" \
  "v_cvt_pk_bf16_f32 v83, v46, v47\n\t" \
  "s_setprio 0\n\t" \
  "v_exp_f32 v48, v48\n\t" \
  "v_exp_f32 v49, v49\n\t" \
  "s_waitcnt lgkmcnt(1)\n\t" \
  "v_exp_f32 v50, v50\n\t" \
  "v_mfma_f32_32x32x16_bf16 v[0:15], v[72:75], v[80:83], v[0:15]\n\t" \
  "v_exp_f32 v51, v51\n\t" \
  "v_cvt_pk_bf16_f32 v76, v48, v49\n\t" \
  "v_exp_f32 v52, v52\n\t" \
  "v_exp_f32 v53, v53\n\t" \
  "s_waitcnt lgkmcnt(0)\n\t" \
  "v_exp_f32 v54, v54\n\t" \
  "v_mfma_f32_32x32x16_bf16 v[32:47], v[64:67], v[84:87], 0\n\t" \
  "v_cvt_pk_bf16_f32 v77, v50, v51\n\t" \
  "v_exp_f32 v55, v55\n\t" \
  "v_cvt_pk_bf16_f32 v78, v52, v53\n\t" \
  "v_exp_f32 v56, v56\n\t" \
  "v_exp_f32 v57, v57\n\t" \
  "v_cvt_pk_bf16_f32 v79, v54, v55\n\t" \
  "v_exp_f32 v58, v58\n\t" \
  "v_exp_f32 v59, v59\n\t" \
  "v_mfma_f32_32x32x16_bf16 v[16:31], v[68:71], v[76:79], v[16:31]\n\t" \
  "ds_read_b128 v[68:71], v93 offset:4096\n\t" \
  "v_cvt_pk_bf16_f32 v80, v56, v57\n\t" \
  "v_exp_f32 v60, v60\n\t" \
  "v_exp_f32 v61, v61\n\t" \
  "v_cvt_pk_bf16_f32 v81, v58, v59\n\t" \
  "v_exp_f32 v62, v62\n\t" \
  "v_exp_f32 v63, v63\n\t" \
  "v_cvt_pk_bf16_f32 v82, v60, v61\n\t" \
  "v_cvt_pk_bf16_f32 v83, v62, v63\n\t" \
  "s_add_u32 %[n], %[n], 1\n\t" \
  "v_add_u32 v92, 0x1000, v92\n\t" \
  "s_lshl_b32 s94, %[n], 6\n\t" \
  "v_add_u32 v93, v94, v93\n\t" \
  "s_add_u32 s94, s92, s94\n\t" \
  "s_addc_u32 s95, s93, 0\n\t" \
  "s_setpc_b64 s[94:95]\n\t" \
  "Lb1%=:\n\t" \
  "v_exp_f32 v32, v32\n\t" \
  "v_exp_f32 v33, v33\n\t" \
  "s_waitcnt lgkmcnt(2)\n\t" \
  "v_exp_f32 v34, v34\n\t" \
  "v_mfma_f32_32x32x16_bf16 v[16:31], v[72:75], v[80:83], v[16:31]\n\t" \
  "ds_read_b128 v[72:75], v93 offset:512\n\t" \
  "v_exp_f32 v35, v35\n\t" \
  "v_cvt_pk_bf16_f32 v76, v32, v33\n\t" \
  "v_exp_f32 v36, v36\n\t" \
  "v_exp_f32 v37, v37\n\t" \
  "v_mfma_f32_32x32x16_bf16 v[48:63], v[64:67], v[88:91], 0\n\t" \
  "ds_read_b128 v[64:67], v92 offset:2048\n\t" \
  "v_cvt_pk_bf16_f32 v77, v34, v35\n\t" \
  "v_exp_f32 v38, v38\n\t" \
  "v_exp_f32 v39, v39\n\t" \
  "v_cvt_pk_bf16_f32 v78, v36, v37\n\t" \
  "v_exp_f32 v40, v40\n\t" \
  "v_exp_f32 v41, v41\n\t" \
  "v_cvt_pk_bf16_f32 v79, v38, v39\n\t" \
  "v_exp_f32 v42, v42\n\t" \
  "v_exp_f32 v43, v43\n\t" \
  "s_waitcnt lgkmcnt(2)\n\t" \
  "v_exp_f32 v44, v44\n\t" \
  "v_mfma_f32_32x32x16_bf16 v[0:15], v[68:71], v[76:79], v[0:15]\n\t" \
  "v_cvt_pk_bf16_f32 v80, v40, v41\n\t" \
  "v_exp_f32 v45, v45\n\t" \
  "v_cvt_pk_bf16_f32 v81, v42, v43\n\t" \
  "v_exp_f32 v46, v46\n\t" \
  "v_exp_f32 v47, v47\n\t" \
  "v_cvt_pk_bf16_f32 v82, v44, v45\n\t" \
  "v_cvt_pk_bf16_f32 v83, v46, v47\n\t" \
  "v_exp_f32 v48, v48\n\t" \
  "v_exp_f32 v49, v49\n\t" \
  "s_waitcnt lgkmcnt(1)\n\t" \
  "v_exp_f32 v50, v50\n\t" \
  "v_mfma_f32_32x32x16_bf16 v[0:15], v[72:75], v[80:83], v[0:15]\n\t" \
  "v_exp_f32 v51, v51\n\t" \
  "v_cvt_pk_bf16_f32 v76, v48, v49\n\t" \
  "v_exp_f32 v52, v52\n\t" \
  "v_exp_f32 v53, v53\n\t" \
  "s_waitcnt lgkmcnt(0)\n\t" \
  "v_exp_f32 v54, v54\n\t" \
  "v_mfma_f32_32x32x16_bf16 v[32:47], v[64:67], v[84:87], 0\n\t" \
  "v_cvt_pk_bf16_f32 v77, v50, v51\n\t" \
  "v_exp_f32 v55, v55\n\t" \
  "v_cvt_pk_bf16_f32 v78, v52, v53\n\t" \
  "v_exp_f32 v56, v56\n\t" \
  "v_exp_f32 v57, v57\n\t" \
  "v_cvt_pk_bf16_f32 v79, v54, v55\n\t" \
  "v_exp_f32 v58, v58\n\t" \
  "v_exp_f32 v59, v59\n\t" \
  "v_mfma_f32_32x32x16_bf16 v[16:31], v[68:71], v[76:79], v[16:31]\n\t" \
  "ds_read_b128 v[68:71], v93 offset:2048\n\t" \
  "v_cvt_pk_bf16_f32 v80, v56, v57\n\t" \
  "v_exp_f32 v60, v60\n\t" \
  "v_exp_f32 v61, v61\n\t" \
  "v_cvt_pk_bf16_f32 v81, v58, v59\n\t" \
  "v_exp_f32 v62, v62\n\t" \
  "v_exp_f32 v63, v63\n\t" \
  "v_cvt_pk_bf16_f32 v82, v60, v61\n\t" \
  "v_cvt_pk_bf16_f32 v83, v62, v63\n\t" \
  "v_exp_f32 v32, v32\n\t" \
  "v_exp_f32 v33, v33\n\t" \
  "s_waitcnt lgkmcnt(2)\n\t" \
  "v_exp_f32 v34, v34\n\t" \
  "v_mfma_f32_32x32x16_bf16 v[16:31], v[72:75], v[80:83], v[16:31]\n\t" \
  "ds_read_b128 v[72:75], v93 offset:2560\n\t" \
  "v_exp_f32 v35, v35\n\t" \
  "v_cvt_pk_bf16_f32 v76, v32, v33\n\t" \
  "v_exp_f32 v36, v36\n\t" \
  "v_exp_f32 v37, v37\n\t" \
  "v_mfma_f32_32x32x16_bf16 v[48:63], v[64:67], v[88:91], 0\n\t" \
  "ds_read_b128 v[64:67], v92 offset:4096\n\t" \
  "v_cvt_pk_bf16_f32 v77, v34, v35\n\t" \
  "v_exp_f32 v38, v38\n\t" \
  "v_exp_f32 v39, v39\n\t" \
  "v_cvt_pk_bf16_f32 v78, v36, v37\n\t" \
  "v_exp_f32 v40, v40\n\t" \
  "v_exp_f32 v41, v41\n\t" \
  "v_cvt_pk_bf16_f32 v79, v38, v39\n\t" \
  "v_exp_f32 v42, v42\n\t" \
  "v_exp_f32 v43, v43\n\t" \
  "s_waitcnt lgkmcnt(2)\n\t" \
  "v_exp_f32 v44, v44\n\t" \
  "v_mfma_f32_32x32x16_bf16 v[0:15], v[68:71], v[76:79], v[0:15]\n\t" \
  "v_cvt_pk_bf16_f32 v80, v40, v41\n\t" \
  "v_exp_f32 v45, v45\n\t" \
  "v_cvt_pk_bf16_f32 v81, v42, v43\n\t" \
  "v_exp_f32 v46, v46\n\t" \
  "v_exp_f32 v47, v47\n\t" \
  "v_cvt_pk_bf16_f32 v82, v44, v45\n\t" \
  "v_cvt_pk_bf16_f32 v83, v46, v47\n\t" \
  "v_exp_f32 v48, v48\n\t" \
  "v_exp_f32 v49, v49\n\t" \
  "s_waitcnt lgkmcnt(1)\n\t" \
  "v_exp_f32 v50, v50\n\t" \
  "v_mfma_f32_32x32x16_bf16 v[0:15], v[72:75], v[80:83], v[0:15]\n\t" \
  "v_exp_f32 v51, v51\n\t" \
  "v_cvt_pk_bf16_f32 v76, v48, v49\n\t" \
  "v_exp_f32 v52, v52\n\t" \
  "v_exp_f32 v53, v53\n\t" \
  "s_waitcnt lgkmcnt(0)\n\t" \
  "v_exp_f32 v54, v54\n\t" \
  "v_mfma_f32_32x32x16_bf16 v[32:47], v[64:67], v[84:87], 0\n\t" \
  "v_cvt_pk_bf16_f32 v77, v50, v51\n\t" \
  "v_exp_f32 v55, v55\n\t" \
  "v_cvt_pk_bf16_f32 v78, v52, v53\n\t" \
  "v_exp_f32 v56, v56\n\t" \
  "v_exp_f32 v57, v57\n\t" \
  "v_cvt_pk_bf16_f32 v79, v54, v55\n\t" \
  "v_exp_f32 v58, v58\n\t" \
  "v_exp_f32 v59, v59\n\t" \
  "v_mfma_f32_32x32x16_bf16 v[16:31], v[68:71], v[76:79], v[16:31]\n\t" \
  "ds_read_b128 v[68:71], v93 offset:4096\n\t" \
  "v_cvt_pk_bf16_f32 v80, v56, v57\n\t" \
  "v_exp_f32 v60, v60\n\t" \
  "v_exp_f32 v61, v61\n\t" \
  "v_cvt_pk_bf16_f32 v81, v58, v59\n\t" \
  "v_exp_f32 v62, v62\n\t" \
  "v_exp_f32 v63, v63\n\t" \
  "v_cvt_pk_bf16_f32 v82, v60, v61\n\t" \
  "v_cvt_pk_bf16_f32 v83, v62, v63\n\t" \
  "s_add_u32 %[n], %[n], 1\n\t" \
  "v_add_u32 v92, 0x1000, v92\n\t" \
  "s_lshl_b32 s94, %[n], 6\n\t" \
  "v_add_u32 v93, v94, v93\n\t" \
  "s_add_u32 s94, s92, s94\n\t" \
  "s_addc_u32 s95, s93, 0\n\t" \
  "s_setpc_b64 s[94:95]\n\t" \
  ".p2align 6\n\t" \
  "Ltab%=:\n\t" \
  "s_nop 0\n\t" \
  ".p2align 6\n\t" \
  "s_branch Lh1%=\n\t" \
  ".p2align 6\n\t" \
  "s_branch Lh2%=\n\t" \
  ".p2align 6\n\t" \
  "s_barrier\n\t" \
  "s_mov_b32 m0, s96\n\t" \
  "s_add_u32 s96, s96, 0x2000\n\t" \
  "global_load_lds_dwordx4 v95, %[wsb] sc1\n\t" \
  "global_load_lds_dwordx4 v95, %[wsb] offset:1024 sc1\n\t" \
  "v_add_u32 v95, 0x2000, v95\n\t" \
  "s_setpc_b64 s[90:91]\n\t" \
  ".p2align 6\n\t" \
  "s_branch Lh4%=\n\t" \
  ".p2align 6\n\t" \
  "s_branch Lh5%=\n\t" \
  ".p2align 6\n\t" \
  "s_setpc_b64 s[90:91]\n\t" \
  ".p2align 6\n\t" \
  "s_barrier\n\t" \
  "s_mov_b32 m0, %[m0s]\n\t" \
  "global_load_dwordx4 v[128:131], v96, %[wsb]\n\t" \
  "global_load_dwordx4 v[132:135], v96, %[wsb] offset:1024\n\t" \
  "v_add_u32 v96, 0x800, v96\n\t" \
  "global_load_dwordx4 v[192:195], v97, %[wsb] offset:0\n\t" \
  "global_load_dwordx4 v[196:199], v97, %[wsb] offset:1024\n\t" \
  "s_setpc_b64 s[90:91]\n\t" \
  ".p2align 6\n\t" \
  "global_load_dwordx4 v[136:139], v96, %[wsb]\n\t" \
  "global_load_dwordx4 v[140:143], v96, %[wsb] offset:1024\n\t" \
  "v_add_u32 v96, 0x800, v96\n\t" \
  "global_load_dwordx4 v[200:203], v97, %[wsb] offset:2048\n\t" \
  "global_load_dwordx4 v[204:207], v97, %[wsb] offset:3072\n\t" \
  "v_add_u32 v97, 0x80000, v97\n\t" \
  "s_setpc_b64 s[90:91]\n\t" \
  ".p2align 6\n\t" \
  "global_load_dwordx4 v[144:147], v96, %[wsb]\n\t" \
  "global_load_dwordx4 v[148:151], v96, %[wsb] offset:1024\n\t" \
  "v_add_u32 v96, 0x800, v96\n\t" \
  "global_load_dwordx4 v[208:211], v97, %[wsb] offset:0\n\t" \
  "global_load_dwordx4 v[212:215], v97, %[wsb] offset:1024\n\t" \
  "s_setpc_b64 s[90:91]\n\t" \
  ".p2align 6\n\t" \
  "global_load_dwordx4 v[152:155], v96, %[wsb]\n\t" \
  "global_load_dwordx4 v[156:159], v96, %[wsb] offset:1024\n\t" \
  "v_add_u32 v96, 0x800, v96\n\t" \
  "global_load_dwordx4 v[216:219], v97, %[wsb] offset:2048\n\t" \
  "global_load_dwordx4 v[220:223], v97, %[wsb] offset:3072\n\t" \
  "v_add_u32 v97, 0x80000, v97\n\t" \
  "s_setpc_b64 s[90:91]\n\t" \
  ".p2align 6\n\t" \
  "global_load_dwordx4 v[160:163], v96, %[wsb]\n\t" \
  "global_load_dwordx4 v[164:167], v96, %[wsb] offset:1024\n\t" \
  "v_add_u32 v96, 0x800, v96\n\t" \
  "global_load_dwordx4 v[224:227], v97, %[wsb] offset:0\n\t" \
  "global_load_dwordx4 v[228:231], v97, %[wsb] offset:1024\n\t" \
  "s_setpc_b64 s[90:91]\n\t" \
  ".p2align 6\n\t" \
  "global_load_dwordx4 v[168:171], v96, %[wsb]\n\t" \
  "global_load_dwordx4 v[172:175], v96, %[wsb] offset:1024\n\t" \
  "v_add_u32 v96, 0x800, v96\n\t" \
  "global_load_dwordx4 v[232:235], v97, %[wsb] offset:2048\n\t" \
  "global_load_dwordx4 v[236:239], v97, %[wsb] offset:3072\n\t" \
  "v_add_u32 v97, 0x80000, v97\n\t" \
  "s_setpc_b64 s[90:91]\n\t" \
  ".p2align 6\n\t" \
  "global_load_dwordx4 v[176:179], v96, %[wsb]\n\t" \
  "global_load_dwordx4 v[180:183], v96, %[wsb] offset:1024\n\t" \
  "v_add_u32 v96, 0x800, v96\n\t" \
  "s_setpc_b64 s[90:91]\n\t" \
  ".p2align 6\n\t" \
  "global_load_dwordx4 v[184:187], v96, %[wsb]\n\t" \
  "global_load_dwordx4 v[188:191], v96, %[wsb] offset:1024\n\t" \
  "v_add_u32 v96, 0x800, v96\n\t" \
  "s_setpc_b64 s[90:91]\n\t" \
  ".p2align 6\n\t" \
  "s_setpc_b64 s[90:91]\n\t" \
  ".p2align 6\n\t" \
  "s_branch Lend%=\n\t" \
  "Lh1%=:\n\t" \
  "s_waitcnt vmcnt(0)\n\t" \
  "s_mov_b32 s97, 0\n\t" \
  "Lch1%=:\n\t" \
  "ds_read_b128 v[48:51], v98 offset:16384\n\t" \
  "ds_read_b128 v[52:55], v98 offset:17408\n\t" \
  "s_waitcnt lgkmcnt(0)\n\t" \
  "v_max3_u32 v48, v48, v49, v50\n\t" \
  "v_max3_u32 v48, v51, v52, v48\n\t" \
  "v_max3_u32 v48, v53, v54, v48\n\t" \
  "v_max3_u32 v48, v55, v50, v48\n\t" \
  "v_cmp_eq_u32 vcc, -1, v48\n\t" \
  "s_nop 1\n\t" \
  "s_cbranch_vccz Loh1%=\n\t" \
  "s_add_u32 s97, s97, 1\n\t" \
  "s_cmp_gt_u32 s97, 0xffff\n\t" \
  "s_cbranch_scc1 Loh1%=\n\t" \
  "s_sleep 2\n\t" \
  "s_add_u32 m0, %[m0a], 16384\n\t" \
  "v_subrev_u32 v48, 8192, v95\n\t" \
  "global_load_lds_dwordx4 v48, %[wsb] sc1\n\t" \
  "global_load_lds_dwordx4 v48, %[wsb] offset:1024 sc1\n\t" \
  "s_waitcnt vmcnt(0)\n\t" \
  "s_branch Lch1%=\n\t" \
  "Loh1%=:\n\t" \
  "s_mov_b32 m0, s96\n\t" \
  "s_add_u32 s96, s96, 0x2000\n\t" \
  "global_load_lds_dwordx4 v95, %[wsb] sc1\n\t" \
  "global_load_lds_dwordx4 v95, %[wsb] offset:1024 sc1\n\t" \
  "v_add_u32 v95, 0x2000, v95\n\t" \
  "s_mov_b32 m0, s96\n\t" \
  "s_add_u32 s96, s96, 0x2000\n\t" \
  "global_load_lds_dwordx4 v95, %[wsb] sc1\n\t" \
  "global_load_lds_dwordx4 v95, %[wsb] offset:1024 sc1\n\t" \
  "v_add_u32 v95, 0x2000, v95\n\t" \
  "s_setpc_b64 s[90:91]\n\t" \
  "Lh2%=:\n\t" \
  "s_waitcnt vmcnt(0)\n\t" \
  "s_mov_b32 s97, 0\n\t" \
  "Lch2%=:\n\t" \
  "ds_read_b128 v[48:51], v98 offset:24576\n\t" \
  "ds_read_b128 v[52:55], v98 offset:25600\n\t" \
  "ds_read_b128 v[56:59], v98 offset:32768\n\t" \
  "ds_read_b128 v[60:63], v98 offset:33792\n\t" \
  "s_waitcnt lgkmcnt(0)\n\t" \
  "v_max3_u32 v48, v48, v49, v50\n\t" \
  "v_max3_u32 v48, v51, v52, v48\n\t" \
  "v_max3_u32 v48, v53, v54, v48\n\t" \
  "v_max3_u32 v48, v55, v56, v48\n\t" \
  "v_max3_u32 v48, v57, v58, v48\n\t" \
  "v_max3_u32 v48, v59, v60, v48\n\t" \
  "v_max3_u32 v48, v61, v62, v48\n\t" \
  "v_max3_u32 v48, v63, v50, v48\n\t" \
  "v_cmp_eq_u32 vcc, -1, v48\n\t" \
  "s_nop 1\n\t" \
  "s_cbranch_vccz Loh2%=\n\t" \
  "s_add_u32 s97, s97, 1\n\t" \
  "s_cmp_gt_u32 s97, 0xffff\n\t" \
  "s_cbranch_scc1 Loh2%=\n\t" \
  "s_sleep 2\n\t" \
  "s_add_u32 m0, %[m0a], 24576\n\t" \
  "v_subrev_u32 v48, 16384, v95\n\t" \
  "global_load_lds_dwordx4 v48, %[wsb] sc1\n\t" \
  "global_load_lds_dwordx4 v48, %[wsb] offset:1024 sc1\n\t" \
  "s_add_u32 m0, %[m0a], 32768\n\t" \
  "v_subrev_u32 v48, 8192, v95\n\t" \
  "global_load_lds_dwordx4 v48, %[wsb] sc1\n\t" \
  "global_load_lds_dwordx4 v48, %[wsb] offset:1024 sc1\n\t" \
  "s_waitcnt vmcnt(0)\n\t" \
  "s_branch Lch2%=\n\t" \
  "Loh2%=:\n\t" \
  "s_mov_b32 m0, s96\n\t" \
  "s_add_u32 s96, s96, 0x2000\n\t" \
  "global_load_lds_dwordx4 v95, %[wsb] sc1\n\t" \
  "global_load_lds_dwordx4 v95, %[wsb] offset:1024 sc1\n\t" \
  "v_add_u32 v95, 0x2000, v95\n\t" \
  "s_mov_b32 m0, s96\n\t" \
  "s_add_u32 s96, s96, 0x2000\n\t" \
  "global_load_lds_dwordx4 v95, %[wsb] sc1\n\t" \
  "global_load_lds_dwordx4 v95, %[wsb] offset:1024 sc1\n\t" \
  "v_add_u32 v95, 0x2000, v95\n\t" \
  "s_setpc_b64 s[90:91]\n\t" \
  "Lh4%=:\n\t" \
  "s_waitcnt vmcnt(2)\n\t" \
  "s_mov_b32 s97, 0\n\t" \
  "Lch4%=:\n\t" \
  "ds_read_b128 v[48:51], v98 offset:40960\n\t" \
  "ds_read_b128 v[52:55], v98 offset:41984\n\t" \
  "ds_read_b128 v[56:59], v98 offset:49152\n\t" \
  "ds_read_b128 v[60:63], v98 offset:50176\n\t" \
  "s_waitcnt lgkmcnt(0)\n\t" \
  "v_max3_u32 v48, v48, v49, v50\n\t" \
  "v_max3_u32 v48, v51, v52, v48\n\t" \
  "v_max3_u32 v48, v53, v54, v48\n\t" \
  "v_max3_u32 v48, v55, v56, v48\n\t" \
  "v_max3_u32 v48, v57, v58, v48\n\t" \
  "v_max3_u32 v48, v59, v60, v48\n\t" \
  "v_max3_u32 v48, v61, v62, v48\n\t" \
  "v_max3_u32 v48, v63, v50, v48\n\t" \
  "v_cmp_eq_u32 vcc, -1, v48\n\t" \
  "s_nop 1\n\t" \
  "s_cbranch_vccz Loh4%=\n\t" \
  "s_add_u32 s97, s97, 1\n\t" \
  "s_cmp_gt_u32 s97, 0xffff\n\t" \
  "s_cbranch_scc1 Loh4%=\n\t" \
  "s_sleep 2\n\t" \
  "s_add_u32 m0, %[m0a], 40960\n\t" \
  "v_subrev_u32 v48, 24576, v95\n\t" \
  "global_load_lds_dwordx4 v48, %[wsb] sc1\n\t" \
  "global_load_lds_dwordx4 v48, %[wsb] offset:1024 sc1\n\t" \
  "s_add_u32 m0, %[m0a], 49152\n\t" \
  "v_subrev_u32 v48, 16384, v95\n\t" \
  "global_load_lds_dwordx4 v48, %[wsb] sc1\n\t" \
  "global_load_lds_dwordx4 v48, %[wsb] offset:1024 sc1\n\t" \
  "s_waitcnt vmcnt(0)\n\t" \
  "s_branch Lch4%=\n\t" \
  "Loh4%=:\n\t" \
  "s_setpc_b64 s[90:91]\n\t" \
  "Lh5%=:\n\t" \
  "s_waitcnt vmcnt(0)\n\t" \
  "s_mov_b32 s97, 0\n\t" \
  "Lch5%=:\n\t" \
  "ds_read_b128 v[48:51], v98 offset:57344\n\t" \
  "ds_read_b128 v[52:55], v98 offset:58368\n\t" \
  "s_waitcnt lgkmcnt(0)\n\t" \
  "v_max3_u32 v48, v48, v49, v50\n\t" \
  "v_max3_u32 v48, v51, v52, v48\n\t" \
  "v_max3_u32 v48, v53, v54, v48\n\t" \
  "v_max3_u32 v48, v55, v50, v48\n\t" \
  "v_cmp_eq_u32 vcc, -1, v48\n\t" \
  "s_nop 1\n\t" \
  "s_cbranch_vccz Loh5%=\n\t" \
  "s_add_u32 s97, s97, 1\n\t" \
  "s_cmp_gt_u32 s97, 0xffff\n\t" \
  "s_cbranch_scc1 Loh5%=\n\t" \
  "s_sleep 2\n\t" \
  "s_add_u32 m0, %[m0a], 57344\n\t" \
  "v_subrev_u32 v48, 8192, v95\n\t" \
  "global_load_lds_dwordx4 v48, %[wsb] sc1\n\t" \
  "global_load_lds_dwordx4 v48, %[wsb] offset:1024 sc1\n\t" \
  "s_waitcnt vmcnt(0)\n\t" \
  "s_branch Lch5%=\n\t" \
  "Loh5%=:\n\t" \
  "s_setpc_b64 s[90:91]\n\t" \
  "Lend%=:\n\t" \
  "s_waitcnt lgkmcnt(0)\n\t" \
  "v_mfma_f32_32x32x16_bf16 v[16:31], v[72:75], v[80:83], v[16:31]\n\t" \
  "s_setprio 0\n\t" \
  "s_waitcnt vmcnt(0)\n\t" \
  "s_nop 7\n\t" \
  "s_nop 7\n\t" \
  ""


template <class W, class H> DI f32x16 gemm_half(const LAS char* act, W wf, H hook) {
  f32x16 acc = zero16();
  u32x4 a[16];
#pragma unroll
  for (int i = 0; i < 4; ++i) a[i] = *(const LAS u32x4*)(act + i * 1024);
#pragma unroll
  for (int i = 0; i < 16; ++i) {
    SBAR();
    if (i + 4 < 16) a[i + 4] = *(const LAS u32x4*)(act + (i + 4) * 1024);
    acc = MFMA(wf(i), a[i], acc);
    SBAR();
    hook(i);
  }
  SBAR();
  return acc;
}
DI f32x4 lds2(const LAS char* p, int half) { return *(const LAS f32x4*)p + *(const LAS f32x4*)(p + half); }
DI void resid_epilogue(const f32x16& acc, u32x4& xr, rsrc_t rs, int xoff, LAS char* part, uint16_t* out, bool final_, int tt, int ks, int lane) {
  const int r = lane & 31, h = lane >> 5;
  const u32x4 y = pack8(acc, 0);
#pragma unroll
  for (int j = 0; j < 4; ++j) xr[j] = cvtpk(bflo(xr[j]) + bflo(y[j]), bfhi(xr[j]) + bfhi(y[j]));
  xr = clean(xr);
  if (final_) {
    auto a = __builtin_amdgcn_permlane32_swap(xr[0], xr[2], false, false);
    auto c = __builtin_amdgcn_permlane32_swap(xr[1], xr[3], false, false);
    *(u32x4*)(out + (size_t)(tt * 32 + r) * DM + ks * 16 + 8 * h) = u32x4{a[0], c[0], a[1], c[1]};
  } else {
    st128(rs, lane * 16, xoff + (tt * 32 + ks) * 1024, xr);
    const float s = xhalf_sum(sum8(xr)), q = xhalf_sum(sq8(xr));
    if (h == 0) *(LAS f32x2*)(part + r * 8) = f32x2{s, q};
  }
}

constexpr int NEARLY = 12;
constexpr int QPUB = 5, RLDAT = 6;
constexpr int W2LO = 8;
__global__ __launch_bounds__(512, 2) void k_mega(char* ws, uint16_t* out) {
  extern __shared__ __attribute__((aligned(16))) char smem_[];
  LAS char* smem = (LAS char*)smem_;
  const unsigned lds0 = (unsigned)(uintptr_t)smem;
  const rsrc_t rs = __builtin_amdgcn_make_buffer_rsrc(ws, 0, WS_END, 0x00020000);
  const int w_ = __builtin_amdgcn_readfirstlane(threadIdx.x >> 6);
#define IDS() int tid = threadIdx.x, w = w_, b = blockIdx.x; asm volatile("" : "+v"(tid)); asm volatile("" : "+s"(w), "+s"(b)); \
  const int lane = tid & 63, r = lane & 31, h = lane >> 5, w4 = w & 3, kh = w >> 2, xcd = b & 7, idx = b >> 3; \
  const int tt = xcd * 8 + (idx >> 2), cb = idx & 3, nt = cb * 4 + w4;                 \
  const int panel = (xcd & 3) * 4 + (idx & 3), hp = (xcd >> 2) * 8 + (idx >> 2);       \
  const unsigned voff = lane * 16, vrot = voff ^ (kh << 8); \
  (void)r; (void)h; (void)tt; (void)nt; (void)panel; (void)hp; (void)voff; (void)vrot
  u32x4 xr;
  {
    IDS();
#pragma unroll
    for (int t = 0; t < 3; ++t) glds4(ws, OFF_W + ((t * 16 + hp) * 32 + 4 * w) * 1024 + voff, lds0 + L_WL + (t * 32 + 4 * w) * 1024);
    xr = ld128(rs, voff, OFF_XF + (tt * 32 + 2 * nt + kh) * 1024);
  }

#pragma unroll 1
  for (int L = 0; L < 2; ++L) {
    const int xa = OFF_XF + (2 * L) * 2048 * KB, xb = xa + 2048 * KB, xc = xb + 2048 * KB;
    const int sa_ = OFF_ST + (2 * L) * 64 * KB, sb_ = sa_ + 64 * KB, sc_ = sb_ + 64 * KB;
    const int pqo = OFF_PQ + L * 2048 * KB, ofo = OFF_OF + L * 2048 * KB, kvo = OFF_KV + L * 128 * KB;
    const int kfo = OFF_KVF + L * KVF_B, kpo = OFF_KVP + L * KVP_B;
    u32x4 wp[16];
    {
      IDS();
      const int t1 = panel * 4 + w4;
      constexpr int WIN = 4;
      u32x4 xf[16];
#define XLD(i) xf[i] = ld128(rs, vrot + ((i) & 3) * 1024, xa + (t1 * 32 + kh * 16 + ((i) & ~3)) * 1024)
#define XMAX(g) umax4(xf[4 * (g)], umax4(xf[4 * (g) + 1], umax4(xf[4 * (g) + 2], umax4(xf[4 * (g) + 3], 0u))))
      if (w == 4) glds1(ws, OFF_CS1 + hp * 1024 + voff, lds0 + 128 * KB);
      u32x4 sg;
#define X1LD() do { _Pragma("unroll") for (int i = 0; i < WIN; ++i) XLD(i); } while (0)
#define S1LD() sg = ld128(rs, (r ^ (kh << 4)) * 32 + h * 16, sa_ + t1 * 1024)
      X1LD();
      SBAR();
      VERIFY(XMAX(0), X1LD());
      BAR();
      f32x16 aq = zero16(), ak = zero16(), av = zero16();
      const LAS char* wl = smem + L_WL + kh * 16384;
      u32x4 wf[2][2];
#define WLD(i) do { wf[(i) & 1][0] = *(const LAS u32x4*)(wl + voff + (32 + (i)) * 1024); wf[(i) & 1][1] = *(const LAS u32x4*)(wl + voff + (64 + (i)) * 1024); } while (0)
      WLD(0);
      RowStat st;
      LAS float* sst = (LAS float*)(smem + 129 * KB + w * 256);
#pragma unroll
      for (int i = 0; i < 16; ++i) {
        SBAR();
        if (i < 15) WLD(i + 1);
        ak = MFMA(xf[i], wf[i & 1][0], ak);
        av = MFMA(xf[i], wf[i & 1][1], av);
        SBAR();
        if (2 * i + WIN < 16) { XLD(2 * i + WIN); XLD(2 * i + WIN + 1); }
        if ((i & 3) == 3 && i < 15 && L == 1) VERIFY_FAST(XMAX((i >> 2) + 1), { XLD(i + 1); XLD(i + 2); XLD(i + 3); XLD(i + 4); });
        if (i == 6) S1LD();
        if (i == 12) {
          VERIFY_FAST(umax4(sg, 0u), S1LD());
          st = row_stats(sg);
          if (h == 0) { sst[r] = st.mu; sst[32 + r] = st.rs; }
        }
      }
      SBAR();
      put_hi(smem + L_ACT + w * 4096 + voff, ak); put_hi(smem + L_ACT + w * 4096 + 2048 + voff, av);
      if (kh == 0) {
        const u32x4 pz = {POISON, POISON, POISON, POISON};
#pragma unroll
        for (int k = 0; k < 2; ++k) {
          st128(rs, voff + k * 1024, pqo + (t1 * 32 + 2 * hp) * 1024, pz);
          st128(rs, voff + k * 1024, xb + (tt * 32 + 2 * nt) * 1024, pz);
          st128(rs, voff + k * 1024, ofo + (tt * 32 + 2 * nt) * 1024, pz);
          if (L == 0) st128(rs, voff + k * 1024, xc + (tt * 32 + 2 * nt) * 1024, pz);
        }
        if (cb == 0 && w < 2) st128(rs, voff, kvo + tt * 2048 + w * 1024, pz);
        if (w == 2 && lane < 32) st64(rs, lane * 32, sb_ + tt * 1024 + cb * 8, u32x2{POISON, POISON});
        if (L == 0 && w == 3 && lane < 32) st64(rs, lane * 32, sc_ + tt * 1024 + cb * 8, u32x2{POISON, POISON});
      }
      LAS char* exr = smem + L_ACT + (w ^ 4) * 4096;
      const LAS char* csl = smem + 128 * KB;
      const float csk = *(const LAS float*)(csl + 128 + r * 4) + *(const LAS float*)(csl + 640 + r * 4);
      const float csv = *(const LAS float*)(csl + 256 + r * 4) + *(const LAS float*)(csl + 768 + r * 4);
      f32x4 mu[2], rr[2];
#pragma unroll
      for (int g = 0; g < 2; ++g) { mu[g] = *(const LAS f32x4*)(sst + 8 * g + 4 * h); rr[g] = *(const LAS f32x4*)(sst + 32 + 8 * g + 4 * h); }
      BAR();
      add_lo(exr + voff, ak); add_lo(exr + 2048 + voff, av);
#pragma unroll
      for (int g = 0; g < 2; ++g) {
#pragma unroll
        for (int j = 0; j < 4; ++j) {
          ak[4 * g + j] = phi(ak[4 * g + j] - mu[g][j] * csk, rr[g][j], rr[g][j] * LOG2E);
          av[4 * g + j] = rr[g][j] * (av[4 * g + j] - mu[g][j] * csv);
        }
      }
      const u32x4 ones = {0x3f803f80u, 0x3f803f80u, 0x3f803f80u, 0x3f803f80u};
      const u32x4 pk = pack8(ak, 0);
      const f32x16 z = MFMA(pk, pack8(av, 0), zero16());
      const f32x16 zs = MFMA(pk, ones, zero16());
#define WQLD(i) wf[(i) & 1][0] = *(const LAS u32x4*)(wl + vrot + (i) * 1024)
      WQLD(0);
      {
        LAS float* red = (LAS float*)exr + h * 8 + (r & 15) * 16;
        if (r < 16) {
          *(LAS f32x4*)red = f32x4{z[0], z[1], z[2], z[3]}; *(LAS f32x4*)(red + 4) = f32x4{z[4], z[5], z[6], z[7]};
          if (r == 0) { *(LAS f32x4*)(red + 256) = f32x4{zs[0], zs[1], zs[2], zs[3]}; *(LAS f32x4*)(red + 260) = f32x4{zs[4], zs[5], zs[6], zs[7]}; }
          asm volatile("" ::: "memory");
        } else {
          *(LAS f32x4*)(red + 272) = f32x4{z[8], z[9], z[10], z[11]}; *(LAS f32x4*)(red + 276) = f32x4{z[12], z[13], z[14], z[15]};
          if (r == 16) { *(LAS f32x4*)(red + 528) = f32x4{zs[8], zs[9], zs[10], zs[11]}; *(LAS f32x4*)(red + 532) = f32x4{zs[12], zs[13], zs[14], zs[15]}; }
        }
      }
      BAR();
      if (tid < 136) {
        const LAS char* ra = smem + L_ACT + tid * 16;
        f32x4 t = {0.f, 0.f, 0.f, 0.f};
#pragma unroll
        for (int k = 0; k < 8; k += 2) t += *(const LAS f32x4*)(ra + k * 4096) + *(const LAS f32x4*)(ra + (k + 1) * 4096);
        st128(rs, tid * 16, kpo + (hp * 16 + panel) * 2176, clean(__builtin_bit_cast(u32x4, t)));
      }
      const bool red_on = w == 1 || w == 2 || (w == 3 && panel < 2);
      const int rrow = w == 3 ? panel * 17 + 16 : (panel >> 3) * 17 + 2 * (panel & 7) + (w - 1);
      u32x4 rv;
      asm volatile("" : "=v"(rv));
#define RLD() rv = ld128(rs, (lane >> 2) * 2176 + (lane & 3) * 16, kpo + hp * 16 * 2176 + rrow * 64)
#define RPUBLISH() do { f32x4 t = __builtin_bit_cast(f32x4, rv); \
        _Pragma("unroll") for (int j = 0; j < 4; ++j) { t[j] += __shfl_xor(t[j], 4); t[j] += __shfl_xor(t[j], 8); t[j] += __shfl_xor(t[j], 16); t[j] = xhalf_sum(t[j]); } \
        if (lane < 4) st64(rs, lane * 8, kfo + hp * 1088 + rrow * 32, u32x2{min(cvtpk(t[0], t[1]), POISON - 1u), min(cvtpk(t[2], t[3]), POISON - 1u)}); } while (0)
#pragma unroll
      for (int k = W2LO; k < 16; ++k) asm volatile("" : "=v"(wp[k]));
#pragma unroll
      for (int i = 0; i < 16; ++i) {
        SBAR();
        if (i < 15) WQLD(i + 1);
        aq = MFMA(wf[i & 1][0], xf[i], aq);
        SBAR();
        if (i == QPUB) {
          DRAIN();
          asm volatile("s_barrier" ::: "memory");
          if (w == 0) arrive(ws, CTR_A + L * 128 + (idx & 15), 16, 8, lane);
        }
        if (i == RLDAT && red_on) RLD();
        if (i >= QPUB + 1 && i < QPUB + 9 && (kh == 1 || i < QPUB + 1 + W2LO / 2)) {
#pragma unroll
          for (int k = 2 * (i - QPUB - 1); k < 2 * (i - QPUB); ++k) wp[k] = ldw(rs, vrot + (k & 3) * 1024, OFF_W + ((T_WOA + nt) * 32 + kh * 16 + (k & ~3)) * 1024);
        }
      }
      SBAR();
      put_hi(smem + 136 * KB + w * 2048 + vrot, aq);
      if (red_on) { VERIFY(umax4(rv, 0u), RLD()); RPUBLISH(); }
      f32x4 cs[2];
#pragma unroll
      for (int g = 0; g < 2; ++g) cs[g] = lds2(csl + kh * 64 + g * 32 + h * 16, 512);
      BAR();
      add_lo(smem + 136 * KB + (w ^ 4) * 2048 + vrot, aq);
      const float rsl = st.rs * LOG2E;
#pragma unroll
      for (int j = 0; j < 8; ++j) aq[j] = phi(aq[j] - st.mu * cs[j >> 2][j & 3], st.rs, rsl);
      st128(rs, vrot, pqo + (t1 * 32 + 2 * hp + kh) * 1024, clean(pack8(aq, 0)));
      if (w == 0) poll(ws, CTR_A + L * 128 + xcd * 16, 16, 16, lane);
      BAR();
    }
    u32x4 wkv[8];
    {
      IDS();
      u32x4 pq[4], kv[4];
      const int row = r < 16 ? r : 16;
#pragma unroll
      for (int i = 0; i < 4; ++i) {
        kv[i] = ld128(rs, row * 32 + h * 16 + i * 544, kfo + w * 2176);
        pq[i] = ld128(rs, voff + i * 1024, pqo + (tt * 32 + w * 4) * 1024);
      }
      SBAR();
#pragma unroll
      for (int i = 0; i < 4; ++i) {
        VERIFY_FAST(umax4(pq[i], umax4(kv[i], 0u)), { kv[i] = ld128(rs, row * 32 + h * 16 + i * 544, kfo + w * 2176);
                                                      pq[i] = ld128(rs, voff + i * 1024, pqo + (tt * 32 + w * 4) * 1024); });
        f32x16 d = MFMA(kv[i], pq[i], zero16());
        const float inv = __builtin_amdgcn_rcpf(d[8] + 1e-6f);
#pragma unroll
        for (int j = 0; j < 8; ++j) d[j] *= inv;
        *(LAS u32x4*)(smem + L_ACT + (w * 4 + i) * 1024 + voff) = pack8(d, 0);
      }
      if (kh == 0) {
#pragma unroll
        for (int k = W2LO; k < 16; ++k) wp[k] = ldw(rs, vrot + (k & 3) * 1024, OFF_W + ((T_WOA + nt) * 32 + (k & ~3)) * 1024);
      }
      const int wqo = OFF_W + ((T_WQ + nt) * 32 + kh * 16) * 1024;
#pragma unroll
      for (int i = 0; i < 8; ++i) asm volatile("" : "=v"(wkv[i]));
      if (cb == 0 && kh == 1) {
#pragma unroll
        for (int i = 0; i < 8; ++i) wkv[i] = ldw(rs, (voff ^ (w4 == 1 ? 256 : 0)) + (i & 3) * 1024, OFF_W + (T_WKV * 32 + w4 * 8 + (i & 4)) * 1024);
      }
      if (w == 1) glds2(ws, OFF_CS3 + cb * 2048 + voff, lds0 + 56 * KB);
      BAR();
      f32x16 acc = gemm_half(smem + L_ACT + kh * 16384 + voff, [&](int i) { return wp[i]; },
                             [&](int i) { if (i < NEARLY) wp[i] = ldw(rs, vrot + (i & 3) * 1024, wqo + (i & ~3) * 1024); });
      put_hi(smem + L_WL + w * 2048 + voff, acc);
      BAR();
      add_lo(smem + L_WL + (w ^ 4) * 2048 + voff, acc);
      resid_epilogue(acc, xr, rs, xb, smem + 48 * KB + w * 256, out, false, tt, 2 * nt + kh, lane);
      BAR();
      if (w == 0) publish_stats<8>(smem + 48 * KB, rs, sb_ + tt * 1024 + cb * 8, lane);
    }
    u32x4 q0;
    {
      IDS();
      u32x4 xa4[4];
#define X3LD() do { _Pragma("unroll") for (int k = 0; k < 4; ++k) xa4[k] = ld128(rs, voff + k * 1024, xb + (tt * 32 + 4 * w) * 1024); } while (0)
      DRAIN();
      X3LD();
      VERIFY(umax4(xa4[0], umax4(xa4[1], umax4(xa4[2], umax4(xa4[3], 0u)))), X3LD());
      u32x4 sg = ld128(rs, r * 32 + h * 16, sb_ + tt * 1024);
#pragma unroll
      for (int k = 0; k < 4; ++k) *(LAS u32x4*)(smem + L_ACT + (4 * w + k) * 1024 + voff) = xa4[k];
      BAR();
#pragma unroll
      for (int i = NEARLY; i < 16; ++i) wp[i] = ldw(rs, vrot + (i & 3) * 1024, OFF_W + ((T_WQ + nt) * 32 + kh * 16 + (i & ~3)) * 1024);
      SBAR();
      const LAS char* csl = smem + 56 * KB + h * 16;
      RowStat st;
#define S3GET() do { VERIFY_FAST(umax4(sg, 0u), sg = ld128(rs, r * 32 + h * 16, sb_ + tt * 1024)); st = row_stats(sg); } while (0)
      if (cb == 0 && kh == 1) {
        f32x16 kacc = zero16();
        __builtin_amdgcn_s_setprio(2);
        {
          u32x4 a[8];
#pragma unroll
          for (int i = 0; i < 8; ++i) a[i] = *(const LAS u32x4*)(smem + L_ACT + (w4 * 8 + i) * 1024 + voff);
          SBAR();
#pragma unroll
          for (int i = 0; i < 8; ++i) kacc = MFMA(wkv[i], a[i], kacc);
          S3GET();
          LAS char* kp = smem + 60 * KB + w4 * 4096 + voff;
          if (w4 != 0) { *(LAS f32x4*)(kp + (w4 == 1 ? 2048 : 0)) = f32x4{kacc[0], kacc[1], kacc[2], kacc[3]}; *(LAS f32x4*)(kp + (w4 == 1 ? 3072 : 1024)) = f32x4{kacc[4], kacc[5], kacc[6], kacc[7]}; }
          put_hi(kp + (w4 == 1 ? 0 : 2048), kacc);
        }
        BAR();
        if (w4 < 2) {
          const LAS char* kp = smem + 60 * KB + w4 * 2048 + voff;
          add_lo(kp + (w4 ^ 1) * 4096, kacc); add_lo(kp + 8192, kacc); add_lo(kp + 12288, kacc);
#pragma unroll
          for (int g = 0; g < 2; ++g) {
            const f32x4 c = lds2(csl + 512 + w4 * 64 + g * 32, 1024);
#pragma unroll
            for (int j = 0; j < 4; ++j) kacc[4 * g + j] = st.rs * (kacc[4 * g + j] - st.mu * c[j]);
          }
          u32x4 v = pack8(kacc, 0);
          if (w == 5) {
            const int t16 = r & 15, hh = (t16 >> 2) & 1, jj = ((t16 >> 3) << 2) | (t16 & 3);
            LAS uint16_t* vb = (LAS uint16_t*)(smem + 76 * KB + (r >> 4) * 512 + hh * 256) + jj;
#pragma unroll
            for (int j = 0; j < 8; ++j) vb[(8 * (j >> 2) + 4 * h + (j & 3)) * 8] = (uint16_t)((j & 1) ? (v[j >> 1] >> 16) : (v[j >> 1] & 0xffffu));
            v = *(const LAS u32x4*)(smem + 76 * KB + voff);
          }
          st128(rs, voff, kvo + tt * 2048 + w4 * 1024, clean(v));
        }
        __builtin_amdgcn_s_setprio(0);
      }
      f32x16 acc = gemm_half(smem + L_ACT + kh * 16384 + voff, [&](int i) { return wp[i]; },
                             [&](int i) {
        if (i == 6 && cb == 0 && kh == 0) asm volatile("s_barrier" ::: "memory");
        if (i == 10 && !(cb == 0 && kh == 1)) S3GET();
      });
      put_hi(smem + 80 * KB + w * 2048 + voff, acc);
      f32x4 c[2];
#pragma unroll
      for (int g = 0; g < 2; ++g) c[g] = lds2(csl + w4 * 128 + kh * 64 + g * 32, 1024);
      BAR();
      add_lo(smem + 80 * KB + (w ^ 4) * 2048 + voff, acc);
      const float sc = st.rs * (0.25f * LOG2E);
#pragma unroll
      for (int j = 0; j < 8; ++j) acc[j] = sc * (acc[j] - st.mu * c[j >> 2][j & 3]);
      q0 = pack8(acc, 0);
      *(LAS u32x4*)(smem + L_COMB + w * 3072 + voff) = q0;
      if (tid >= 64 && tid < 74) {
        const int s5 = (tid - 64) >> 1, o5 = s5 == 0 ? 0 : (s5 == 1 ? 512 : (s5 == 2 ? 2048 : (s5 == 3 ? 2560 : 4096)));
        const unsigned c = (tid & 1) ? 0u : 0x3f803f80u;
        *(LAS u32x4*)(smem + L_CONST + o5 + (tid & 1) * 16) = u32x4{c, c, c, c};
      }
      PIN(q0);
      BAR();
    }
    u32x16 wo0, wo1, wo2, wo3, wn0, wn1, wn2;
    {
      IDS();
      f32x16 oa = zero16(), ob = zero16(), sa, sb;
      u32x4 kf, v0, v1, p0, p1 = {0u, 0u, 0u, 0u};
      unsigned ka = lds0 + kh * 65536 + lane * 16;
      unsigned va = r < 16 ? lds0 + kh * 65536 + 1024 + (h * 16 + r) * 16 : lds0 + L_CONST + ((r == 16 || r == 20) ? 0 : 16);
      const unsigned vinc = r < 16 ? 4096u : 0u;
      const unsigned o0 = kh * 65536 + w4 * 2048;
      unsigned ga = kvo + o0 + voff, wo = OFF_W + ((T_WOB + nt) * 32 + kh * 16) * 1024 + vrot, n, m0s;
      unsigned wn = OFF_W + (hp * 32 + 4 * w) * 1024 + voff;
      const u32x4 q1 = *(const LAS u32x4*)(smem + L_COMB + (w ^ 4) * 3072 + voff);
      asm volatile(ATTN_ASM
                   : "+{v[0:15]}"(oa), "+{v[16:31]}"(ob), "=&{v[32:47]}"(sa), "=&{v[48:63]}"(sb), "=&{v[64:67]}"(kf), "=&{v[68:71]}"(v0),
                     "=&{v[72:75]}"(v1), "=&{v[76:79]}"(p0), "+{v[80:83]}"(p1), "+{v92}"(ka), "+{v93}"(va), "+{v95}"(ga), "+{v96}"(wo),
                     "=&{v[128:143]}"(wo0), "=&{v[144:159]}"(wo1), "=&{v[160:175]}"(wo2), "=&{v[176:191]}"(wo3),
                     "+{v97}"(wn), "=&{v[192:207]}"(wn0), "=&{v[208:223]}"(wn1), "=&{v[224:239]}"(wn2),
                     [n] "=&s"(n), [m0s] "=&s"(m0s)
                   : "{v[84:87]}"(q0), "{v[88:91]}"(q1), "{v94}"(vinc), "{v98}"(lds0 + o0 + voff), [wsb] "s"(ws), [kh] "s"(kh), [m0a] "s"(__builtin_amdgcn_readfirstlane(lds0 + o0))
                   : "memory", "scc", "vcc", "s90", "s91", "s92", "s93", "s94", "s95", "s96", "s97");
      LAS char* cw = smem + L_COMB + w * 3072 + voff;
      const LAS char* cr = smem + L_COMB + (w ^ 4) * 3072 + voff;
      *(LAS f32x4*)cw = f32x4{ob[0], ob[1], ob[2], ob[3]}; *(LAS f32x4*)(cw + 1024) = f32x4{ob[4], ob[5], ob[6], ob[7]};
      *(LAS float*)(cw + 2048) = ob[8];
      BAR();
      add_lo(cr, oa);
      oa[8] += *(const LAS float*)(cr + 2048);
      float mx = fmaxf(fmaxf(fabsf(oa[0]), fabsf(oa[1])), fabsf(oa[2]));
      mx = fmaxf(fmaxf(mx, fabsf(oa[3])), fabsf(oa[4])); mx = fmaxf(fmaxf(mx, fabsf(oa[5])), fabsf(oa[6]));
      const float sum8 = ((oa[0] + oa[1]) + (oa[2] + oa[3])) + ((oa[4] + oa[5]) + (oa[6] + oa[7]));
      const bool ok = fmaxf(fmaxf(mx, fabsf(oa[7])), oa[8]) < 1.0e36f && oa[8] > 1.0e-27f && sum8 == sum8;
      if (__builtin_expect(!__all(ok), 0)) {
        const LAS char* kb0 = smem + lane * 16;
        const LAS char* vb0 = r < 16 ? smem + 1024 + (h * 16 + r) * 16 : smem + L_CONST + ((r == 16 || r == 20) ? 0 : 16);
        const int vstep = r < 16 ? 2048 : 0;
        float m = -INFINITY;
        oa = zero16();
#pragma unroll 1
        for (int t = 0; t < 64; ++t) {
          f32x16 s = MFMA(*(const LAS u32x4*)(kb0 + t * 2048), q0, zero16());
          const float mn = fmaxf(m, xhalf_max(max16(s)));
          const float al = __builtin_amdgcn_exp2f(m - mn);
          m = mn;
#pragma unroll
          for (int i = 0; i < 16; ++i) { oa[i] *= al; s[i] = __builtin_amdgcn_exp2f(s[i] - mn); }
          oa = MFMA(*(const LAS u32x4*)(vb0 + t * vstep), pack8(s, 0), oa);
          oa = MFMA(*(const LAS u32x4*)(vb0 + t * vstep + 512), pack8(s, 1), oa);
        }
      }
      const float inv = __builtin_amdgcn_rcpf(oa[8]);
#pragma unroll
      for (int i = 0; i < 8; ++i) oa[i] *= inv;
      st128(rs, voff, ofo + (tt * 32 + 2 * nt + kh) * 1024, clean(pack8(oa, 0)));
    }
    {
      IDS();
      u32x4 o4[4];
#define O5LD() do { _Pragma("unroll") for (int k = 0; k < 4; ++k) o4[k] = ld128(rs, voff + k * 1024, ofo + (tt * 32 + 4 * w) * 1024); } while (0)
      DRAIN();
      BAR();
      O5LD();
      SBAR();
      if (L == 0) {
#pragma unroll
        for (int i = 0; i < 4; ++i) {
          LAS char* d = smem + L_WL + (4 * w + i) * 1024 + voff;
          *(LAS u32x4*)d = u32x4{wn0[4 * i], wn0[4 * i + 1], wn0[4 * i + 2], wn0[4 * i + 3]};
          *(LAS u32x4*)(d + 32768) = u32x4{wn1[4 * i], wn1[4 * i + 1], wn1[4 * i + 2], wn1[4 * i + 3]};
          *(LAS u32x4*)(d + 65536) = u32x4{wn2[4 * i], wn2[4 * i + 1], wn2[4 * i + 2], wn2[4 * i + 3]};
        }
      }
      SBAR();
      VERIFY(umax4(o4[0], umax4(o4[1], umax4(o4[2], umax4(o4[3], 0u)))), O5LD());
#pragma unroll
      for (int k = 0; k < 4; ++k) *(LAS u32x4*)(smem + L_ACT + (4 * w + k) * 1024 + voff) = o4[k];
      BAR();
      f32x16 acc = gemm_half(smem + L_ACT + kh * 16384 + voff, [&](int i) {
        const u32x16& v = i < 4 ? wo0 : (i < 8 ? wo1 : (i < 12 ? wo2 : wo3));
        return u32x4{v[4 * (i & 3)], v[4 * (i & 3) + 1], v[4 * (i & 3) + 2], v[4 * (i & 3) + 3]};
      }, [](int) {});
      put_hi(smem + 136 * KB + w * 2048 + voff, acc);
      BAR();
      add_lo(smem + 136 * KB + (w ^ 4) * 2048 + voff, acc);
      resid_epilogue(acc, xr, rs, xc, smem + 152 * KB + w * 256, out, L == 1, tt, 2 * nt + kh, lane);
      if (L == 0) {
        BAR();
        if (w == 0) publish_stats<8>(smem + 152 * KB, rs, sc_ + tt * 1024 + cb * 8, lane);
        DRAIN();
        __builtin_amdgcn_s_sleep(10);
      }
    }
  }
}

extern "C" void kernel_launch(void* const* d_in, const int* in_sizes, int n_in, void* d_out, int out_size, void* d_ws,
                              size_t ws_size, hipStream_t stream) {
  char* ws = (char*)d_ws;
  (void)hipFuncSetAttribute((const void*)k_mega, hipFuncAttributeMaxDynamicSharedMemorySize, 160 * KB);
  k_prep<<<PREP_BLOCKS, 256, 0, stream>>>((const uint16_t*)d_in[0], (const uint16_t*)d_in[1], (const uint16_t*)d_in[2], (const uint16_t*)d_in[4],
                                          (const uint16_t*)d_in[5], (const uint16_t*)d_in[6], (const uint16_t*)d_in[7], ws);
  k_mega<<<256, 512, 160 * KB, stream>>>(ws, (uint16_t*)d_out);
}
```
